```python
import torch

def _softmax(x, axis=-1):
    return torch.softmax(x, dim=axis)
def _gelu(x):
    return torch.nn.functional.gelu(x, approximate="tanh")
def _silu(x):
    return torch.nn.functional.silu(x)
def _sigmoid(x):
    return torch.sigmoid(x)
def _layernorm(x, axis=-1, eps=1e-5):
    m = x.mean(dim=axis, keepdim=True)
    v = x.var(dim=axis, keepdim=True, unbiased=False)
    return (x - m) / torch.sqrt(v + eps)
def _scatter_add(tgt, idx, src, axis=0):
    return tgt.clone().index_add_(axis, idx, src)
def _segment_sum(src, seg, n):
    out = torch.zeros((n,) + src.shape[1:], dtype=src.dtype)
    return out.index_add_(0, seg, src)
def _pad(x, pw):
    flat = tuple(v for pair in reversed(pw) for v in pair)
    return torch.nn.functional.pad(x, flat)

kernel_name = 'concat_heads__compressed_kv_cla__ssm_conv_h3__prompt'

def setup_inputs():

    _w = (lambda a: (lambda t: t.to(torch.bfloat16) if t.is_floating_point() else t)(torch.as_tensor(a)))
    def _A_setup():
        B, H, Sq, Sk, D, M = 1, 16, 2048, 2048, 256, 448
        Dc = 4 * D
        return {
            "x": _w((lambda *s: torch.randn(*s, dtype=torch.bfloat16) * 3.2)(B, Sq, M)),
            "c_kv": _w((lambda *s: torch.randn(*s, dtype=torch.bfloat16) * 0.05)(B, Sk, Dc)),
            "w_q": _w((lambda *s: torch.randn(*s, dtype=torch.bfloat16) * 3.2)(M, H, D) * (M ** -0.5)),
            "w_uk": _w((lambda *s: torch.randn(*s, dtype=torch.bfloat16) * 3.2)(Dc, H, D) * (Dc ** -0.5)),
            "w_uv": _w((lambda *s: torch.randn(*s, dtype=torch.bfloat16) * 0.05)(Dc, H, D) * (Dc ** -0.5)),
            "w_o": _w((lambda *s: torch.randn(*s, dtype=torch.bfloat16) * 0.05)(H, D, M) * ((H * D) ** -0.5)),
        }
    _A_in = _A_setup()

    def _B_setup():
        B, S, D = 1, 2048, 448
        ks = 4
        return {
            "q": _w((lambda *s: torch.randn(*s, dtype=torch.bfloat16) * 0.05)(B, S, D)),
            "k": _w((lambda *s: torch.randn(*s, dtype=torch.bfloat16) * 0.1)(B, S, D)),
            "v": _w((lambda *s: torch.randn(*s, dtype=torch.bfloat16) * 0.1)(B, S, D)),
            "shift_k": _w((lambda *s: torch.randn(*s, dtype=torch.bfloat16) * 0.1)(D, ks) * 0.5),
            "ssm_k": _w((lambda *s: torch.randn(*s, dtype=torch.bfloat16) * 0.1)(D, S) * (1.0 / S ** 0.5)),
            "ssm_D": _w((lambda *s: torch.randn(*s, dtype=torch.bfloat16) * 0.1)(D)),
        }
    _B_in = _B_setup()

    return {
        "A_x": _A_in["x"],
        "A_c_kv": _A_in["c_kv"],
        "A_w_q": _A_in["w_q"],
        "A_w_uk": _A_in["w_uk"],
        "A_w_uv": _A_in["w_uv"],
        "A_w_o": _A_in["w_o"],
        "B_q": _B_in["q"],
        "B_k": _B_in["k"],
        "B_v": _B_in["v"],
        "B_shift_k": _B_in["shift_k"],
        "B_ssm_k": _B_in["ssm_k"],
        "B_ssm_D": _B_in["ssm_D"],
    }

def reference(A_x, A_c_kv, A_w_q, A_w_uk, A_w_uv, A_w_o, B_q, B_k, B_v, B_shift_k, B_ssm_k, B_ssm_D):
    def _A_ref(x, c_kv, w_q, w_uk, w_uv, w_o):
        B, Sq, M = x.shape[0], x.shape[1], x.shape[2]
        Sk = c_kv.shape[1]
        H, D = w_q.shape[1], w_q.shape[2]
        q = torch.einsum("bqm,mhd->bhqd", x, w_q)
        k = torch.einsum("bkc,chd->bhkd", c_kv, w_uk)
        v = torch.einsum("bkc,chd->bhkd", c_kv, w_uv)
        scores = torch.einsum("bhqd,bhkd->bhqk", q, k) * (D ** -0.5)
        off = Sk - Sq
        qi = torch.arange(Sq)[:, None]
        kj = torch.arange(Sk)[None, :]
        cmask = kj <= (qi + off)
        scores = torch.where(cmask[None, None], scores, -1e9)
        p = _softmax(scores, axis=-1)
        ctx = torch.einsum("bhqk,bhkd->bhqd", p, v)
        return torch.einsum("bhqd,hdm->bqm", ctx, w_o)

    def _B_ref(q, k, v, shift_k, ssm_k, ssm_D):
        B, S, D = q.shape
        ks = shift_k.shape[-1]
        k_pad = _pad(k, ((0, 0), (ks - 1, 0), (0, 0)))
        k_shift = torch.zeros_like(k)
        for t in range(ks):
            k_shift = k_shift + k_pad[:, t:t + S, :] * shift_k[:, ks - 1 - t][None, None, :]
        kv = k_shift * v
        L = 2 * S
        kv_pad = _pad(kv, ((0, 0), (0, S), (0, 0)))
        kf_pad = _pad(ssm_k, ((0, 0), (0, S)))
        kv_f = (lambda x, axes=(-2,-1): torch.fft.rfft2(x.float(), dim=axes))(kv_pad[:, None, :, :], axes=(1, 2))
        kf_f = (lambda x, axes=(-2,-1): torch.fft.rfft2(x.float(), dim=axes))(kf_pad[:, None, :], axes=(1, 2))
        kf_f = (lambda x, *d: x.permute(*d))(kf_f, 1, 2, 0)
        y_f = kv_f * kf_f[None, :, :, :]
        y = (lambda x, s, axes=(-2,-1): torch.fft.irfft2(x, s=s, dim=axes).to(torch.bfloat16))(y_f, (1, L), axes=(1, 2))[:, 0, :S, :]
        y = y + kv * ssm_D[None, None, :]
        return q * y

    a = _A_ref(A_x, A_c_kv, A_w_q, A_w_uk, A_w_uv, A_w_o)
    b = _B_ref(B_q, B_k, B_v, B_shift_k, B_ssm_k, B_ssm_D)
    return torch.concatenate([a, b], axis=-1)
```

```cpp
#include <hip/hip_runtime.h>
typedef unsigned short u16;
typedef unsigned u32x4 __attribute__((ext_vector_type(4)));
typedef unsigned u32x2 __attribute__((ext_vector_type(2)));
typedef unsigned u32x8 __attribute__((ext_vector_type(8)));
typedef unsigned u32x16 __attribute__((ext_vector_type(16)));
typedef float f32x4 __attribute__((ext_vector_type(4)));
typedef __bf16 bf16x2_t __attribute__((ext_vector_type(2)));
typedef float f32x2 __attribute__((ext_vector_type(2)));
typedef __amdgpu_buffer_rsrc_t rsrc_t;
#define DI __device__ __forceinline__
constexpr unsigned WS_KV = 0, WS_Q = 36u << 20, WS_P = 52u << 20, WS_PO = 76u << 20, WS_U = 104u << 20, WS_Z = 106u << 20, WS_FLAG = 108u << 20, WS_TOK = WS_FLAG + 65536, WS_STAMP = 109u << 20, WS_END = 110u << 20;
constexpr unsigned HSTR = 36 * 65536;
constexpr unsigned F_KV = WS_FLAG, F_Q = WS_FLAG + 4096, F_C = WS_FLAG + 131072, F_U = WS_FLAG + 24576, F_Z = WS_FLAG + 28672, OOB = 0x7fffff00u;
constexpr unsigned NSLACK = 2, BPAR = 128;
constexpr unsigned UP = 2112, NA = 18;
enum { P_PX_LO = 0, P_PX_HI = 1, P_PWQ_LO = 2, P_PWQ_HI = 3, P_WQREC = 4, P_TAB0 = 5, P_TAB1 = 6, P_TAB2 = 7, P_TAB3 = 8, P_OFFQ_ST = 9, P_OFFUS = 10, P_FKV_OWN = 11, P_FQ_OWN = 12, P_OFFKV = 13, P_SWF3 = 14, P_NNORM = 15, P_NMASK = 16, P_ACT = 17, P_NN2 = 18, P_SWF = 19, P_SWT2 = 20, P_OFFQ = 21, P_OFFC = 22, P_OFFP = 23, P_FKV = 24, P_FQ = 25, P_FP = 26, P_FC_OWN = 27, P_PH_LO = 28, P_PH_HI = 29, P_PH_REC = 30, P_OFFU = 31, P_OFFZ = 32, P_DV0 = 33, P_FU = 34, P_FZ_OWN = 35, P_FU_OWN = 36, P_T0_LO = 37, P_T0_HI = 38, P_T0_REC = 39, P_PWO_LO = 40, P_PWO_HI = 41, P_WOREC = 42, P_OFFWF = 43, P_CT = 44, P_RUN2 = 45 };
DI unsigned lane_id() { unsigned l; asm volatile("v_mbcnt_lo_u32_b32 %0, -1, 0\n\tv_mbcnt_hi_u32_b32 %0, -1, %0" : "=v"(l)); return l; }
DI float bf2f(unsigned h) { return __uint_as_float(h << 16); }
DI float bfhi(unsigned w) { return __uint_as_float(w & 0xffff0000u); }
DI unsigned pk2(float lo, float hi) { f32x2 v = {lo, hi}; return __builtin_bit_cast(unsigned, __builtin_convertvector(v, bf16x2_t)); }
DI float rbf(float x) { return __uint_as_float(pk2(x, x) << 16); }
DI rsrc_t mk_rsrc(const void* p, unsigned n) { return __builtin_amdgcn_make_buffer_rsrc((void*)p, (short)0, n, 0x00020000); }
#define LDS_BAR() asm volatile("s_waitcnt lgkmcnt(0)\n\ts_barrier" ::: "memory")

DI void b_stage3(unsigned tb, unsigned tok, rsrc_t rws, const u16* __restrict__ bq, rsrc_t rout, char* lds, unsigned wid, unsigned l) {
  const unsigned t0 = tb * 64, tid = wid * 64 + l;
  const unsigned trow = t0 + (wid & 1) * 32 + (l & 15) * 2 + (wid >> 1), cl = (l >> 4) * 8;
  u32x4 qq[14];
#pragma unroll
  for (int it = 0; it < 14; ++it) qq[it] = *(const u32x4*)(bq + (size_t)trow * 448 + it * 32 + cl);
  _Pragma("unroll 1") for (unsigned spin = 0; spin < 100000; ++spin) {
    bool ok = true;
#pragma unroll
    for (int i = 0; i < 4; ++i) { u32x4 f = __builtin_amdgcn_raw_buffer_load_b128(rws, l * 16 + i * 1024, F_Z, 16);
      ok = ok && (l * 16 + i * 1024 >= 3584 || (f.x == tok && f.y == tok && f.z == tok && f.w == tok)); }
    if (__all(ok)) break;
    __builtin_amdgcn_s_sleep(8);
  }
  asm volatile("" ::: "memory");
  u32x4 zz[14];
#pragma unroll
  for (int it = 0; it < 14; ++it) zz[it] = __builtin_amdgcn_raw_buffer_load_b128(rws, (it * 32 + (tid >> 3)) * 4096 + ((tid >> 2) & 1) * 2048 + t0 + (tid & 3) * 16, WS_Z, 16);
#pragma unroll
  for (int it = 0; it < 14; ++it) *(u32x4*)(lds + (it * 32 + (tid >> 3)) * 144 + (tid & 7) * 16) = zz[it];
  LDS_BAR();
  const unsigned ra = (unsigned)(size_t)lds + (cl + ((l >> 2) & 3)) * 144 + wid * 32 + (l & 3) * 8;
  u32x2 za[14], zb[14];
#pragma unroll
  for (int h = 0; h < 2; ++h) {
#pragma unroll
    for (int it = 7 * h; it < 7 * h + 7; ++it)
      asm volatile("ds_read_b64_tr_b16 %0, %2 offset:%3\n\tds_read_b64_tr_b16 %1, %2 offset:%4" : "=&v"(za[it]), "=&v"(zb[it]) : "v"(ra), "i"(it * 32 * 144), "i"(it * 32 * 144 + 576) : "memory");
    asm volatile("s_waitcnt lgkmcnt(0)" : "+v"(za[7 * h]), "+v"(za[7 * h + 1]), "+v"(za[7 * h + 2]), "+v"(za[7 * h + 3]), "+v"(za[7 * h + 4]), "+v"(za[7 * h + 5]), "+v"(za[7 * h + 6]),
                 "+v"(zb[7 * h]), "+v"(zb[7 * h + 1]), "+v"(zb[7 * h + 2]), "+v"(zb[7 * h + 3]), "+v"(zb[7 * h + 4]), "+v"(zb[7 * h + 5]), "+v"(zb[7 * h + 6]) :: "memory");
#pragma unroll
    for (int it = 7 * h; it < 7 * h + 7; ++it) {
      const unsigned z[4] = {za[it].x, za[it].y, zb[it].x, zb[it].y};
      u32x4 o;
#pragma unroll
      for (int e = 0; e < 4; ++e) o[e] = pk2(bf2f(qq[it][e] & 0xffff) * bf2f(z[e] & 0xffff), bfhi(qq[it][e]) * bfhi(z[e]));
      __builtin_amdgcn_raw_buffer_store_b128(o, rout, (trow * 896 + 448 + it * 32 + cl) * 2, 0, 16);
    }
  }
}

__global__ __launch_bounds__(256, 1) void mega(const u16* __restrict__ x, const u16* __restrict__ c, const u16* __restrict__ wq, const u16* __restrict__ wuk, const u16* __restrict__ wuv,
                                               const u16* __restrict__ wo, const u16* __restrict__ bq, const u16* __restrict__ bk, const u16* __restrict__ bv, const u16* __restrict__ shk,
                                               const u16* __restrict__ ssmk, const u16* __restrict__ ssmD, char* __restrict__ ws, u16* __restrict__ out) {
  __shared__ __attribute__((aligned(16))) char lds[163840];
  const unsigned tid = threadIdx.x, l = tid & 63, wid = __builtin_amdgcn_readfirstlane(tid >> 6);
  const unsigned bid = blockIdx.x;

  asm volatile("s_getpc_b64 s[60:61]\nL_code_begin:\n\ts_and_b32 s61, s61, 0xffff\n\ts_mov_b32 s62, L_code_end-L_code_begin\n\ts_mov_b32 s63, 0x20000\n\t"
               "buffer_load_dword v254, %0, s[60:63], 0 offen" :: "v"(((bid >> 3) * 4 + (tid >> 6)) * 512 + (tid & 3) * 128) : "s60", "s61", "s62", "s63", "v254", "scc", "memory");
  const unsigned xcd = bid & 7, j = bid >> 3;
  const unsigned hp = j >> 4, head = xcd * 2 + hp, var = (j >> 3) & 1, kb = j & 7;
  const unsigned typeB = var, pi = kb, hb = 15 - pi;
  const rsrc_t rws = mk_rsrc(ws, WS_END);
  unsigned ldsb = (unsigned)(size_t)lds;
  unsigned tok;
  {
    const bool s1 = bid < 224;
    const unsigned tb = bid / 7, cb = bid % 7, t0 = tb * 64, c0 = cb * 64, cp = tid & 31, tg = tid >> 5;
    const rsrc_t rk = mk_rsrc(bk, s1 ? 2048 * 896 : 0), rv = mk_rsrc(bv, s1 ? 2048 * 896 : 0), rs = mk_rsrc(shk, s1 ? 448 * 8 : 0), rd = mk_rsrc(ssmD, 896);
    const unsigned vo = (t0 + 8 * tg) * 896 + (c0 + 2 * cp) * 2;
    const bool top = t0 + tg == 0;
    const unsigned dch = s1 ? bid * 2 + (wid >> 1) : 0, rho = wid & 1;
    u32x16 pre0; u32x8 pre1; u32x2 pre2;
    const u16* pc = c + (size_t)kb * 256 * 1024;
    const u16* pw = (var ? wuv : wuk) + head * 256;
    unsigned wrec = 1024u * 8192u - head * 512u;
    unsigned bpar = BPAR * ((kb ^ var) & 1);
    const unsigned jw = (j & 15) * 4 + wid, wc = jw >> 3, wds = jw & 7;
    const unsigned long long pwo = (unsigned long long)(wo + (size_t)(head * 256 + wds * 32) * 448 + (jw < 56 ? wc : 0) * 64);
    unsigned offA[4], offA2[4], offW[4], offX[4];
#pragma unroll
    for (int p = 0; p < 4; ++p) {
      unsigned row = 8 * (4 * wid + p) + (l >> 3), swz = 16 * ((l & 7) ^ (4 * (p & 1) + (l >> 4)));
      offA[p] = row * 2048 + swz - 1024 * p; offA2[p] = offA[p] + 262144; offX[p] = row * 896 + swz - 1024 * p;
      unsigned Q = l >> 2, q = Q & 3, b = Q >> 2, kp = 4 * (wid >> 1) + p;
      offW[p] = (4 * kp + q) * 8192 + 256 * (wid & 1) + 64 * b + 16 * ((l & 3) ^ (2 * ((p >> 1) & 1))) - 1024 * p;
    }
    asm volatile(
"s_mov_b32 s44, 4480\n"
"buffer_load_dword v184, v192, s[28:31], 0 offen offset:0 sc1\n"
"buffer_load_ushort v185, v193, s[68:71], 0 offen offset:0\n"
"buffer_load_dwordx4 v[180:183], v194, s[60:63], 0 offen offset:0\n"
"buffer_load_dword v160, v188, s[52:55], 0 offen offset:0\n"
"buffer_load_dword v161, v189, s[52:55], 0 offen offset:0\n"
"buffer_load_dword v162, v190, s[52:55], 0 offen offset:0\n"
"buffer_load_dword v163, v191, s[52:55], 0 offen offset:0\n"
"buffer_load_dword v164, v191, s[52:55], 0 offen offset:896\n"
"buffer_load_dword v165, v191, s[52:55], 0 offen offset:1792\n"
"buffer_load_dword v166, v191, s[52:55], 0 offen offset:2688\n"
"buffer_load_dword v167, v191, s[52:55], 0 offen offset:3584\n"
"buffer_load_dword v168, v191, s[52:55], s44 offen offset:0\n"
"buffer_load_dword v169, v191, s[52:55], s44 offen offset:896\n"
"buffer_load_dword v170, v191, s[52:55], s44 offen offset:1792\n"
"buffer_load_dword v171, v191, s[56:59], 0 offen offset:0\n"
"buffer_load_dword v172, v191, s[56:59], 0 offen offset:896\n"
"buffer_load_dword v173, v191, s[56:59], 0 offen offset:1792\n"
"buffer_load_dword v174, v191, s[56:59], 0 offen offset:2688\n"
"buffer_load_dword v175, v191, s[56:59], 0 offen offset:3584\n"
"buffer_load_dword v176, v191, s[56:59], s44 offen offset:0\n"
"buffer_load_dword v177, v191, s[56:59], s44 offen offset:896\n"
"buffer_load_dword v178, v191, s[56:59], s44 offen offset:1792\n"
"s_and_b32 s21, s21, 0xffff\n"
"s_mov_b32 s22, 524288\n"
"s_mov_b32 s23, 0x20000\n"
"s_and_b32 s25, s25, 0xffff\n"
"s_mov_b32 s27, 0x20000\n"
"s_lshl_b32 s44, s46, 12\n"
"s_add_u32 s41, s44, s48\n"
"s_add_u32 s41, s41, 65536\n"
"s_and_b32 s44, s46, 1\n"
"s_lshl_b32 s44, s44, 13\n"
"s_lshr_b32 s45, s46, 1\n"
"s_lshl_b32 s45, s45, 12\n"
"s_add_u32 s42, s44, s45\n"
"s_add_u32 s42, s42, s48\n"
"s_lshl_b32 s67, s37, 12\n"
"s_mov_b32 s64, 0\n"
"s_mov_b32 s65, 0\n"
"s_xor_b32 s36, s64, s37\n"
"s_xor_b32 s38, s65, s67\n"
"s_mov_b32 s39, 0\n"
"s_add_u32 m0, s42, 0\n"
"s_nop 0\n"
"buffer_load_dwordx4 v240, s[24:27], s38 offen offset:0 lds\n"
"buffer_load_dwordx4 v241, s[24:27], s38 offen offset:1024 lds\n"
"buffer_load_dwordx4 v242, s[24:27], s38 offen offset:2048 lds\n"
"buffer_load_dwordx4 v243, s[24:27], s38 offen offset:3072 lds\n"
"s_add_u32 s65, s65, 262144\n"
"s_xor_b32 s38, s65, s67\n"
"s_add_u32 m0, s41, s39\n"
"s_nop 0\n"
"buffer_load_dwordx4 v232, s[20:23], s36 offen offset:0 lds\n"
"buffer_load_dwordx4 v233, s[20:23], s36 offen offset:1024 lds\n"
"buffer_load_dwordx4 v234, s[20:23], s36 offen offset:2048 lds\n"
"buffer_load_dwordx4 v235, s[20:23], s36 offen offset:3072 lds\n"
"s_add_u32 s39, s39, 16384\n"
"s_cmp_eq_u32 s39, 98304\n"
"s_cselect_b32 s39, 0, s39\n"
"s_add_u32 m0, s41, s39\n"
"s_nop 0\n"
"buffer_load_dwordx4 v236, s[20:23], s36 offen offset:0 lds\n"
"buffer_load_dwordx4 v237, s[20:23], s36 offen offset:1024 lds\n"
"buffer_load_dwordx4 v238, s[20:23], s36 offen offset:2048 lds\n"
"buffer_load_dwordx4 v239, s[20:23], s36 offen offset:3072 lds\n"
"s_add_u32 s39, s39, 16384\n"
"s_cmp_eq_u32 s39, 98304\n"
"s_cselect_b32 s39, 0, s39\n"
"s_add_u32 s64, s64, 128\n"
"s_xor_b32 s36, s64, s37\n"
"s_add_u32 m0, s42, 16384\n"
"s_nop 0\n"
"buffer_load_dwordx4 v240, s[24:27], s38 offen offset:0 lds\n"
"buffer_load_dwordx4 v241, s[24:27], s38 offen offset:1024 lds\n"
"buffer_load_dwordx4 v242, s[24:27], s38 offen offset:2048 lds\n"
"buffer_load_dwordx4 v243, s[24:27], s38 offen offset:3072 lds\n"
"s_add_u32 s65, s65, 262144\n"
"s_xor_b32 s38, s65, s67\n"
      : "+{s[20:21]}"(pc), "+{s[24:25]}"(pw), "={v[160:175]}"(pre0), "={v[176:183]}"(pre1), "={v[184:185]}"(pre2)
      : "{s26}"(wrec), "{s37}"(bpar), "{s46}"(wid), "{s48}"(ldsb), "{s[52:55]}"(rk), "{s[56:59]}"(rv), "{s[60:63]}"(rs), "{s[68:71]}"(rd), "{s[28:31]}"(rws),
        "{v188}"(top ? OOB : vo - 3 * 896), "{v189}"(top ? OOB : vo - 2 * 896), "{v190}"(top ? OOB : vo - 896), "{v191}"(vo), "{v192}"(WS_TOK), "{v193}"(dch * 2), "{v194}"((c0 + 2 * cp) * 8),
        "{v232}"(offA[0]), "{v233}"(offA[1]), "{v234}"(offA[2]), "{v235}"(offA[3]), "{v236}"(offA2[0]), "{v237}"(offA2[1]), "{v238}"(offA2[2]), "{v239}"(offA2[3]),
        "{v240}"(offW[0]), "{v241}"(offW[1]), "{v242}"(offW[2]), "{v243}"(offW[3])
      : "s22", "s23", "s27", "s36", "s38", "s39", "s41", "s42", "s44", "s45", "s64", "s65", "s67", "vcc", "scc", "memory");
    const bool split = 2 * hb + 2 > NA + NSLACK;
    const unsigned na = NA < 2 * hb ? NA : 2 * hb;
    const unsigned qb = typeB ? pi : hb;
    const unsigned nfirst = typeB ? (split ? 2 * hb - na : 2 * pi) : (split ? na - 1 : 2 * hb), nmask = (!typeB && split) ? 0 : 2, act = split ? 2 - typeB : 0;
    const unsigned nn2 = 2 * pi, swf = (typeB && split) ? (2 * hb + 2) * 65536 : 0xffffffffu, swt2 = (typeB ? 2 * pi + 2 : split ? na : 2 * hb + 2) * 65536, swf3 = (typeB && split) ? na * 65536 : 0;
    const unsigned gw = bid * 4 + wid;
    const unsigned long long pt0 = gw < 128 ? (unsigned long long)x + gw * 14336 : (unsigned long long)wq + (gw < 384 ? gw - 128 : 0) * 14336;
    const unsigned long long px = (unsigned long long)(x + (size_t)qb * 128 * 448), pwq = (unsigned long long)(wq + head * 256);
    const unsigned kvo = WS_KV + head * HSTR + 4 * kb * 65536 + (var ? 32768 : 0);
    const unsigned offq = WS_Q + (head * 8 + pi) * 65536, fq = F_Q + (head * 8 + pi) * 64;
    const unsigned long long ph = (unsigned long long)(ssmk + (size_t)dch * 2048);
    unsigned pv = 0;
#define SETP(n, v) asm volatile("v_writelane_b32 %0, %1, %2" : "+v"(pv) : "s"((unsigned)(v)), "i"(n))
    SETP(P_PX_LO, px); SETP(P_PX_HI, (px >> 32) & 0xffff); SETP(P_PWQ_LO, pwq); SETP(P_PWQ_HI, (pwq >> 32) & 0xffff); SETP(P_WQREC, 448u * 8192u - head * 512u);
    SETP(P_TAB0, kvo); SETP(P_TAB1, kvo + (var ? 4096 : 8192)); SETP(P_TAB2, kvo + 16384); SETP(P_TAB3, kvo + (var ? 20480 : 24576));
    SETP(P_OFFQ_ST, split ? offq + typeB * (8u << 20) : OOB); SETP(P_RUN2, (!typeB && split) ? 0xfffffffeu : 0u);
    SETP(P_FKV_OWN, F_KV + head * 256 + ((var * 8 + kb) * 4 + wid) * 4); SETP(P_FQ_OWN, typeB ? OOB : fq + wid * 4);
    SETP(P_OFFKV, WS_KV + head * HSTR); SETP(P_SWF3, swf3); SETP(P_NNORM, nfirst); SETP(P_NMASK, nmask); SETP(P_ACT, act); SETP(P_NN2, nn2);
    SETP(P_SWF, swf); SETP(P_SWT2, swt2); SETP(P_OFFQ, offq); SETP(P_OFFC, WS_PO + (qb * 8 + wid) * 229376 + head * 1024);
    SETP(P_OFFP, WS_P + ((head * 8 + pi) * 4 + wid) * 32768); SETP(P_FKV, F_KV + head * 256); SETP(P_FQ, fq); SETP(P_FP, fq + 16 + wid * 4);
    SETP(P_FC_OWN, wid == 0 ? F_C + (qb * 16 + head) * 128 : OOB);
    SETP(P_PH_LO, ph); SETP(P_PH_HI, (ph >> 32) & 0xffff); SETP(P_PH_REC, s1 ? 4096 : 0); SETP(P_OFFU, s1 ? WS_U + dch * UP * 2 + 2 * rho : OOB);
    SETP(P_OFFZ, s1 ? WS_Z + dch * 4096 + (1 - rho) * 2048 : OOB);
    SETP(P_FU, F_U + (dch >> 6) * 128); SETP(P_FZ_OWN, s1 ? F_Z + (dch * 2 + rho) * 4 : OOB);
    SETP(P_FU_OWN, (s1 && wid == 0) ? F_U + cb * 128 + tb * 4 : OOB);
    SETP(P_T0_LO, pt0); SETP(P_T0_HI, (pt0 >> 32) & 0xffff); SETP(P_T0_REC, gw < 384 ? 14336 : 0);
    SETP(P_PWO_LO, pwo); SETP(P_PWO_HI, (pwo >> 32) & 0xffff); SETP(P_WOREC, jw < 56 ? 31 * 896 + 128 : 0); SETP(P_OFFUS, s1 ? WS_U : 0x70000000u);
    SETP(P_CT, (j * 4 + wid) * 512);
    SETP(P_OFFWF, jw < 56 ? WS_KV + head * HSTR + 32 * 65536 + wc * 32768 + wds * 1024 : OOB);
    const unsigned chalf = wid >> 1, whalf = wid & 1, r = l & 15, g = l >> 4, q = (l >> 2) & 3, p = l & 3;
    unsigned vA0 = ldsb + 65536 + 16384 * chalf + 128 * r + 16 * ((0 + g) ^ (r >> 1));
    unsigned vA1 = ldsb + 65536 + 16384 * chalf + 128 * r + 16 * ((4 + g) ^ (r >> 1));
    unsigned vWb = ldsb + 8192 * whalf + 2048 * g + 64 * q + 16 * (p >> 1) + 8 * (p & 1);
    unsigned vWe = vWb + 32 * (g & 1), vWo = vWb + 32 * (1 ^ (g & 1));
    unsigned vst = 16 * l + 2 * chalf * 65536 + (var ? 8 * whalf * 1024 : 4 * whalf * 1024);
    unsigned vstq = 16 * l + chalf * 32768 + whalf * 4096;
    unsigned vus = ((c0 + 2 * cp) * UP + (2048 - 64 - t0) + (7 - tg) * 8) * 2, vupad = top ? ((c0 + 2 * cp) * UP + 2048) * 2 : OOB;

    char* pws = ws; unsigned svar = var;
    asm volatile("s_waitcnt lgkmcnt(0)\n"
"s_and_b32 s21, s21, 0xffff\n"
"s_mov_b32 s22, 524288\n"
"s_mov_b32 s23, 0x20000\n"
"s_and_b32 s25, s25, 0xffff\n"
"s_mov_b32 s27, 0x20000\n"
"s_and_b32 s29, s29, 0xffff\n"
"s_mov_b32 s30, 115343360\n"
"s_mov_b32 s31, 0x20000\n"
"s_lshl_b32 s44, s46, 12\n"
"s_add_u32 s41, s44, s48\n"
"s_add_u32 s41, s41, 65536\n"
"s_and_b32 s44, s46, 1\n"
"s_lshl_b32 s44, s44, 13\n"
"s_lshr_b32 s45, s46, 1\n"
"s_lshl_b32 s45, s45, 12\n"
"s_add_u32 s42, s44, s45\n"
"s_add_u32 s42, s42, s48\n"
"s_lshl_b32 s67, s37, 12\n"
"s_mov_b32 s64, 128\n"
"s_mov_b32 s65, 524288\n"
"s_xor_b32 s36, s64, s37\n"
"s_xor_b32 s38, s65, s67\n"
"s_mov_b32 s39, 32768\n"
"s_mov_b32 s40, 0\n"
"s_mov_b32 s43, 0\n"
"s_mov_b32 s54, 0\n"
"s_mov_b32 s66, 0\n"
"v_readlane_b32 s50, v255, 5\n"
"v_readlane_b32 s51, v255, 6\n"
"v_readlane_b32 s52, v255, 7\n"
"v_readlane_b32 s53, v255, 8\n"
"v_readlane_b32 s60, v255, 37\n"
"v_readlane_b32 s61, v255, 38\n"
"v_readlane_b32 s62, v255, 39\n"
"s_mov_b32 s63, 0x20000\n"
"v_mov_b32 v128, 0\n"
"v_mov_b32 v129, 0\n"
"v_mov_b32 v130, 0\n"
"v_mov_b32 v131, 0\n"
"s_nop 1\n"
"v_mfma_f32_32x32x16_bf16 a[0:15], v[128:131], v[128:131], 0\n"
"v_mfma_f32_32x32x16_bf16 a[16:31], v[128:131], v[128:131], 0\n"
"v_mfma_f32_32x32x16_bf16 a[32:47], v[128:131], v[128:131], 0\n"
"v_mfma_f32_32x32x16_bf16 a[48:63], v[128:131], v[128:131], 0\n"
"v_mfma_f32_32x32x16_bf16 a[64:79], v[128:131], v[128:131], 0\n"
"v_mfma_f32_32x32x16_bf16 a[80:95], v[128:131], v[128:131], 0\n"
"v_mfma_f32_32x32x16_bf16 a[96:111], v[128:131], v[128:131], 0\n"
"v_mfma_f32_32x32x16_bf16 a[112:127], v[128:131], v[128:131], 0\n"
"v_mfma_f32_32x32x16_bf16 a[128:143], v[128:131], v[128:131], 0\n"
"v_mfma_f32_32x32x16_bf16 a[144:159], v[128:131], v[128:131], 0\n"
"v_mfma_f32_32x32x16_bf16 a[160:175], v[128:131], v[128:131], 0\n"
"v_mfma_f32_32x32x16_bf16 a[176:191], v[128:131], v[128:131], 0\n"
"v_mfma_f32_32x32x16_bf16 a[192:207], v[128:131], v[128:131], 0\n"
"v_mfma_f32_32x32x16_bf16 a[208:223], v[128:131], v[128:131], 0\n"
"v_mfma_f32_32x32x16_bf16 a[224:239], v[128:131], v[128:131], 0\n"
"v_mfma_f32_32x32x16_bf16 a[240:255], v[128:131], v[128:131], 0\n"
"v_add_u32 v254, s40, v248\n"
"v_mbcnt_lo_u32_b32 v226, -1, 0\n"
"v_and_b32 v226, 15, v226\n"
"v_lshlrev_b32 v226, 7, v226\n"
"s_getpc_b64 s[72:73]\n"
"L_pc0:\n"
"s_cmp_eq_u32 s47, 1\n"
"s_cselect_b32 s44, L_KV_V_loop-L_KV_K_loop, 0\n"
"s_add_u32 s44, s44, L_KV_K_loop-L_pc0\n"
"s_add_u32 s72, s72, s44\n"
"s_addc_u32 s73, s73, 0\n"
"s_waitcnt vmcnt(16)\n"
"s_nop 7\n"
"s_mov_b32 s45, 0xffff0000\n"
"v_readfirstlane_b32 s57, v184\n"
"v_readfirstlane_b32 s44, v185\n"
"v_lshlrev_b32 v210, 16, v180\n"
"v_lshlrev_b32 v211, 16, v182\n"
"v_and_b32 v212, s45, v180\n"
"v_and_b32 v213, s45, v182\n"
"v_lshlrev_b32 v214, 16, v181\n"
"v_lshlrev_b32 v215, 16, v183\n"
"v_and_b32 v216, s45, v181\n"
"v_and_b32 v217, s45, v183\n"
"s_add_u32 s57, s57, 1\n"
"s_lshl_b32 s44, s44, 16\n"
"v_writelane_b32 v255, s44, 33\n"
"v_lshlrev_b32 v188, 16, v160\n"
"v_and_b32 v189, s45, v160\n"
"v_lshlrev_b32 v190, 16, v161\n"
"v_and_b32 v191, s45, v161\n"
"v_lshlrev_b32 v192, 16, v162\n"
"v_and_b32 v193, s45, v162\n"
"v_lshlrev_b32 v194, 16, v163\n"
"v_and_b32 v195, s45, v163\n"
"v_lshlrev_b32 v196, 16, v164\n"
"v_and_b32 v197, s45, v164\n"
"v_lshlrev_b32 v198, 16, v165\n"
"v_and_b32 v199, s45, v165\n"
"v_lshlrev_b32 v200, 16, v166\n"
"v_and_b32 v201, s45, v166\n"
"v_lshlrev_b32 v202, 16, v167\n"
"v_and_b32 v203, s45, v167\n"
"v_lshlrev_b32 v204, 16, v168\n"
"v_and_b32 v205, s45, v168\n"
"v_lshlrev_b32 v206, 16, v169\n"
"v_and_b32 v207, s45, v169\n"
"v_lshlrev_b32 v208, 16, v170\n"
"v_and_b32 v209, s45, v170\n"
"v_pk_mul_f32 v[218:219], v[188:189], v[216:217]\n"
"v_cvt_pk_bf16_f32 v152, v218, v219\n"
"v_pk_mul_f32 v[218:219], v[190:191], v[214:215]\n"
"v_lshlrev_b32 v220, 16, v152\n"
"v_and_b32 v221, s45, v152\n"
"v_cvt_pk_bf16_f32 v154, v218, v219\n"
"v_lshlrev_b32 v222, 16, v154\n"
"v_and_b32 v223, s45, v154\n"
"v_pk_add_f32 v[220:221], v[220:221], v[222:223]\n"
"v_cvt_pk_bf16_f32 v152, v220, v221\n"
"v_pk_mul_f32 v[218:219], v[192:193], v[212:213]\n"
"v_lshlrev_b32 v220, 16, v152\n"
"v_and_b32 v221, s45, v152\n"
"v_cvt_pk_bf16_f32 v154, v218, v219\n"
"v_lshlrev_b32 v222, 16, v154\n"
"v_and_b32 v223, s45, v154\n"
"v_pk_add_f32 v[220:221], v[220:221], v[222:223]\n"
"v_cvt_pk_bf16_f32 v152, v220, v221\n"
"v_pk_mul_f32 v[218:219], v[194:195], v[210:211]\n"
"v_lshlrev_b32 v220, 16, v152\n"
"v_and_b32 v221, s45, v152\n"
"v_cvt_pk_bf16_f32 v154, v218, v219\n"
"v_lshlrev_b32 v222, 16, v154\n"
"v_and_b32 v223, s45, v154\n"
"v_pk_add_f32 v[220:221], v[220:221], v[222:223]\n"
"v_cvt_pk_bf16_f32 v152, v220, v221\n"
"v_lshlrev_b32 v222, 16, v171\n"
"v_and_b32 v223, s45, v171\n"
"v_lshlrev_b32 v220, 16, v152\n"
"v_and_b32 v221, s45, v152\n"
"v_pk_mul_f32 v[128:129], v[220:221], v[222:223]\n"
"v_pk_mul_f32 v[218:219], v[190:191], v[216:217]\n"
"v_cvt_pk_bf16_f32 v153, v218, v219\n"
"v_pk_mul_f32 v[218:219], v[192:193], v[214:215]\n"
"v_lshlrev_b32 v220, 16, v153\n"
"v_and_b32 v221, s45, v153\n"
"v_cvt_pk_bf16_f32 v154, v218, v219\n"
"v_lshlrev_b32 v222, 16, v154\n"
"v_and_b32 v223, s45, v154\n"
"v_pk_add_f32 v[220:221], v[220:221], v[222:223]\n"
"v_cvt_pk_bf16_f32 v153, v220, v221\n"
"v_pk_mul_f32 v[218:219], v[194:195], v[212:213]\n"
"v_lshlrev_b32 v220, 16, v153\n"
"v_and_b32 v221, s45, v153\n"
"v_cvt_pk_bf16_f32 v154, v218, v219\n"
"v_lshlrev_b32 v222, 16, v154\n"
"v_and_b32 v223, s45, v154\n"
"v_pk_add_f32 v[220:221], v[220:221], v[222:223]\n"
"v_cvt_pk_bf16_f32 v153, v220, v221\n"
"v_pk_mul_f32 v[218:219], v[196:197], v[210:211]\n"
"v_lshlrev_b32 v220, 16, v153\n"
"v_and_b32 v221, s45, v153\n"
"v_cvt_pk_bf16_f32 v154, v218, v219\n"
"v_lshlrev_b32 v222, 16, v154\n"
"v_and_b32 v223, s45, v154\n"
"v_pk_add_f32 v[220:221], v[220:221], v[222:223]\n"
"v_cvt_pk_bf16_f32 v153, v220, v221\n"
"v_lshlrev_b32 v222, 16, v172\n"
"v_and_b32 v223, s45, v172\n"
"v_lshlrev_b32 v220, 16, v153\n"
"v_and_b32 v221, s45, v153\n"
"v_pk_mul_f32 v[130:131], v[220:221], v[222:223]\n"
"v_pk_mul_f32 v[218:219], v[192:193], v[216:217]\n"
"v_cvt_pk_bf16_f32 v152, v218, v219\n"
"v_pk_mul_f32 v[218:219], v[194:195], v[214:215]\n"
"v_lshlrev_b32 v220, 16, v152\n"
"v_and_b32 v221, s45, v152\n"
"v_cvt_pk_bf16_f32 v154, v218, v219\n"
"v_lshlrev_b32 v222, 16, v154\n"
"v_and_b32 v223, s45, v154\n"
"v_pk_add_f32 v[220:221], v[220:221], v[222:223]\n"
"v_cvt_pk_bf16_f32 v152, v220, v221\n"
"v_pk_mul_f32 v[218:219], v[196:197], v[212:213]\n"
"v_lshlrev_b32 v220, 16, v152\n"
"v_and_b32 v221, s45, v152\n"
"v_cvt_pk_bf16_f32 v154, v218, v219\n"
"v_lshlrev_b32 v222, 16, v154\n"
"v_and_b32 v223, s45, v154\n"
"v_pk_add_f32 v[220:221], v[220:221], v[222:223]\n"
"v_cvt_pk_bf16_f32 v152, v220, v221\n"
"v_pk_mul_f32 v[218:219], v[198:199], v[210:211]\n"
"v_lshlrev_b32 v220, 16, v152\n"
"v_and_b32 v221, s45, v152\n"
"v_cvt_pk_bf16_f32 v154, v218, v219\n"
"v_lshlrev_b32 v222, 16, v154\n"
"v_and_b32 v223, s45, v154\n"
"v_pk_add_f32 v[220:221], v[220:221], v[222:223]\n"
"v_cvt_pk_bf16_f32 v152, v220, v221\n"
"v_lshlrev_b32 v222, 16, v173\n"
"v_and_b32 v223, s45, v173\n"
"v_lshlrev_b32 v220, 16, v152\n"
"v_and_b32 v221, s45, v152\n"
"v_pk_mul_f32 v[132:133], v[220:221], v[222:223]\n"
"v_pk_mul_f32 v[218:219], v[194:195], v[216:217]\n"
"v_cvt_pk_bf16_f32 v153, v218, v219\n"
"v_pk_mul_f32 v[218:219], v[196:197], v[214:215]\n"
"v_lshlrev_b32 v220, 16, v153\n"
"v_and_b32 v221, s45, v153\n"
"v_cvt_pk_bf16_f32 v154, v218, v219\n"
"v_lshlrev_b32 v222, 16, v154\n"
"v_and_b32 v223, s45, v154\n"
"v_pk_add_f32 v[220:221], v[220:221], v[222:223]\n"
"v_cvt_pk_bf16_f32 v153, v220, v221\n"
"v_pk_mul_f32 v[218:219], v[198:199], v[212:213]\n"
"v_lshlrev_b32 v220, 16, v153\n"
"v_and_b32 v221, s45, v153\n"
"v_cvt_pk_bf16_f32 v154, v218, v219\n"
"v_lshlrev_b32 v222, 16, v154\n"
"v_and_b32 v223, s45, v154\n"
"v_pk_add_f32 v[220:221], v[220:221], v[222:223]\n"
"v_cvt_pk_bf16_f32 v153, v220, v221\n"
"v_pk_mul_f32 v[218:219], v[200:201], v[210:211]\n"
"v_lshlrev_b32 v220, 16, v153\n"
"s_add_u32 m0, s41, s39\n"
"s_nop 0\n"
"buffer_load_dwordx4 v232, s[20:23], s36 offen offset:0 lds\n"
"buffer_load_dwordx4 v233, s[20:23], s36 offen offset:1024 lds\n"
"buffer_load_dwordx4 v234, s[20:23], s36 offen offset:2048 lds\n"
"buffer_load_dwordx4 v235, s[20:23], s36 offen offset:3072 lds\n"
"s_add_u32 s39, s39, 16384\n"
"s_cmp_eq_u32 s39, 98304\n"
"s_cselect_b32 s39, 0, s39\n"
"s_add_u32 m0, s42, 32768\n"
"s_nop 0\n"
"buffer_load_dwordx4 v240, s[24:27], s38 offen offset:0 lds\n"
"buffer_load_dwordx4 v241, s[24:27], s38 offen offset:1024 lds\n"
"buffer_load_dwordx4 v242, s[24:27], s38 offen offset:2048 lds\n"
"buffer_load_dwordx4 v243, s[24:27], s38 offen offset:3072 lds\n"
"s_add_u32 s65, s65, 262144\n"
"s_xor_b32 s38, s65, s67\n"
"v_and_b32 v221, s45, v153\n"
"v_cvt_pk_bf16_f32 v154, v218, v219\n"
"v_lshlrev_b32 v222, 16, v154\n"
"v_and_b32 v223, s45, v154\n"
"v_pk_add_f32 v[220:221], v[220:221], v[222:223]\n"
"v_cvt_pk_bf16_f32 v153, v220, v221\n"
"v_lshlrev_b32 v222, 16, v174\n"
"v_and_b32 v223, s45, v174\n"
"v_lshlrev_b32 v220, 16, v153\n"
"v_and_b32 v221, s45, v153\n"
"v_pk_mul_f32 v[134:135], v[220:221], v[222:223]\n"
"v_pk_mul_f32 v[218:219], v[196:197], v[216:217]\n"
"v_cvt_pk_bf16_f32 v152, v218, v219\n"
"v_pk_mul_f32 v[218:219], v[198:199], v[214:215]\n"
"v_lshlrev_b32 v220, 16, v152\n"
"v_and_b32 v221, s45, v152\n"
"v_cvt_pk_bf16_f32 v154, v218, v219\n"
"v_lshlrev_b32 v222, 16, v154\n"
"v_and_b32 v223, s45, v154\n"
"v_pk_add_f32 v[220:221], v[220:221], v[222:223]\n"
"v_cvt_pk_bf16_f32 v152, v220, v221\n"
"v_pk_mul_f32 v[218:219], v[200:201], v[212:213]\n"
"v_lshlrev_b32 v220, 16, v152\n"
"v_and_b32 v221, s45, v152\n"
"v_cvt_pk_bf16_f32 v154, v218, v219\n"
"v_lshlrev_b32 v222, 16, v154\n"
"v_and_b32 v223, s45, v154\n"
"v_pk_add_f32 v[220:221], v[220:221], v[222:223]\n"
"v_cvt_pk_bf16_f32 v152, v220, v221\n"
"v_pk_mul_f32 v[218:219], v[202:203], v[210:211]\n"
"v_lshlrev_b32 v220, 16, v152\n"
"v_and_b32 v221, s45, v152\n"
"v_cvt_pk_bf16_f32 v154, v218, v219\n"
"v_lshlrev_b32 v222, 16, v154\n"
"v_and_b32 v223, s45, v154\n"
"v_pk_add_f32 v[220:221], v[220:221], v[222:223]\n"
"v_cvt_pk_bf16_f32 v152, v220, v221\n"
"v_lshlrev_b32 v222, 16, v175\n"
"v_and_b32 v223, s45, v175\n"
"v_lshlrev_b32 v220, 16, v152\n"
"v_and_b32 v221, s45, v152\n"
"v_pk_mul_f32 v[136:137], v[220:221], v[222:223]\n"
"v_pk_mul_f32 v[218:219], v[198:199], v[216:217]\n"
"v_cvt_pk_bf16_f32 v153, v218, v219\n"
"v_pk_mul_f32 v[218:219], v[200:201], v[214:215]\n"
"v_lshlrev_b32 v220, 16, v153\n"
"v_and_b32 v221, s45, v153\n"
"v_cvt_pk_bf16_f32 v154, v218, v219\n"
"v_lshlrev_b32 v222, 16, v154\n"
"v_and_b32 v223, s45, v154\n"
"v_pk_add_f32 v[220:221], v[220:221], v[222:223]\n"
"v_cvt_pk_bf16_f32 v153, v220, v221\n"
"v_pk_mul_f32 v[218:219], v[202:203], v[212:213]\n"
"v_lshlrev_b32 v220, 16, v153\n"
"v_and_b32 v221, s45, v153\n"
"v_cvt_pk_bf16_f32 v154, v218, v219\n"
"v_lshlrev_b32 v222, 16, v154\n"
"v_and_b32 v223, s45, v154\n"
"v_pk_add_f32 v[220:221], v[220:221], v[222:223]\n"
"v_cvt_pk_bf16_f32 v153, v220, v221\n"
"v_pk_mul_f32 v[218:219], v[204:205], v[210:211]\n"
"v_lshlrev_b32 v220, 16, v153\n"
"v_and_b32 v221, s45, v153\n"
"v_cvt_pk_bf16_f32 v154, v218, v219\n"
"v_lshlrev_b32 v222, 16, v154\n"
"v_and_b32 v223, s45, v154\n"
"v_pk_add_f32 v[220:221], v[220:221], v[222:223]\n"
"v_cvt_pk_bf16_f32 v153, v220, v221\n"
"v_lshlrev_b32 v222, 16, v176\n"
"v_and_b32 v223, s45, v176\n"
"v_lshlrev_b32 v220, 16, v153\n"
"v_and_b32 v221, s45, v153\n"
"v_pk_mul_f32 v[138:139], v[220:221], v[222:223]\n"
"v_pk_mul_f32 v[218:219], v[200:201], v[216:217]\n"
"v_cvt_pk_bf16_f32 v152, v218, v219\n"
"v_pk_mul_f32 v[218:219], v[202:203], v[214:215]\n"
"v_lshlrev_b32 v220, 16, v152\n"
"v_and_b32 v221, s45, v152\n"
"v_cvt_pk_bf16_f32 v154, v218, v219\n"
"v_lshlrev_b32 v222, 16, v154\n"
"v_and_b32 v223, s45, v154\n"
"v_pk_add_f32 v[220:221], v[220:221], v[222:223]\n"
"v_cvt_pk_bf16_f32 v152, v220, v221\n"
"v_pk_mul_f32 v[218:219], v[204:205], v[212:213]\n"
"v_lshlrev_b32 v220, 16, v152\n"
"v_and_b32 v221, s45, v152\n"
"v_cvt_pk_bf16_f32 v154, v218, v219\n"
"v_lshlrev_b32 v222, 16, v154\n"
"v_and_b32 v223, s45, v154\n"
"v_pk_add_f32 v[220:221], v[220:221], v[222:223]\n"
"v_cvt_pk_bf16_f32 v152, v220, v221\n"
"v_pk_mul_f32 v[218:219], v[206:207], v[210:211]\n"
"v_lshlrev_b32 v220, 16, v152\n"
"v_and_b32 v221, s45, v152\n"
"v_cvt_pk_bf16_f32 v154, v218, v219\n"
"v_lshlrev_b32 v222, 16, v154\n"
"v_and_b32 v223, s45, v154\n"
"v_pk_add_f32 v[220:221], v[220:221], v[222:223]\n"
"v_cvt_pk_bf16_f32 v152, v220, v221\n"
"v_lshlrev_b32 v222, 16, v177\n"
"v_and_b32 v223, s45, v177\n"
"v_lshlrev_b32 v220, 16, v152\n"
"v_and_b32 v221, s45, v152\n"
"v_pk_mul_f32 v[140:141], v[220:221], v[222:223]\n"
"v_pk_mul_f32 v[218:219], v[202:203], v[216:217]\n"
"v_cvt_pk_bf16_f32 v153, v218, v219\n"
"v_pk_mul_f32 v[218:219], v[204:205], v[214:215]\n"
"v_lshlrev_b32 v220, 16, v153\n"
"v_and_b32 v221, s45, v153\n"
"v_cvt_pk_bf16_f32 v154, v218, v219\n"
"v_lshlrev_b32 v222, 16, v154\n"
"v_and_b32 v223, s45, v154\n"
"v_pk_add_f32 v[220:221], v[220:221], v[222:223]\n"
"v_cvt_pk_bf16_f32 v153, v220, v221\n"
"v_pk_mul_f32 v[218:219], v[206:207], v[212:213]\n"
"v_lshlrev_b32 v220, 16, v153\n"
"v_and_b32 v221, s45, v153\n"
"v_cvt_pk_bf16_f32 v154, v218, v219\n"
"v_lshlrev_b32 v222, 16, v154\n"
"v_and_b32 v223, s45, v154\n"
"v_pk_add_f32 v[220:221], v[220:221], v[222:223]\n"
"v_cvt_pk_bf16_f32 v153, v220, v221\n"
"v_pk_mul_f32 v[218:219], v[208:209], v[210:211]\n"
"v_lshlrev_b32 v220, 16, v153\n"
"v_and_b32 v221, s45, v153\n"
"v_cvt_pk_bf16_f32 v154, v218, v219\n"
"v_lshlrev_b32 v222, 16, v154\n"
"v_and_b32 v223, s45, v154\n"
"v_pk_add_f32 v[220:221], v[220:221], v[222:223]\n"
"v_cvt_pk_bf16_f32 v153, v220, v221\n"
"v_lshlrev_b32 v222, 16, v178\n"
"v_and_b32 v223, s45, v178\n"
"v_lshlrev_b32 v220, 16, v153\n"
"v_and_b32 v221, s45, v153\n"
"v_pk_mul_f32 v[142:143], v[220:221], v[222:223]\n"
"v_readlane_b32 s44, v255, 10\n"
"v_mov_b32 v154, 0\n"
"v_cvt_pk_bf16_f32 v144, v142, v140\n"
"v_cvt_pk_bf16_f32 v145, v138, v136\n"
"v_cvt_pk_bf16_f32 v146, v134, v132\n"
"v_cvt_pk_bf16_f32 v147, v130, v128\n"
"v_cvt_pk_bf16_f32 v148, v143, v141\n"
"v_cvt_pk_bf16_f32 v149, v139, v137\n"
"v_cvt_pk_bf16_f32 v150, v135, v133\n"
"v_cvt_pk_bf16_f32 v151, v131, v129\n"
"s_add_u32 s45, s44, 4224\n"
"buffer_store_dwordx4 v[144:147], v186, s[28:31], s44 offen offset:0 sc1\n"
"buffer_store_dword v154, v187, s[28:31], s44 offen offset:0 sc1\n"
"buffer_store_dwordx4 v[148:151], v186, s[28:31], s45 offen offset:0 sc1\n"
"buffer_store_dword v154, v187, s[28:31], s45 offen offset:0 sc1\n"
"s_waitcnt vmcnt(12)\n"
"s_barrier\n"
"s_mov_b32 s54, 1\n"
"s_mov_b32 s43, 1\n"
"ds_read_b64_tr_b16 v[32:33], v250 offset:0\n"
"ds_read_b64_tr_b16 v[34:35], v250 offset:1024\n"
"ds_read_b64_tr_b16 v[36:37], v251 offset:0\n"
"ds_read_b64_tr_b16 v[38:39], v251 offset:1024\n"
"ds_read_b64_tr_b16 v[40:41], v250 offset:256\n"
"ds_read_b64_tr_b16 v[42:43], v250 offset:1280\n"
"ds_read_b64_tr_b16 v[44:45], v251 offset:256\n"
"ds_read_b64_tr_b16 v[46:47], v251 offset:1280\n"
"ds_read_b128 v[0:3], v254 offset:0\n"
"ds_read_b128 v[4:7], v254 offset:2048\n"
"ds_read_b128 v[8:11], v254 offset:4096\n"
"ds_read_b128 v[12:15], v254 offset:6144\n"
"s_add_u32 m0, s41, s39\n"
"s_nop 0\n"
"buffer_load_dwordx4 v236, s[20:23], s36 offen offset:0 lds\n"
"buffer_load_dwordx4 v237, s[20:23], s36 offen offset:1024 lds\n"
"buffer_load_dwordx4 v238, s[20:23], s36 offen offset:2048 lds\n"
"buffer_load_dwordx4 v239, s[20:23], s36 offen offset:3072 lds\n"
"s_add_u32 s39, s39, 16384\n"
"s_cmp_eq_u32 s39, 98304\n"
"s_cselect_b32 s39, 0, s39\n"
"s_add_u32 s64, s64, 128\n"
"s_xor_b32 s36, s64, s37\n"
"s_add_u32 m0, s42, 49152\n"
"s_nop 0\n"
"buffer_load_dwordx4 v240, s[24:27], s38 offen offset:0 lds\n"
"buffer_load_dwordx4 v241, s[24:27], s38 offen offset:1024 lds\n"
"buffer_load_dwordx4 v242, s[24:27], s38 offen offset:2048 lds\n"
"buffer_load_dwordx4 v243, s[24:27], s38 offen offset:3072 lds\n"
"s_add_u32 s65, s65, 262144\n"
"s_xor_b32 s38, s65, s67\n"
"ds_read_b64_tr_b16 v[48:49], v250 offset:512\n"
"ds_read_b64_tr_b16 v[50:51], v250 offset:1536\n"
"ds_read_b64_tr_b16 v[52:53], v251 offset:512\n"
"ds_read_b64_tr_b16 v[54:55], v251 offset:1536\n"
"ds_read_b64_tr_b16 v[56:57], v250 offset:768\n"
"ds_read_b64_tr_b16 v[58:59], v250 offset:1792\n"
"ds_read_b64_tr_b16 v[60:61], v251 offset:768\n"
"ds_read_b64_tr_b16 v[62:63], v251 offset:1792\n"
"ds_read_b128 v[16:19], v254 offset:8192\n"
"ds_read_b128 v[20:23], v254 offset:10240\n"
"ds_read_b128 v[24:27], v254 offset:12288\n"
"ds_read_b128 v[28:31], v254 offset:14336\n"
"s_add_u32 m0, s41, s39\n"
"s_nop 0\n"
"buffer_load_dwordx4 v232, s[20:23], s36 offen offset:0 lds\n"
"buffer_load_dwordx4 v233, s[20:23], s36 offen offset:1024 lds\n"
"buffer_load_dwordx4 v234, s[20:23], s36 offen offset:2048 lds\n"
"buffer_load_dwordx4 v235, s[20:23], s36 offen offset:3072 lds\n"
"s_add_u32 s39, s39, 16384\n"
"s_cmp_eq_u32 s39, 98304\n"
"s_cselect_b32 s39, 0, s39\n"
"s_waitcnt lgkmcnt(0)\n"
"s_barrier\n"
"s_setpc_b64 s[72:73]\n"
"L_KV_K_loop:\n"
"v_mfma_f32_16x16x32_bf16 a[0:3], v[32:35], v[0:3], a[0:3]\n"
"v_add_u32 v254, s40, v249\n"
"v_mfma_f32_16x16x32_bf16 a[4:7], v[36:39], v[0:3], a[4:7]\n"
"ds_read_b64_tr_b16 v[96:97], v250 offset:16384\n"
"v_mfma_f32_16x16x32_bf16 a[8:11], v[40:43], v[0:3], a[8:11]\n"
"s_add_u32 m0, s42, 0\n"
"ds_read_b64_tr_b16 v[98:99], v250 offset:17408\n"
"v_mfma_f32_16x16x32_bf16 a[12:15], v[44:47], v[0:3], a[12:15]\n"
"buffer_load_dwordx4 v240, s[24:27], s38 offen offset:0 lds\n"
"v_mfma_f32_16x16x32_bf16 a[16:19], v[48:51], v[0:3], a[16:19]\n"
"ds_read_b64_tr_b16 v[100:101], v251 offset:16384\n"
"v_mfma_f32_16x16x32_bf16 a[20:23], v[52:55], v[0:3], a[20:23]\n"
"ds_read_b64_tr_b16 v[102:103], v251 offset:17408\n"
"v_mfma_f32_16x16x32_bf16 a[24:27], v[56:59], v[0:3], a[24:27]\n"
"v_mfma_f32_16x16x32_bf16 a[28:31], v[60:63], v[0:3], a[28:31]\n"
"ds_read_b64_tr_b16 v[104:105], v250 offset:16640\n"
"v_mfma_f32_16x16x32_bf16 a[60:63], v[60:63], v[4:7], a[60:63]\n"
"ds_read_b64_tr_b16 v[106:107], v250 offset:17664\n"
"v_mfma_f32_16x16x32_bf16 a[56:59], v[56:59], v[4:7], a[56:59]\n"
"buffer_load_dwordx4 v241, s[24:27], s38 offen offset:1024 lds\n"
"v_mfma_f32_16x16x32_bf16 a[52:55], v[52:55], v[4:7], a[52:55]\n"
"ds_read_b64_tr_b16 v[108:109], v251 offset:16640\n"
"v_mfma_f32_16x16x32_bf16 a[48:51], v[48:51], v[4:7], a[48:51]\n"
"ds_read_b64_tr_b16 v[110:111], v251 offset:17664\n"
"v_mfma_f32_16x16x32_bf16 a[44:47], v[44:47], v[4:7], a[44:47]\n"
"v_mfma_f32_16x16x32_bf16 a[40:43], v[40:43], v[4:7], a[40:43]\n"
"ds_read_b128 v[64:67], v254 offset:0\n"
"v_mfma_f32_16x16x32_bf16 a[36:39], v[36:39], v[4:7], a[36:39]\n"
"ds_read_b128 v[68:71], v254 offset:2048\n"
"v_mfma_f32_16x16x32_bf16 a[32:35], v[32:35], v[4:7], a[32:35]\n"
"buffer_load_dwordx4 v242, s[24:27], s38 offen offset:2048 lds\n"
"v_mfma_f32_16x16x32_bf16 a[64:67], v[32:35], v[8:11], a[64:67]\n"
"ds_read_b128 v[72:75], v254 offset:4096\n"
"v_mfma_f32_16x16x32_bf16 a[68:71], v[36:39], v[8:11], a[68:71]\n"
"ds_read_b128 v[76:79], v254 offset:6144\n"
"v_mfma_f32_16x16x32_bf16 a[72:75], v[40:43], v[8:11], a[72:75]\n"
"v_mfma_f32_16x16x32_bf16 a[76:79], v[44:47], v[8:11], a[76:79]\n"
"ds_read_b64_tr_b16 v[112:113], v250 offset:16896\n"
"v_mfma_f32_16x16x32_bf16 a[80:83], v[48:51], v[8:11], a[80:83]\n"
"ds_read_b64_tr_b16 v[114:115], v250 offset:17920\n"
"v_mfma_f32_16x16x32_bf16 a[84:87], v[52:55], v[8:11], a[84:87]\n"
"buffer_load_dwordx4 v243, s[24:27], s38 offen offset:3072 lds\n"
"v_mfma_f32_16x16x32_bf16 a[88:91], v[56:59], v[8:11], a[88:91]\n"
"ds_read_b64_tr_b16 v[116:117], v251 offset:16896\n"
"v_mfma_f32_16x16x32_bf16 a[92:95], v[60:63], v[8:11], a[92:95]\n"
"ds_read_b64_tr_b16 v[118:119], v251 offset:17920\n"
"v_mfma_f32_16x16x32_bf16 a[124:127], v[60:63], v[12:15], a[124:127]\n"
"buffer_load_dword v227, v226, s[60:63], s66 offen offset:0\n"
"v_mfma_f32_16x16x32_bf16 a[120:123], v[56:59], v[12:15], a[120:123]\n"
"ds_read_b64_tr_b16 v[120:121], v250 offset:17152\n"
"v_mfma_f32_16x16x32_bf16 a[116:119], v[52:55], v[12:15], a[116:119]\n"
"s_add_u32 m0, s41, s39\n"
"ds_read_b64_tr_b16 v[122:123], v250 offset:18176\n"
"v_mfma_f32_16x16x32_bf16 a[112:115], v[48:51], v[12:15], a[112:115]\n"
"buffer_load_dwordx4 v236, s[20:23], s36 offen offset:0 lds\n"
"v_mfma_f32_16x16x32_bf16 a[108:111], v[44:47], v[12:15], a[108:111]\n"
"ds_read_b64_tr_b16 v[124:125], v251 offset:17152\n"
"v_mfma_f32_16x16x32_bf16 a[104:107], v[40:43], v[12:15], a[104:107]\n"
"ds_read_b64_tr_b16 v[126:127], v251 offset:18176\n"
"v_mfma_f32_16x16x32_bf16 a[100:103], v[36:39], v[12:15], a[100:103]\n"
"v_mfma_f32_16x16x32_bf16 a[96:99], v[32:35], v[12:15], a[96:99]\n"
"ds_read_b128 v[80:83], v254 offset:8192\n"
"v_mfma_f32_16x16x32_bf16 a[128:131], v[32:35], v[16:19], a[128:131]\n"
"ds_read_b128 v[84:87], v254 offset:10240\n"
"v_mfma_f32_16x16x32_bf16 a[132:135], v[36:39], v[16:19], a[132:135]\n"
"buffer_load_dwordx4 v237, s[20:23], s36 offen offset:1024 lds\n"
"v_mfma_f32_16x16x32_bf16 a[136:139], v[40:43], v[16:19], a[136:139]\n"
"ds_read_b128 v[88:91], v254 offset:12288\n"
"v_mfma_f32_16x16x32_bf16 a[140:143], v[44:47], v[16:19], a[140:143]\n"
"ds_read_b128 v[92:95], v254 offset:14336\n"
"v_mfma_f32_16x16x32_bf16 a[144:147], v[48:51], v[16:19], a[144:147]\n"
"v_mfma_f32_16x16x32_bf16 a[148:151], v[52:55], v[16:19], a[148:151]\n"
"v_mfma_f32_16x16x32_bf16 a[152:155], v[56:59], v[16:19], a[152:155]\n"
"v_mfma_f32_16x16x32_bf16 a[156:159], v[60:63], v[16:19], a[156:159]\n"
"buffer_load_dwordx4 v238, s[20:23], s36 offen offset:2048 lds\n"
"v_mfma_f32_16x16x32_bf16 a[188:191], v[60:63], v[20:23], a[188:191]\n"
"v_mfma_f32_16x16x32_bf16 a[184:187], v[56:59], v[20:23], a[184:187]\n"
"v_mfma_f32_16x16x32_bf16 a[180:183], v[52:55], v[20:23], a[180:183]\n"
"v_mfma_f32_16x16x32_bf16 a[176:179], v[48:51], v[20:23], a[176:179]\n"
"v_mfma_f32_16x16x32_bf16 a[172:175], v[44:47], v[20:23], a[172:175]\n"
"v_mfma_f32_16x16x32_bf16 a[168:171], v[40:43], v[20:23], a[168:171]\n"
"buffer_load_dwordx4 v239, s[20:23], s36 offen offset:3072 lds\n"
"v_mfma_f32_16x16x32_bf16 a[164:167], v[36:39], v[20:23], a[164:167]\n"
"s_add_u32 s66, s66, 2048\n"
"v_mfma_f32_16x16x32_bf16 a[160:163], v[32:35], v[20:23], a[160:163]\n"
"v_mfma_f32_16x16x32_bf16 a[192:195], v[32:35], v[24:27], a[192:195]\n"
"s_add_u32 s65, s65, 262144\n"
"s_xor_b32 s38, s65, s67\n"
"v_mfma_f32_16x16x32_bf16 a[196:199], v[36:39], v[24:27], a[196:199]\n"
"s_add_u32 s39, s39, 16384\n"
"v_mfma_f32_16x16x32_bf16 a[200:203], v[40:43], v[24:27], a[200:203]\n"
"s_cmp_eq_u32 s39, 98304\n"
"s_cselect_b32 s39, 0, s39\n"
"v_mfma_f32_16x16x32_bf16 a[204:207], v[44:47], v[24:27], a[204:207]\n"
"s_add_u32 s64, s64, 128\n"
"s_xor_b32 s36, s64, s37\n"
"v_mfma_f32_16x16x32_bf16 a[208:211], v[48:51], v[24:27], a[208:211]\n"
"s_add_u32 s40, s40, 32768\n"
"v_mfma_f32_16x16x32_bf16 a[212:215], v[52:55], v[24:27], a[212:215]\n"
"s_cmp_eq_u32 s40, 98304\n"
"s_cselect_b32 s40, 0, s40\n"
"v_mfma_f32_16x16x32_bf16 a[216:219], v[56:59], v[24:27], a[216:219]\n"
"v_mfma_f32_16x16x32_bf16 a[220:223], v[60:63], v[24:27], a[220:223]\n"
"v_mfma_f32_16x16x32_bf16 a[252:255], v[60:63], v[28:31], a[252:255]\n"
"v_mfma_f32_16x16x32_bf16 a[248:251], v[56:59], v[28:31], a[248:251]\n"
"v_mfma_f32_16x16x32_bf16 a[244:247], v[52:55], v[28:31], a[244:247]\n"
"v_mfma_f32_16x16x32_bf16 a[240:243], v[48:51], v[28:31], a[240:243]\n"
"v_mfma_f32_16x16x32_bf16 a[236:239], v[44:47], v[28:31], a[236:239]\n"
"v_mfma_f32_16x16x32_bf16 a[232:235], v[40:43], v[28:31], a[232:235]\n"
"v_mfma_f32_16x16x32_bf16 a[228:231], v[36:39], v[28:31], a[228:231]\n"
"s_waitcnt vmcnt(16) lgkmcnt(0)\n"
"s_barrier\n"
"v_mfma_f32_16x16x32_bf16 a[224:227], v[32:35], v[28:31], a[224:227]\n"
"v_mfma_f32_16x16x32_bf16 a[0:3], v[96:99], v[64:67], a[0:3]\n"
"v_add_u32 v254, s40, v248\n"
"v_mfma_f32_16x16x32_bf16 a[4:7], v[100:103], v[64:67], a[4:7]\n"
"ds_read_b64_tr_b16 v[32:33], v250 offset:32768\n"
"v_mfma_f32_16x16x32_bf16 a[8:11], v[104:107], v[64:67], a[8:11]\n"
"s_add_u32 m0, s42, 16384\n"
"ds_read_b64_tr_b16 v[34:35], v250 offset:33792\n"
"v_mfma_f32_16x16x32_bf16 a[12:15], v[108:111], v[64:67], a[12:15]\n"
"buffer_load_dwordx4 v240, s[24:27], s38 offen offset:0 lds\n"
"v_mfma_f32_16x16x32_bf16 a[16:19], v[112:115], v[64:67], a[16:19]\n"
"ds_read_b64_tr_b16 v[36:37], v251 offset:32768\n"
"v_mfma_f32_16x16x32_bf16 a[20:23], v[116:119], v[64:67], a[20:23]\n"
"ds_read_b64_tr_b16 v[38:39], v251 offset:33792\n"
"v_mfma_f32_16x16x32_bf16 a[24:27], v[120:123], v[64:67], a[24:27]\n"
"v_mfma_f32_16x16x32_bf16 a[28:31], v[124:127], v[64:67], a[28:31]\n"
"ds_read_b64_tr_b16 v[40:41], v250 offset:33024\n"
"v_mfma_f32_16x16x32_bf16 a[60:63], v[124:127], v[68:71], a[60:63]\n"
"ds_read_b64_tr_b16 v[42:43], v250 offset:34048\n"
"v_mfma_f32_16x16x32_bf16 a[56:59], v[120:123], v[68:71], a[56:59]\n"
"buffer_load_dwordx4 v241, s[24:27], s38 offen offset:1024 lds\n"
"v_mfma_f32_16x16x32_bf16 a[52:55], v[116:119], v[68:71], a[52:55]\n"
"ds_read_b64_tr_b16 v[44:45], v251 offset:33024\n"
"v_mfma_f32_16x16x32_bf16 a[48:51], v[112:115], v[68:71], a[48:51]\n"
"ds_read_b64_tr_b16 v[46:47], v251 offset:34048\n"
"v_mfma_f32_16x16x32_bf16 a[44:47], v[108:111], v[68:71], a[44:47]\n"
"v_mfma_f32_16x16x32_bf16 a[40:43], v[104:107], v[68:71], a[40:43]\n"
"ds_read_b128 v[0:3], v254 offset:0\n"
"v_mfma_f32_16x16x32_bf16 a[36:39], v[100:103], v[68:71], a[36:39]\n"
"ds_read_b128 v[4:7], v254 offset:2048\n"
"v_mfma_f32_16x16x32_bf16 a[32:35], v[96:99], v[68:71], a[32:35]\n"
"buffer_load_dwordx4 v242, s[24:27], s38 offen offset:2048 lds\n"
"v_mfma_f32_16x16x32_bf16 a[64:67], v[96:99], v[72:75], a[64:67]\n"
"ds_read_b128 v[8:11], v254 offset:4096\n"
"v_mfma_f32_16x16x32_bf16 a[68:71], v[100:103], v[72:75], a[68:71]\n"
"ds_read_b128 v[12:15], v254 offset:6144\n"
"v_mfma_f32_16x16x32_bf16 a[72:75], v[104:107], v[72:75], a[72:75]\n"
"v_mfma_f32_16x16x32_bf16 a[76:79], v[108:111], v[72:75], a[76:79]\n"
"ds_read_b64_tr_b16 v[48:49], v250 offset:33280\n"
"v_mfma_f32_16x16x32_bf16 a[80:83], v[112:115], v[72:75], a[80:83]\n"
"ds_read_b64_tr_b16 v[50:51], v250 offset:34304\n"
"v_mfma_f32_16x16x32_bf16 a[84:87], v[116:119], v[72:75], a[84:87]\n"
"buffer_load_dwordx4 v243, s[24:27], s38 offen offset:3072 lds\n"
"v_mfma_f32_16x16x32_bf16 a[88:91], v[120:123], v[72:75], a[88:91]\n"
"ds_read_b64_tr_b16 v[52:53], v251 offset:33280\n"
"v_mfma_f32_16x16x32_bf16 a[92:95], v[124:127], v[72:75], a[92:95]\n"
"ds_read_b64_tr_b16 v[54:55], v251 offset:34304\n"
"v_mfma_f32_16x16x32_bf16 a[124:127], v[124:127], v[76:79], a[124:127]\n"
"v_mfma_f32_16x16x32_bf16 a[120:123], v[120:123], v[76:79], a[120:123]\n"
"ds_read_b64_tr_b16 v[56:57], v250 offset:33536\n"
"v_mfma_f32_16x16x32_bf16 a[116:119], v[116:119], v[76:79], a[116:119]\n"
"s_add_u32 m0, s41, s39\n"
"ds_read_b64_tr_b16 v[58:59], v250 offset:34560\n"
"v_mfma_f32_16x16x32_bf16 a[112:115], v[112:115], v[76:79], a[112:115]\n"
"buffer_load_dwordx4 v232, s[20:23], s36 offen offset:0 lds\n"
"v_mfma_f32_16x16x32_bf16 a[108:111], v[108:111], v[76:79], a[108:111]\n"
"ds_read_b64_tr_b16 v[60:61], v251 offset:33536\n"
"v_mfma_f32_16x16x32_bf16 a[104:107], v[104:107], v[76:79], a[104:107]\n"
"ds_read_b64_tr_b16 v[62:63], v251 offset:34560\n"
"v_mfma_f32_16x16x32_bf16 a[100:103], v[100:103], v[76:79], a[100:103]\n"
"v_mfma_f32_16x16x32_bf16 a[96:99], v[96:99], v[76:79], a[96:99]\n"
"ds_read_b128 v[16:19], v254 offset:8192\n"
"v_mfma_f32_16x16x32_bf16 a[128:131], v[96:99], v[80:83], a[128:131]\n"
"ds_read_b128 v[20:23], v254 offset:10240\n"
"v_mfma_f32_16x16x32_bf16 a[132:135], v[100:103], v[80:83], a[132:135]\n"
"buffer_load_dwordx4 v233, s[20:23], s36 offen offset:1024 lds\n"
"v_mfma_f32_16x16x32_bf16 a[136:139], v[104:107], v[80:83], a[136:139]\n"
"ds_read_b128 v[24:27], v254 offset:12288\n"
"v_mfma_f32_16x16x32_bf16 a[140:143], v[108:111], v[80:83], a[140:143]\n"
"ds_read_b128 v[28:31], v254 offset:14336\n"
"v_mfma_f32_16x16x32_bf16 a[144:147], v[112:115], v[80:83], a[144:147]\n"
"v_mfma_f32_16x16x32_bf16 a[148:151], v[116:119], v[80:83], a[148:151]\n"
"v_mfma_f32_16x16x32_bf16 a[152:155], v[120:123], v[80:83], a[152:155]\n"
"v_mfma_f32_16x16x32_bf16 a[156:159], v[124:127], v[80:83], a[156:159]\n"
"buffer_load_dwordx4 v234, s[20:23], s36 offen offset:2048 lds\n"
"v_mfma_f32_16x16x32_bf16 a[188:191], v[124:127], v[84:87], a[188:191]\n"
"v_mfma_f32_16x16x32_bf16 a[184:187], v[120:123], v[84:87], a[184:187]\n"
"v_mfma_f32_16x16x32_bf16 a[180:183], v[116:119], v[84:87], a[180:183]\n"
"v_mfma_f32_16x16x32_bf16 a[176:179], v[112:115], v[84:87], a[176:179]\n"
"v_mfma_f32_16x16x32_bf16 a[172:175], v[108:111], v[84:87], a[172:175]\n"
"v_mfma_f32_16x16x32_bf16 a[168:171], v[104:107], v[84:87], a[168:171]\n"
"buffer_load_dwordx4 v235, s[20:23], s36 offen offset:3072 lds\n"
"v_mfma_f32_16x16x32_bf16 a[164:167], v[100:103], v[84:87], a[164:167]\n"
"v_mfma_f32_16x16x32_bf16 a[160:163], v[96:99], v[84:87], a[160:163]\n"
"v_mfma_f32_16x16x32_bf16 a[192:195], v[96:99], v[88:91], a[192:195]\n"
"s_add_u32 s65, s65, 262144\n"
"s_xor_b32 s38, s65, s67\n"
"v_mfma_f32_16x16x32_bf16 a[196:199], v[100:103], v[88:91], a[196:199]\n"
"s_add_u32 s39, s39, 16384\n"
"v_mfma_f32_16x16x32_bf16 a[200:203], v[104:107], v[88:91], a[200:203]\n"
"s_cmp_eq_u32 s39, 98304\n"
"s_cselect_b32 s39, 0, s39\n"
"v_mfma_f32_16x16x32_bf16 a[204:207], v[108:111], v[88:91], a[204:207]\n"
"v_mfma_f32_16x16x32_bf16 a[208:211], v[112:115], v[88:91], a[208:211]\n"
"v_mfma_f32_16x16x32_bf16 a[212:215], v[116:119], v[88:91], a[212:215]\n"
"v_mfma_f32_16x16x32_bf16 a[216:219], v[120:123], v[88:91], a[216:219]\n"
"v_mfma_f32_16x16x32_bf16 a[220:223], v[124:127], v[88:91], a[220:223]\n"
"v_mfma_f32_16x16x32_bf16 a[252:255], v[124:127], v[92:95], a[252:255]\n"
"v_mfma_f32_16x16x32_bf16 a[248:251], v[120:123], v[92:95], a[248:251]\n"
"v_mfma_f32_16x16x32_bf16 a[244:247], v[116:119], v[92:95], a[244:247]\n"
"v_mfma_f32_16x16x32_bf16 a[240:243], v[112:115], v[92:95], a[240:243]\n"
"v_mfma_f32_16x16x32_bf16 a[236:239], v[108:111], v[92:95], a[236:239]\n"
"v_mfma_f32_16x16x32_bf16 a[232:235], v[104:107], v[92:95], a[232:235]\n"
"v_mfma_f32_16x16x32_bf16 a[228:231], v[100:103], v[92:95], a[228:231]\n"
"s_waitcnt vmcnt(16) lgkmcnt(0)\n"
"s_barrier\n"
"v_mfma_f32_16x16x32_bf16 a[224:227], v[96:99], v[92:95], a[224:227]\n"
"v_mfma_f32_16x16x32_bf16 a[0:3], v[32:35], v[0:3], a[0:3]\n"
"v_add_u32 v254, s40, v249\n"
"v_mfma_f32_16x16x32_bf16 a[4:7], v[36:39], v[0:3], a[4:7]\n"
"ds_read_b64_tr_b16 v[96:97], v250 offset:49152\n"
"v_mfma_f32_16x16x32_bf16 a[8:11], v[40:43], v[0:3], a[8:11]\n"
"s_add_u32 m0, s42, 32768\n"
"ds_read_b64_tr_b16 v[98:99], v250 offset:50176\n"
"v_mfma_f32_16x16x32_bf16 a[12:15], v[44:47], v[0:3], a[12:15]\n"
"buffer_load_dwordx4 v240, s[24:27], s38 offen offset:0 lds\n"
"v_mfma_f32_16x16x32_bf16 a[16:19], v[48:51], v[0:3], a[16:19]\n"
"ds_read_b64_tr_b16 v[100:101], v251 offset:49152\n"
"v_mfma_f32_16x16x32_bf16 a[20:23], v[52:55], v[0:3], a[20:23]\n"
"ds_read_b64_tr_b16 v[102:103], v251 offset:50176\n"
"v_mfma_f32_16x16x32_bf16 a[24:27], v[56:59], v[0:3], a[24:27]\n"
"v_mfma_f32_16x16x32_bf16 a[28:31], v[60:63], v[0:3], a[28:31]\n"
"ds_read_b64_tr_b16 v[104:105], v250 offset:49408\n"
"v_mfma_f32_16x16x32_bf16 a[60:63], v[60:63], v[4:7], a[60:63]\n"
"ds_read_b64_tr_b16 v[106:107], v250 offset:50432\n"
"v_mfma_f32_16x16x32_bf16 a[56:59], v[56:59], v[4:7], a[56:59]\n"
"buffer_load_dwordx4 v241, s[24:27], s38 offen offset:1024 lds\n"
"v_mfma_f32_16x16x32_bf16 a[52:55], v[52:55], v[4:7], a[52:55]\n"
"ds_read_b64_tr_b16 v[108:109], v251 offset:49408\n"
"v_mfma_f32_16x16x32_bf16 a[48:51], v[48:51], v[4:7], a[48:51]\n"
"ds_read_b64_tr_b16 v[110:111], v251 offset:50432\n"
"v_mfma_f32_16x16x32_bf16 a[44:47], v[44:47], v[4:7], a[44:47]\n"
"v_mfma_f32_16x16x32_bf16 a[40:43], v[40:43], v[4:7], a[40:43]\n"
"ds_read_b128 v[64:67], v254 offset:0\n"
"v_mfma_f32_16x16x32_bf16 a[36:39], v[36:39], v[4:7], a[36:39]\n"
"ds_read_b128 v[68:71], v254 offset:2048\n"
"v_mfma_f32_16x16x32_bf16 a[32:35], v[32:35], v[4:7], a[32:35]\n"
"buffer_load_dwordx4 v242, s[24:27], s38 offen offset:2048 lds\n"
"v_mfma_f32_16x16x32_bf16 a[64:67], v[32:35], v[8:11], a[64:67]\n"
"ds_read_b128 v[72:75], v254 offset:4096\n"
"v_mfma_f32_16x16x32_bf16 a[68:71], v[36:39], v[8:11], a[68:71]\n"
"ds_read_b128 v[76:79], v254 offset:6144\n"
"v_mfma_f32_16x16x32_bf16 a[72:75], v[40:43], v[8:11], a[72:75]\n"
"v_mfma_f32_16x16x32_bf16 a[76:79], v[44:47], v[8:11], a[76:79]\n"
"ds_read_b64_tr_b16 v[112:113], v250 offset:49664\n"
"v_mfma_f32_16x16x32_bf16 a[80:83], v[48:51], v[8:11], a[80:83]\n"
"ds_read_b64_tr_b16 v[114:115], v250 offset:50688\n"
"v_mfma_f32_16x16x32_bf16 a[84:87], v[52:55], v[8:11], a[84:87]\n"
"buffer_load_dwordx4 v243, s[24:27], s38 offen offset:3072 lds\n"
"v_mfma_f32_16x16x32_bf16 a[88:91], v[56:59], v[8:11], a[88:91]\n"
"ds_read_b64_tr_b16 v[116:117], v251 offset:49664\n"
"v_mfma_f32_16x16x32_bf16 a[92:95], v[60:63], v[8:11], a[92:95]\n"
"ds_read_b64_tr_b16 v[118:119], v251 offset:50688\n"
"v_mfma_f32_16x16x32_bf16 a[124:127], v[60:63], v[12:15], a[124:127]\n"
"v_mfma_f32_16x16x32_bf16 a[120:123], v[56:59], v[12:15], a[120:123]\n"
"ds_read_b64_tr_b16 v[120:121], v250 offset:49920\n"
"v_mfma_f32_16x16x32_bf16 a[116:119], v[52:55], v[12:15], a[116:119]\n"
"s_add_u32 m0, s41, s39\n"
"ds_read_b64_tr_b16 v[122:123], v250 offset:50944\n"
"v_mfma_f32_16x16x32_bf16 a[112:115], v[48:51], v[12:15], a[112:115]\n"
"buffer_load_dwordx4 v236, s[20:23], s36 offen offset:0 lds\n"
"v_mfma_f32_16x16x32_bf16 a[108:111], v[44:47], v[12:15], a[108:111]\n"
"ds_read_b64_tr_b16 v[124:125], v251 offset:49920\n"
"v_mfma_f32_16x16x32_bf16 a[104:107], v[40:43], v[12:15], a[104:107]\n"
"ds_read_b64_tr_b16 v[126:127], v251 offset:50944\n"
"v_mfma_f32_16x16x32_bf16 a[100:103], v[36:39], v[12:15], a[100:103]\n"
"v_mfma_f32_16x16x32_bf16 a[96:99], v[32:35], v[12:15], a[96:99]\n"
"ds_read_b128 v[80:83], v254 offset:8192\n"
"v_mfma_f32_16x16x32_bf16 a[128:131], v[32:35], v[16:19], a[128:131]\n"
"ds_read_b128 v[84:87], v254 offset:10240\n"
"v_mfma_f32_16x16x32_bf16 a[132:135], v[36:39], v[16:19], a[132:135]\n"
"buffer_load_dwordx4 v237, s[20:23], s36 offen offset:1024 lds\n"
"v_mfma_f32_16x16x32_bf16 a[136:139], v[40:43], v[16:19], a[136:139]\n"
"ds_read_b128 v[88:91], v254 offset:12288\n"
"v_mfma_f32_16x16x32_bf16 a[140:143], v[44:47], v[16:19], a[140:143]\n"
"ds_read_b128 v[92:95], v254 offset:14336\n"
"v_mfma_f32_16x16x32_bf16 a[144:147], v[48:51], v[16:19], a[144:147]\n"
"v_mfma_f32_16x16x32_bf16 a[148:151], v[52:55], v[16:19], a[148:151]\n"
"v_mfma_f32_16x16x32_bf16 a[152:155], v[56:59], v[16:19], a[152:155]\n"
"v_mfma_f32_16x16x32_bf16 a[156:159], v[60:63], v[16:19], a[156:159]\n"
"buffer_load_dwordx4 v238, s[20:23], s36 offen offset:2048 lds\n"
"v_mfma_f32_16x16x32_bf16 a[188:191], v[60:63], v[20:23], a[188:191]\n"
"v_mfma_f32_16x16x32_bf16 a[184:187], v[56:59], v[20:23], a[184:187]\n"
"v_mfma_f32_16x16x32_bf16 a[180:183], v[52:55], v[20:23], a[180:183]\n"
"v_mfma_f32_16x16x32_bf16 a[176:179], v[48:51], v[20:23], a[176:179]\n"
"v_mfma_f32_16x16x32_bf16 a[172:175], v[44:47], v[20:23], a[172:175]\n"
"v_mfma_f32_16x16x32_bf16 a[168:171], v[40:43], v[20:23], a[168:171]\n"
"buffer_load_dwordx4 v239, s[20:23], s36 offen offset:3072 lds\n"
"v_mfma_f32_16x16x32_bf16 a[164:167], v[36:39], v[20:23], a[164:167]\n"
"v_mfma_f32_16x16x32_bf16 a[160:163], v[32:35], v[20:23], a[160:163]\n"
"v_mfma_f32_16x16x32_bf16 a[192:195], v[32:35], v[24:27], a[192:195]\n"
"s_add_u32 s65, s65, 262144\n"
"s_xor_b32 s38, s65, s67\n"
"v_mfma_f32_16x16x32_bf16 a[196:199], v[36:39], v[24:27], a[196:199]\n"
"s_add_u32 s39, s39, 16384\n"
"v_mfma_f32_16x16x32_bf16 a[200:203], v[40:43], v[24:27], a[200:203]\n"
"s_cmp_eq_u32 s39, 98304\n"
"s_cselect_b32 s39, 0, s39\n"
"v_mfma_f32_16x16x32_bf16 a[204:207], v[44:47], v[24:27], a[204:207]\n"
"s_add_u32 s64, s64, 128\n"
"s_xor_b32 s36, s64, s37\n"
"v_mfma_f32_16x16x32_bf16 a[208:211], v[48:51], v[24:27], a[208:211]\n"
"s_add_u32 s40, s40, 32768\n"
"v_mfma_f32_16x16x32_bf16 a[212:215], v[52:55], v[24:27], a[212:215]\n"
"s_cmp_eq_u32 s40, 98304\n"
"s_cselect_b32 s40, 0, s40\n"
"v_mfma_f32_16x16x32_bf16 a[216:219], v[56:59], v[24:27], a[216:219]\n"
"v_mfma_f32_16x16x32_bf16 a[220:223], v[60:63], v[24:27], a[220:223]\n"
"v_mfma_f32_16x16x32_bf16 a[252:255], v[60:63], v[28:31], a[252:255]\n"
"v_mfma_f32_16x16x32_bf16 a[248:251], v[56:59], v[28:31], a[248:251]\n"
"v_mfma_f32_16x16x32_bf16 a[244:247], v[52:55], v[28:31], a[244:247]\n"
"v_mfma_f32_16x16x32_bf16 a[240:243], v[48:51], v[28:31], a[240:243]\n"
"v_mfma_f32_16x16x32_bf16 a[236:239], v[44:47], v[28:31], a[236:239]\n"
"v_mfma_f32_16x16x32_bf16 a[232:235], v[40:43], v[28:31], a[232:235]\n"
"v_mfma_f32_16x16x32_bf16 a[228:231], v[36:39], v[28:31], a[228:231]\n"
"s_waitcnt vmcnt(16) lgkmcnt(0)\n"
"s_barrier\n"
"v_mfma_f32_16x16x32_bf16 a[224:227], v[32:35], v[28:31], a[224:227]\n"
"v_mfma_f32_16x16x32_bf16 a[0:3], v[96:99], v[64:67], a[0:3]\n"
"v_add_u32 v254, s40, v248\n"
"v_mfma_f32_16x16x32_bf16 a[4:7], v[100:103], v[64:67], a[4:7]\n"
"ds_read_b64_tr_b16 v[32:33], v250 offset:0\n"
"v_mfma_f32_16x16x32_bf16 a[8:11], v[104:107], v[64:67], a[8:11]\n"
"s_add_u32 m0, s42, 49152\n"
"ds_read_b64_tr_b16 v[34:35], v250 offset:1024\n"
"v_mfma_f32_16x16x32_bf16 a[12:15], v[108:111], v[64:67], a[12:15]\n"
"buffer_load_dwordx4 v240, s[24:27], s38 offen offset:0 lds\n"
"v_mfma_f32_16x16x32_bf16 a[16:19], v[112:115], v[64:67], a[16:19]\n"
"ds_read_b64_tr_b16 v[36:37], v251 offset:0\n"
"v_mfma_f32_16x16x32_bf16 a[20:23], v[116:119], v[64:67], a[20:23]\n"
"ds_read_b64_tr_b16 v[38:39], v251 offset:1024\n"
"v_mfma_f32_16x16x32_bf16 a[24:27], v[120:123], v[64:67], a[24:27]\n"
"v_mfma_f32_16x16x32_bf16 a[28:31], v[124:127], v[64:67], a[28:31]\n"
"ds_read_b64_tr_b16 v[40:41], v250 offset:256\n"
"v_mfma_f32_16x16x32_bf16 a[60:63], v[124:127], v[68:71], a[60:63]\n"
"ds_read_b64_tr_b16 v[42:43], v250 offset:1280\n"
"v_mfma_f32_16x16x32_bf16 a[56:59], v[120:123], v[68:71], a[56:59]\n"
"buffer_load_dwordx4 v241, s[24:27], s38 offen offset:1024 lds\n"
"v_mfma_f32_16x16x32_bf16 a[52:55], v[116:119], v[68:71], a[52:55]\n"
"ds_read_b64_tr_b16 v[44:45], v251 offset:256\n"
"v_mfma_f32_16x16x32_bf16 a[48:51], v[112:115], v[68:71], a[48:51]\n"
"ds_read_b64_tr_b16 v[46:47], v251 offset:1280\n"
"v_mfma_f32_16x16x32_bf16 a[44:47], v[108:111], v[68:71], a[44:47]\n"
"v_mfma_f32_16x16x32_bf16 a[40:43], v[104:107], v[68:71], a[40:43]\n"
"ds_read_b128 v[0:3], v254 offset:0\n"
"v_mfma_f32_16x16x32_bf16 a[36:39], v[100:103], v[68:71], a[36:39]\n"
"ds_read_b128 v[4:7], v254 offset:2048\n"
"v_mfma_f32_16x16x32_bf16 a[32:35], v[96:99], v[68:71], a[32:35]\n"
"buffer_load_dwordx4 v242, s[24:27], s38 offen offset:2048 lds\n"
"v_mfma_f32_16x16x32_bf16 a[64:67], v[96:99], v[72:75], a[64:67]\n"
"ds_read_b128 v[8:11], v254 offset:4096\n"
"v_mfma_f32_16x16x32_bf16 a[68:71], v[100:103], v[72:75], a[68:71]\n"
"ds_read_b128 v[12:15], v254 offset:6144\n"
"v_mfma_f32_16x16x32_bf16 a[72:75], v[104:107], v[72:75], a[72:75]\n"
"v_mfma_f32_16x16x32_bf16 a[76:79], v[108:111], v[72:75], a[76:79]\n"
"ds_read_b64_tr_b16 v[48:49], v250 offset:512\n"
"v_mfma_f32_16x16x32_bf16 a[80:83], v[112:115], v[72:75], a[80:83]\n"
"ds_read_b64_tr_b16 v[50:51], v250 offset:1536\n"
"v_mfma_f32_16x16x32_bf16 a[84:87], v[116:119], v[72:75], a[84:87]\n"
"buffer_load_dwordx4 v243, s[24:27], s38 offen offset:3072 lds\n"
"v_mfma_f32_16x16x32_bf16 a[88:91], v[120:123], v[72:75], a[88:91]\n"
"ds_read_b64_tr_b16 v[52:53], v251 offset:512\n"
"v_mfma_f32_16x16x32_bf16 a[92:95], v[124:127], v[72:75], a[92:95]\n"
"ds_read_b64_tr_b16 v[54:55], v251 offset:1536\n"
"v_mfma_f32_16x16x32_bf16 a[124:127], v[124:127], v[76:79], a[124:127]\n"
"v_mfma_f32_16x16x32_bf16 a[120:123], v[120:123], v[76:79], a[120:123]\n"
"ds_read_b64_tr_b16 v[56:57], v250 offset:768\n"
"v_mfma_f32_16x16x32_bf16 a[116:119], v[116:119], v[76:79], a[116:119]\n"
"s_add_u32 m0, s41, s39\n"
"ds_read_b64_tr_b16 v[58:59], v250 offset:1792\n"
"v_mfma_f32_16x16x32_bf16 a[112:115], v[112:115], v[76:79], a[112:115]\n"
"buffer_load_dwordx4 v232, s[20:23], s36 offen offset:0 lds\n"
"v_mfma_f32_16x16x32_bf16 a[108:111], v[108:111], v[76:79], a[108:111]\n"
"ds_read_b64_tr_b16 v[60:61], v251 offset:768\n"
"v_mfma_f32_16x16x32_bf16 a[104:107], v[104:107], v[76:79], a[104:107]\n"
"ds_read_b64_tr_b16 v[62:63], v251 offset:1792\n"
"v_mfma_f32_16x16x32_bf16 a[100:103], v[100:103], v[76:79], a[100:103]\n"
"v_mfma_f32_16x16x32_bf16 a[96:99], v[96:99], v[76:79], a[96:99]\n"
"ds_read_b128 v[16:19], v254 offset:8192\n"
"v_mfma_f32_16x16x32_bf16 a[128:131], v[96:99], v[80:83], a[128:131]\n"
"ds_read_b128 v[20:23], v254 offset:10240\n"
"v_mfma_f32_16x16x32_bf16 a[132:135], v[100:103], v[80:83], a[132:135]\n"
"buffer_load_dwordx4 v233, s[20:23], s36 offen offset:1024 lds\n"
"v_mfma_f32_16x16x32_bf16 a[136:139], v[104:107], v[80:83], a[136:139]\n"
"ds_read_b128 v[24:27], v254 offset:12288\n"
"v_mfma_f32_16x16x32_bf16 a[140:143], v[108:111], v[80:83], a[140:143]\n"
"ds_read_b128 v[28:31], v254 offset:14336\n"
"v_mfma_f32_16x16x32_bf16 a[144:147], v[112:115], v[80:83], a[144:147]\n"
"v_mfma_f32_16x16x32_bf16 a[148:151], v[116:119], v[80:83], a[148:151]\n"
"v_mfma_f32_16x16x32_bf16 a[152:155], v[120:123], v[80:83], a[152:155]\n"
"v_mfma_f32_16x16x32_bf16 a[156:159], v[124:127], v[80:83], a[156:159]\n"
"buffer_load_dwordx4 v234, s[20:23], s36 offen offset:2048 lds\n"
"v_mfma_f32_16x16x32_bf16 a[188:191], v[124:127], v[84:87], a[188:191]\n"
"v_mfma_f32_16x16x32_bf16 a[184:187], v[120:123], v[84:87], a[184:187]\n"
"v_mfma_f32_16x16x32_bf16 a[180:183], v[116:119], v[84:87], a[180:183]\n"
"v_mfma_f32_16x16x32_bf16 a[176:179], v[112:115], v[84:87], a[176:179]\n"
"v_mfma_f32_16x16x32_bf16 a[172:175], v[108:111], v[84:87], a[172:175]\n"
"v_mfma_f32_16x16x32_bf16 a[168:171], v[104:107], v[84:87], a[168:171]\n"
"buffer_load_dwordx4 v235, s[20:23], s36 offen offset:3072 lds\n"
"v_mfma_f32_16x16x32_bf16 a[164:167], v[100:103], v[84:87], a[164:167]\n"
"v_mfma_f32_16x16x32_bf16 a[160:163], v[96:99], v[84:87], a[160:163]\n"
"v_mfma_f32_16x16x32_bf16 a[192:195], v[96:99], v[88:91], a[192:195]\n"
"s_add_u32 s65, s65, 262144\n"
"s_xor_b32 s38, s65, s67\n"
"v_mfma_f32_16x16x32_bf16 a[196:199], v[100:103], v[88:91], a[196:199]\n"
"s_add_u32 s39, s39, 16384\n"
"v_mfma_f32_16x16x32_bf16 a[200:203], v[104:107], v[88:91], a[200:203]\n"
"s_cmp_eq_u32 s39, 98304\n"
"s_cselect_b32 s39, 0, s39\n"
"v_mfma_f32_16x16x32_bf16 a[204:207], v[108:111], v[88:91], a[204:207]\n"
"v_mfma_f32_16x16x32_bf16 a[208:211], v[112:115], v[88:91], a[208:211]\n"
"v_mfma_f32_16x16x32_bf16 a[212:215], v[116:119], v[88:91], a[212:215]\n"
"v_mfma_f32_16x16x32_bf16 a[216:219], v[120:123], v[88:91], a[216:219]\n"
"v_mfma_f32_16x16x32_bf16 a[220:223], v[124:127], v[88:91], a[220:223]\n"
"v_mfma_f32_16x16x32_bf16 a[252:255], v[124:127], v[92:95], a[252:255]\n"
"v_mfma_f32_16x16x32_bf16 a[248:251], v[120:123], v[92:95], a[248:251]\n"
"v_mfma_f32_16x16x32_bf16 a[244:247], v[116:119], v[92:95], a[244:247]\n"
"v_mfma_f32_16x16x32_bf16 a[240:243], v[112:115], v[92:95], a[240:243]\n"
"v_mfma_f32_16x16x32_bf16 a[236:239], v[108:111], v[92:95], a[236:239]\n"
"v_mfma_f32_16x16x32_bf16 a[232:235], v[104:107], v[92:95], a[232:235]\n"
"v_mfma_f32_16x16x32_bf16 a[228:231], v[100:103], v[92:95], a[228:231]\n"
"s_waitcnt vmcnt(16) lgkmcnt(0)\n"
"s_barrier\n"
"v_mfma_f32_16x16x32_bf16 a[224:227], v[96:99], v[92:95], a[224:227]\n"
"s_sub_u32 s43, s43, 1\n"
"s_cbranch_scc0 L_KV_K_loop\n"
"s_branch L_KV_stub\n"
"L_KV_V_loop:\n"
"v_mfma_f32_16x16x32_bf16 a[0:3], v[0:3], v[32:35], a[0:3]\n"
"v_add_u32 v254, s40, v249\n"
"v_mfma_f32_16x16x32_bf16 a[8:11], v[0:3], v[36:39], a[8:11]\n"
"ds_read_b64_tr_b16 v[96:97], v250 offset:16384\n"
"v_mfma_f32_16x16x32_bf16 a[16:19], v[0:3], v[40:43], a[16:19]\n"
"s_add_u32 m0, s42, 0\n"
"ds_read_b64_tr_b16 v[98:99], v250 offset:17408\n"
"v_mfma_f32_16x16x32_bf16 a[24:27], v[0:3], v[44:47], a[24:27]\n"
"buffer_load_dwordx4 v240, s[24:27], s38 offen offset:0 lds\n"
"v_mfma_f32_16x16x32_bf16 a[32:35], v[0:3], v[48:51], a[32:35]\n"
"ds_read_b64_tr_b16 v[100:101], v251 offset:16384\n"
"v_mfma_f32_16x16x32_bf16 a[40:43], v[0:3], v[52:55], a[40:43]\n"
"ds_read_b64_tr_b16 v[102:103], v251 offset:17408\n"
"v_mfma_f32_16x16x32_bf16 a[48:51], v[0:3], v[56:59], a[48:51]\n"
"v_mfma_f32_16x16x32_bf16 a[56:59], v[0:3], v[60:63], a[56:59]\n"
"ds_read_b64_tr_b16 v[104:105], v250 offset:16640\n"
"v_mfma_f32_16x16x32_bf16 a[60:63], v[4:7], v[60:63], a[60:63]\n"
"ds_read_b64_tr_b16 v[106:107], v250 offset:17664\n"
"v_mfma_f32_16x16x32_bf16 a[52:55], v[4:7], v[56:59], a[52:55]\n"
"buffer_load_dwordx4 v241, s[24:27], s38 offen offset:1024 lds\n"
"v_mfma_f32_16x16x32_bf16 a[44:47], v[4:7], v[52:55], a[44:47]\n"
"ds_read_b64_tr_b16 v[108:109], v251 offset:16640\n"
"v_mfma_f32_16x16x32_bf16 a[36:39], v[4:7], v[48:51], a[36:39]\n"
"ds_read_b64_tr_b16 v[110:111], v251 offset:17664\n"
"v_mfma_f32_16x16x32_bf16 a[28:31], v[4:7], v[44:47], a[28:31]\n"
"v_mfma_f32_16x16x32_bf16 a[20:23], v[4:7], v[40:43], a[20:23]\n"
"ds_read_b128 v[64:67], v254 offset:0\n"
"v_mfma_f32_16x16x32_bf16 a[12:15], v[4:7], v[36:39], a[12:15]\n"
"ds_read_b128 v[68:71], v254 offset:2048\n"
"v_mfma_f32_16x16x32_bf16 a[4:7], v[4:7], v[32:35], a[4:7]\n"
"buffer_load_dwordx4 v242, s[24:27], s38 offen offset:2048 lds\n"
"v_mfma_f32_16x16x32_bf16 a[64:67], v[8:11], v[32:35], a[64:67]\n"
"ds_read_b128 v[72:75], v254 offset:4096\n"
"v_mfma_f32_16x16x32_bf16 a[72:75], v[8:11], v[36:39], a[72:75]\n"
"ds_read_b128 v[76:79], v254 offset:6144\n"
"v_mfma_f32_16x16x32_bf16 a[80:83], v[8:11], v[40:43], a[80:83]\n"
"v_mfma_f32_16x16x32_bf16 a[88:91], v[8:11], v[44:47], a[88:91]\n"
"ds_read_b64_tr_b16 v[112:113], v250 offset:16896\n"
"v_mfma_f32_16x16x32_bf16 a[96:99], v[8:11], v[48:51], a[96:99]\n"
"ds_read_b64_tr_b16 v[114:115], v250 offset:17920\n"
"v_mfma_f32_16x16x32_bf16 a[104:107], v[8:11], v[52:55], a[104:107]\n"
"buffer_load_dwordx4 v243, s[24:27], s38 offen offset:3072 lds\n"
"v_mfma_f32_16x16x32_bf16 a[112:115], v[8:11], v[56:59], a[112:115]\n"
"ds_read_b64_tr_b16 v[116:117], v251 offset:16896\n"
"v_mfma_f32_16x16x32_bf16 a[120:123], v[8:11], v[60:63], a[120:123]\n"
"ds_read_b64_tr_b16 v[118:119], v251 offset:17920\n"
"v_mfma_f32_16x16x32_bf16 a[124:127], v[12:15], v[60:63], a[124:127]\n"
"buffer_load_dword v227, v226, s[60:63], s66 offen offset:0\n"
"v_mfma_f32_16x16x32_bf16 a[116:119], v[12:15], v[56:59], a[116:119]\n"
"ds_read_b64_tr_b16 v[120:121], v250 offset:17152\n"
"v_mfma_f32_16x16x32_bf16 a[108:111], v[12:15], v[52:55], a[108:111]\n"
"s_add_u32 m0, s41, s39\n"
"ds_read_b64_tr_b16 v[122:123], v250 offset:18176\n"
"v_mfma_f32_16x16x32_bf16 a[100:103], v[12:15], v[48:51], a[100:103]\n"
"buffer_load_dwordx4 v236, s[20:23], s36 offen offset:0 lds\n"
"v_mfma_f32_16x16x32_bf16 a[92:95], v[12:15], v[44:47], a[92:95]\n"
"ds_read_b64_tr_b16 v[124:125], v251 offset:17152\n"
"v_mfma_f32_16x16x32_bf16 a[84:87], v[12:15], v[40:43], a[84:87]\n"
"ds_read_b64_tr_b16 v[126:127], v251 offset:18176\n"
"v_mfma_f32_16x16x32_bf16 a[76:79], v[12:15], v[36:39], a[76:79]\n"
"v_mfma_f32_16x16x32_bf16 a[68:71], v[12:15], v[32:35], a[68:71]\n"
"ds_read_b128 v[80:83], v254 offset:8192\n"
"v_mfma_f32_16x16x32_bf16 a[128:131], v[16:19], v[32:35], a[128:131]\n"
"ds_read_b128 v[84:87], v254 offset:10240\n"
"v_mfma_f32_16x16x32_bf16 a[136:139], v[16:19], v[36:39], a[136:139]\n"
"buffer_load_dwordx4 v237, s[20:23], s36 offen offset:1024 lds\n"
"v_mfma_f32_16x16x32_bf16 a[144:147], v[16:19], v[40:43], a[144:147]\n"
"ds_read_b128 v[88:91], v254 offset:12288\n"
"v_mfma_f32_16x16x32_bf16 a[152:155], v[16:19], v[44:47], a[152:155]\n"
"ds_read_b128 v[92:95], v254 offset:14336\n"
"v_mfma_f32_16x16x32_bf16 a[160:163], v[16:19], v[48:51], a[160:163]\n"
"v_mfma_f32_16x16x32_bf16 a[168:171], v[16:19], v[52:55], a[168:171]\n"
"v_mfma_f32_16x16x32_bf16 a[176:179], v[16:19], v[56:59], a[176:179]\n"
"v_mfma_f32_16x16x32_bf16 a[184:187], v[16:19], v[60:63], a[184:187]\n"
"buffer_load_dwordx4 v238, s[20:23], s36 offen offset:2048 lds\n"
"v_mfma_f32_16x16x32_bf16 a[188:191], v[20:23], v[60:63], a[188:191]\n"
"v_mfma_f32_16x16x32_bf16 a[180:183], v[20:23], v[56:59], a[180:183]\n"
"v_mfma_f32_16x16x32_bf16 a[172:175], v[20:23], v[52:55], a[172:175]\n"
"v_mfma_f32_16x16x32_bf16 a[164:167], v[20:23], v[48:51], a[164:167]\n"
"v_mfma_f32_16x16x32_bf16 a[156:159], v[20:23], v[44:47], a[156:159]\n"
"v_mfma_f32_16x16x32_bf16 a[148:151], v[20:23], v[40:43], a[148:151]\n"
"buffer_load_dwordx4 v239, s[20:23], s36 offen offset:3072 lds\n"
"v_mfma_f32_16x16x32_bf16 a[140:143], v[20:23], v[36:39], a[140:143]\n"
"s_add_u32 s66, s66, 2048\n"
"v_mfma_f32_16x16x32_bf16 a[132:135], v[20:23], v[32:35], a[132:135]\n"
"v_mfma_f32_16x16x32_bf16 a[192:195], v[24:27], v[32:35], a[192:195]\n"
"s_add_u32 s65, s65, 262144\n"
"s_xor_b32 s38, s65, s67\n"
"v_mfma_f32_16x16x32_bf16 a[200:203], v[24:27], v[36:39], a[200:203]\n"
"s_add_u32 s39, s39, 16384\n"
"v_mfma_f32_16x16x32_bf16 a[208:211], v[24:27], v[40:43], a[208:211]\n"
"s_cmp_eq_u32 s39, 98304\n"
"s_cselect_b32 s39, 0, s39\n"
"v_mfma_f32_16x16x32_bf16 a[216:219], v[24:27], v[44:47], a[216:219]\n"
"s_add_u32 s64, s64, 128\n"
"s_xor_b32 s36, s64, s37\n"
"v_mfma_f32_16x16x32_bf16 a[224:227], v[24:27], v[48:51], a[224:227]\n"
"s_add_u32 s40, s40, 32768\n"
"v_mfma_f32_16x16x32_bf16 a[232:235], v[24:27], v[52:55], a[232:235]\n"
"s_cmp_eq_u32 s40, 98304\n"
"s_cselect_b32 s40, 0, s40\n"
"v_mfma_f32_16x16x32_bf16 a[240:243], v[24:27], v[56:59], a[240:243]\n"
"v_mfma_f32_16x16x32_bf16 a[248:251], v[24:27], v[60:63], a[248:251]\n"
"v_mfma_f32_16x16x32_bf16 a[252:255], v[28:31], v[60:63], a[252:255]\n"
"v_mfma_f32_16x16x32_bf16 a[244:247], v[28:31], v[56:59], a[244:247]\n"
"v_mfma_f32_16x16x32_bf16 a[236:239], v[28:31], v[52:55], a[236:239]\n"
"v_mfma_f32_16x16x32_bf16 a[228:231], v[28:31], v[48:51], a[228:231]\n"
"v_mfma_f32_16x16x32_bf16 a[220:223], v[28:31], v[44:47], a[220:223]\n"
"v_mfma_f32_16x16x32_bf16 a[212:215], v[28:31], v[40:43], a[212:215]\n"
"v_mfma_f32_16x16x32_bf16 a[204:207], v[28:31], v[36:39], a[204:207]\n"
"s_waitcnt vmcnt(16) lgkmcnt(0)\n"
"s_barrier\n"
"v_mfma_f32_16x16x32_bf16 a[196:199], v[28:31], v[32:35], a[196:199]\n"
"v_mfma_f32_16x16x32_bf16 a[0:3], v[64:67], v[96:99], a[0:3]\n"
"v_add_u32 v254, s40, v248\n"
"v_mfma_f32_16x16x32_bf16 a[8:11], v[64:67], v[100:103], a[8:11]\n"
"ds_read_b64_tr_b16 v[32:33], v250 offset:32768\n"
"v_mfma_f32_16x16x32_bf16 a[16:19], v[64:67], v[104:107], a[16:19]\n"
"s_add_u32 m0, s42, 16384\n"
"ds_read_b64_tr_b16 v[34:35], v250 offset:33792\n"
"v_mfma_f32_16x16x32_bf16 a[24:27], v[64:67], v[108:111], a[24:27]\n"
"buffer_load_dwordx4 v240, s[24:27], s38 offen offset:0 lds\n"
"v_mfma_f32_16x16x32_bf16 a[32:35], v[64:67], v[112:115], a[32:35]\n"
"ds_read_b64_tr_b16 v[36:37], v251 offset:32768\n"
"v_mfma_f32_16x16x32_bf16 a[40:43], v[64:67], v[116:119], a[40:43]\n"
"ds_read_b64_tr_b16 v[38:39], v251 offset:33792\n"
"v_mfma_f32_16x16x32_bf16 a[48:51], v[64:67], v[120:123], a[48:51]\n"
"v_mfma_f32_16x16x32_bf16 a[56:59], v[64:67], v[124:127], a[56:59]\n"
"ds_read_b64_tr_b16 v[40:41], v250 offset:33024\n"
"v_mfma_f32_16x16x32_bf16 a[60:63], v[68:71], v[124:127], a[60:63]\n"
"ds_read_b64_tr_b16 v[42:43], v250 offset:34048\n"
"v_mfma_f32_16x16x32_bf16 a[52:55], v[68:71], v[120:123], a[52:55]\n"
"buffer_load_dwordx4 v241, s[24:27], s38 offen offset:1024 lds\n"
"v_mfma_f32_16x16x32_bf16 a[44:47], v[68:71], v[116:119], a[44:47]\n"
"ds_read_b64_tr_b16 v[44:45], v251 offset:33024\n"
"v_mfma_f32_16x16x32_bf16 a[36:39], v[68:71], v[112:115], a[36:39]\n"
"ds_read_b64_tr_b16 v[46:47], v251 offset:34048\n"
"v_mfma_f32_16x16x32_bf16 a[28:31], v[68:71], v[108:111], a[28:31]\n"
"v_mfma_f32_16x16x32_bf16 a[20:23], v[68:71], v[104:107], a[20:23]\n"
"ds_read_b128 v[0:3], v254 offset:0\n"
"v_mfma_f32_16x16x32_bf16 a[12:15], v[68:71], v[100:103], a[12:15]\n"
"ds_read_b128 v[4:7], v254 offset:2048\n"
"v_mfma_f32_16x16x32_bf16 a[4:7], v[68:71], v[96:99], a[4:7]\n"
"buffer_load_dwordx4 v242, s[24:27], s38 offen offset:2048 lds\n"
"v_mfma_f32_16x16x32_bf16 a[64:67], v[72:75], v[96:99], a[64:67]\n"
"ds_read_b128 v[8:11], v254 offset:4096\n"
"v_mfma_f32_16x16x32_bf16 a[72:75], v[72:75], v[100:103], a[72:75]\n"
"ds_read_b128 v[12:15], v254 offset:6144\n"
"v_mfma_f32_16x16x32_bf16 a[80:83], v[72:75], v[104:107], a[80:83]\n"
"v_mfma_f32_16x16x32_bf16 a[88:91], v[72:75], v[108:111], a[88:91]\n"
"ds_read_b64_tr_b16 v[48:49], v250 offset:33280\n"
"v_mfma_f32_16x16x32_bf16 a[96:99], v[72:75], v[112:115], a[96:99]\n"
"ds_read_b64_tr_b16 v[50:51], v250 offset:34304\n"
"v_mfma_f32_16x16x32_bf16 a[104:107], v[72:75], v[116:119], a[104:107]\n"
"buffer_load_dwordx4 v243, s[24:27], s38 offen offset:3072 lds\n"
"v_mfma_f32_16x16x32_bf16 a[112:115], v[72:75], v[120:123], a[112:115]\n"
"ds_read_b64_tr_b16 v[52:53], v251 offset:33280\n"
"v_mfma_f32_16x16x32_bf16 a[120:123], v[72:75], v[124:127], a[120:123]\n"
"ds_read_b64_tr_b16 v[54:55], v251 offset:34304\n"
"v_mfma_f32_16x16x32_bf16 a[124:127], v[76:79], v[124:127], a[124:127]\n"
"v_mfma_f32_16x16x32_bf16 a[116:119], v[76:79], v[120:123], a[116:119]\n"
"ds_read_b64_tr_b16 v[56:57], v250 offset:33536\n"
"v_mfma_f32_16x16x32_bf16 a[108:111], v[76:79], v[116:119], a[108:111]\n"
"s_add_u32 m0, s41, s39\n"
"ds_read_b64_tr_b16 v[58:59], v250 offset:34560\n"
"v_mfma_f32_16x16x32_bf16 a[100:103], v[76:79], v[112:115], a[100:103]\n"
"buffer_load_dwordx4 v232, s[20:23], s36 offen offset:0 lds\n"
"v_mfma_f32_16x16x32_bf16 a[92:95], v[76:79], v[108:111], a[92:95]\n"
"ds_read_b64_tr_b16 v[60:61], v251 offset:33536\n"
"v_mfma_f32_16x16x32_bf16 a[84:87], v[76:79], v[104:107], a[84:87]\n"
"ds_read_b64_tr_b16 v[62:63], v251 offset:34560\n"
"v_mfma_f32_16x16x32_bf16 a[76:79], v[76:79], v[100:103], a[76:79]\n"
"v_mfma_f32_16x16x32_bf16 a[68:71], v[76:79], v[96:99], a[68:71]\n"
"ds_read_b128 v[16:19], v254 offset:8192\n"
"v_mfma_f32_16x16x32_bf16 a[128:131], v[80:83], v[96:99], a[128:131]\n"
"ds_read_b128 v[20:23], v254 offset:10240\n"
"v_mfma_f32_16x16x32_bf16 a[136:139], v[80:83], v[100:103], a[136:139]\n"
"buffer_load_dwordx4 v233, s[20:23], s36 offen offset:1024 lds\n"
"v_mfma_f32_16x16x32_bf16 a[144:147], v[80:83], v[104:107], a[144:147]\n"
"ds_read_b128 v[24:27], v254 offset:12288\n"
"v_mfma_f32_16x16x32_bf16 a[152:155], v[80:83], v[108:111], a[152:155]\n"
"ds_read_b128 v[28:31], v254 offset:14336\n"
"v_mfma_f32_16x16x32_bf16 a[160:163], v[80:83], v[112:115], a[160:163]\n"
"v_mfma_f32_16x16x32_bf16 a[168:171], v[80:83], v[116:119], a[168:171]\n"
"v_mfma_f32_16x16x32_bf16 a[176:179], v[80:83], v[120:123], a[176:179]\n"
"v_mfma_f32_16x16x32_bf16 a[184:187], v[80:83], v[124:127], a[184:187]\n"
"buffer_load_dwordx4 v234, s[20:23], s36 offen offset:2048 lds\n"
"v_mfma_f32_16x16x32_bf16 a[188:191], v[84:87], v[124:127], a[188:191]\n"
"v_mfma_f32_16x16x32_bf16 a[180:183], v[84:87], v[120:123], a[180:183]\n"
"v_mfma_f32_16x16x32_bf16 a[172:175], v[84:87], v[116:119], a[172:175]\n"
"v_mfma_f32_16x16x32_bf16 a[164:167], v[84:87], v[112:115], a[164:167]\n"
"v_mfma_f32_16x16x32_bf16 a[156:159], v[84:87], v[108:111], a[156:159]\n"
"v_mfma_f32_16x16x32_bf16 a[148:151], v[84:87], v[104:107], a[148:151]\n"
"buffer_load_dwordx4 v235, s[20:23], s36 offen offset:3072 lds\n"
"v_mfma_f32_16x16x32_bf16 a[140:143], v[84:87], v[100:103], a[140:143]\n"
"v_mfma_f32_16x16x32_bf16 a[132:135], v[84:87], v[96:99], a[132:135]\n"
"v_mfma_f32_16x16x32_bf16 a[192:195], v[88:91], v[96:99], a[192:195]\n"
"s_add_u32 s65, s65, 262144\n"
"s_xor_b32 s38, s65, s67\n"
"v_mfma_f32_16x16x32_bf16 a[200:203], v[88:91], v[100:103], a[200:203]\n"
"s_add_u32 s39, s39, 16384\n"
"v_mfma_f32_16x16x32_bf16 a[208:211], v[88:91], v[104:107], a[208:211]\n"
"s_cmp_eq_u32 s39, 98304\n"
"s_cselect_b32 s39, 0, s39\n"
"v_mfma_f32_16x16x32_bf16 a[216:219], v[88:91], v[108:111], a[216:219]\n"
"v_mfma_f32_16x16x32_bf16 a[224:227], v[88:91], v[112:115], a[224:227]\n"
"v_mfma_f32_16x16x32_bf16 a[232:235], v[88:91], v[116:119], a[232:235]\n"
"v_mfma_f32_16x16x32_bf16 a[240:243], v[88:91], v[120:123], a[240:243]\n"
"v_mfma_f32_16x16x32_bf16 a[248:251], v[88:91], v[124:127], a[248:251]\n"
"v_mfma_f32_16x16x32_bf16 a[252:255], v[92:95], v[124:127], a[252:255]\n"
"v_mfma_f32_16x16x32_bf16 a[244:247], v[92:95], v[120:123], a[244:247]\n"
"v_mfma_f32_16x16x32_bf16 a[236:239], v[92:95], v[116:119], a[236:239]\n"
"v_mfma_f32_16x16x32_bf16 a[228:231], v[92:95], v[112:115], a[228:231]\n"
"v_mfma_f32_16x16x32_bf16 a[220:223], v[92:95], v[108:111], a[220:223]\n"
"v_mfma_f32_16x16x32_bf16 a[212:215], v[92:95], v[104:107], a[212:215]\n"
"v_mfma_f32_16x16x32_bf16 a[204:207], v[92:95], v[100:103], a[204:207]\n"
"s_waitcnt vmcnt(16) lgkmcnt(0)\n"
"s_barrier\n"
"v_mfma_f32_16x16x32_bf16 a[196:199], v[92:95], v[96:99], a[196:199]\n"
"v_mfma_f32_16x16x32_bf16 a[0:3], v[0:3], v[32:35], a[0:3]\n"
"v_add_u32 v254, s40, v249\n"
"v_mfma_f32_16x16x32_bf16 a[8:11], v[0:3], v[36:39], a[8:11]\n"
"ds_read_b64_tr_b16 v[96:97], v250 offset:49152\n"
"v_mfma_f32_16x16x32_bf16 a[16:19], v[0:3], v[40:43], a[16:19]\n"
"s_add_u32 m0, s42, 32768\n"
"ds_read_b64_tr_b16 v[98:99], v250 offset:50176\n"
"v_mfma_f32_16x16x32_bf16 a[24:27], v[0:3], v[44:47], a[24:27]\n"
"buffer_load_dwordx4 v240, s[24:27], s38 offen offset:0 lds\n"
"v_mfma_f32_16x16x32_bf16 a[32:35], v[0:3], v[48:51], a[32:35]\n"
"ds_read_b64_tr_b16 v[100:101], v251 offset:49152\n"
"v_mfma_f32_16x16x32_bf16 a[40:43], v[0:3], v[52:55], a[40:43]\n"
"ds_read_b64_tr_b16 v[102:103], v251 offset:50176\n"
"v_mfma_f32_16x16x32_bf16 a[48:51], v[0:3], v[56:59], a[48:51]\n"
"v_mfma_f32_16x16x32_bf16 a[56:59], v[0:3], v[60:63], a[56:59]\n"
"ds_read_b64_tr_b16 v[104:105], v250 offset:49408\n"
"v_mfma_f32_16x16x32_bf16 a[60:63], v[4:7], v[60:63], a[60:63]\n"
"ds_read_b64_tr_b16 v[106:107], v250 offset:50432\n"
"v_mfma_f32_16x16x32_bf16 a[52:55], v[4:7], v[56:59], a[52:55]\n"
"buffer_load_dwordx4 v241, s[24:27], s38 offen offset:1024 lds\n"
"v_mfma_f32_16x16x32_bf16 a[44:47], v[4:7], v[52:55], a[44:47]\n"
"ds_read_b64_tr_b16 v[108:109], v251 offset:49408\n"
"v_mfma_f32_16x16x32_bf16 a[36:39], v[4:7], v[48:51], a[36:39]\n"
"ds_read_b64_tr_b16 v[110:111], v251 offset:50432\n"
"v_mfma_f32_16x16x32_bf16 a[28:31], v[4:7], v[44:47], a[28:31]\n"
"v_mfma_f32_16x16x32_bf16 a[20:23], v[4:7], v[40:43], a[20:23]\n"
"ds_read_b128 v[64:67], v254 offset:0\n"
"v_mfma_f32_16x16x32_bf16 a[12:15], v[4:7], v[36:39], a[12:15]\n"
"ds_read_b128 v[68:71], v254 offset:2048\n"
"v_mfma_f32_16x16x32_bf16 a[4:7], v[4:7], v[32:35], a[4:7]\n"
"buffer_load_dwordx4 v242, s[24:27], s38 offen offset:2048 lds\n"
"v_mfma_f32_16x16x32_bf16 a[64:67], v[8:11], v[32:35], a[64:67]\n"
"ds_read_b128 v[72:75], v254 offset:4096\n"
"v_mfma_f32_16x16x32_bf16 a[72:75], v[8:11], v[36:39], a[72:75]\n"
"ds_read_b128 v[76:79], v254 offset:6144\n"
"v_mfma_f32_16x16x32_bf16 a[80:83], v[8:11], v[40:43], a[80:83]\n"
"v_mfma_f32_16x16x32_bf16 a[88:91], v[8:11], v[44:47], a[88:91]\n"
"ds_read_b64_tr_b16 v[112:113], v250 offset:49664\n"
"v_mfma_f32_16x16x32_bf16 a[96:99], v[8:11], v[48:51], a[96:99]\n"
"ds_read_b64_tr_b16 v[114:115], v250 offset:50688\n"
"v_mfma_f32_16x16x32_bf16 a[104:107], v[8:11], v[52:55], a[104:107]\n"
"buffer_load_dwordx4 v243, s[24:27], s38 offen offset:3072 lds\n"
"v_mfma_f32_16x16x32_bf16 a[112:115], v[8:11], v[56:59], a[112:115]\n"
"ds_read_b64_tr_b16 v[116:117], v251 offset:49664\n"
"v_mfma_f32_16x16x32_bf16 a[120:123], v[8:11], v[60:63], a[120:123]\n"
"ds_read_b64_tr_b16 v[118:119], v251 offset:50688\n"
"v_mfma_f32_16x16x32_bf16 a[124:127], v[12:15], v[60:63], a[124:127]\n"
"v_mfma_f32_16x16x32_bf16 a[116:119], v[12:15], v[56:59], a[116:119]\n"
"ds_read_b64_tr_b16 v[120:121], v250 offset:49920\n"
"v_mfma_f32_16x16x32_bf16 a[108:111], v[12:15], v[52:55], a[108:111]\n"
"s_add_u32 m0, s41, s39\n"
"ds_read_b64_tr_b16 v[122:123], v250 offset:50944\n"
"v_mfma_f32_16x16x32_bf16 a[100:103], v[12:15], v[48:51], a[100:103]\n"
"buffer_load_dwordx4 v236, s[20:23], s36 offen offset:0 lds\n"
"v_mfma_f32_16x16x32_bf16 a[92:95], v[12:15], v[44:47], a[92:95]\n"
"ds_read_b64_tr_b16 v[124:125], v251 offset:49920\n"
"v_mfma_f32_16x16x32_bf16 a[84:87], v[12:15], v[40:43], a[84:87]\n"
"ds_read_b64_tr_b16 v[126:127], v251 offset:50944\n"
"v_mfma_f32_16x16x32_bf16 a[76:79], v[12:15], v[36:39], a[76:79]\n"
"v_mfma_f32_16x16x32_bf16 a[68:71], v[12:15], v[32:35], a[68:71]\n"
"ds_read_b128 v[80:83], v254 offset:8192\n"
"v_mfma_f32_16x16x32_bf16 a[128:131], v[16:19], v[32:35], a[128:131]\n"
"ds_read_b128 v[84:87], v254 offset:10240\n"
"v_mfma_f32_16x16x32_bf16 a[136:139], v[16:19], v[36:39], a[136:139]\n"
"buffer_load_dwordx4 v237, s[20:23], s36 offen offset:1024 lds\n"
"v_mfma_f32_16x16x32_bf16 a[144:147], v[16:19], v[40:43], a[144:147]\n"
"ds_read_b128 v[88:91], v254 offset:12288\n"
"v_mfma_f32_16x16x32_bf16 a[152:155], v[16:19], v[44:47], a[152:155]\n"
"ds_read_b128 v[92:95], v254 offset:14336\n"
"v_mfma_f32_16x16x32_bf16 a[160:163], v[16:19], v[48:51], a[160:163]\n"
"v_mfma_f32_16x16x32_bf16 a[168:171], v[16:19], v[52:55], a[168:171]\n"
"v_mfma_f32_16x16x32_bf16 a[176:179], v[16:19], v[56:59], a[176:179]\n"
"v_mfma_f32_16x16x32_bf16 a[184:187], v[16:19], v[60:63], a[184:187]\n"
"buffer_load_dwordx4 v238, s[20:23], s36 offen offset:2048 lds\n"
"v_mfma_f32_16x16x32_bf16 a[188:191], v[20:23], v[60:63], a[188:191]\n"
"v_mfma_f32_16x16x32_bf16 a[180:183], v[20:23], v[56:59], a[180:183]\n"
"v_mfma_f32_16x16x32_bf16 a[172:175], v[20:23], v[52:55], a[172:175]\n"
"v_mfma_f32_16x16x32_bf16 a[164:167], v[20:23], v[48:51], a[164:167]\n"
"v_mfma_f32_16x16x32_bf16 a[156:159], v[20:23], v[44:47], a[156:159]\n"
"v_mfma_f32_16x16x32_bf16 a[148:151], v[20:23], v[40:43], a[148:151]\n"
"buffer_load_dwordx4 v239, s[20:23], s36 offen offset:3072 lds\n"
"v_mfma_f32_16x16x32_bf16 a[140:143], v[20:23], v[36:39], a[140:143]\n"
"v_mfma_f32_16x16x32_bf16 a[132:135], v[20:23], v[32:35], a[132:135]\n"
"v_mfma_f32_16x16x32_bf16 a[192:195], v[24:27], v[32:35], a[192:195]\n"
"s_add_u32 s65, s65, 262144\n"
"s_xor_b32 s38, s65, s67\n"
"v_mfma_f32_16x16x32_bf16 a[200:203], v[24:27], v[36:39], a[200:203]\n"
"s_add_u32 s39, s39, 16384\n"
"v_mfma_f32_16x16x32_bf16 a[208:211], v[24:27], v[40:43], a[208:211]\n"
"s_cmp_eq_u32 s39, 98304\n"
"s_cselect_b32 s39, 0, s39\n"
"v_mfma_f32_16x16x32_bf16 a[216:219], v[24:27], v[44:47], a[216:219]\n"
"s_add_u32 s64, s64, 128\n"
"s_xor_b32 s36, s64, s37\n"
"v_mfma_f32_16x16x32_bf16 a[224:227], v[24:27], v[48:51], a[224:227]\n"
"s_add_u32 s40, s40, 32768\n"
"v_mfma_f32_16x16x32_bf16 a[232:235], v[24:27], v[52:55], a[232:235]\n"
"s_cmp_eq_u32 s40, 98304\n"
"s_cselect_b32 s40, 0, s40\n"
"v_mfma_f32_16x16x32_bf16 a[240:243], v[24:27], v[56:59], a[240:243]\n"
"v_mfma_f32_16x16x32_bf16 a[248:251], v[24:27], v[60:63], a[248:251]\n"
"v_mfma_f32_16x16x32_bf16 a[252:255], v[28:31], v[60:63], a[252:255]\n"
"v_mfma_f32_16x16x32_bf16 a[244:247], v[28:31], v[56:59], a[244:247]\n"
"v_mfma_f32_16x16x32_bf16 a[236:239], v[28:31], v[52:55], a[236:239]\n"
"v_mfma_f32_16x16x32_bf16 a[228:231], v[28:31], v[48:51], a[228:231]\n"
"v_mfma_f32_16x16x32_bf16 a[220:223], v[28:31], v[44:47], a[220:223]\n"
"v_mfma_f32_16x16x32_bf16 a[212:215], v[28:31], v[40:43], a[212:215]\n"
"v_mfma_f32_16x16x32_bf16 a[204:207], v[28:31], v[36:39], a[204:207]\n"
"s_waitcnt vmcnt(16) lgkmcnt(0)\n"
"s_barrier\n"
"v_mfma_f32_16x16x32_bf16 a[196:199], v[28:31], v[32:35], a[196:199]\n"
"v_mfma_f32_16x16x32_bf16 a[0:3], v[64:67], v[96:99], a[0:3]\n"
"v_add_u32 v254, s40, v248\n"
"v_mfma_f32_16x16x32_bf16 a[8:11], v[64:67], v[100:103], a[8:11]\n"
"ds_read_b64_tr_b16 v[32:33], v250 offset:0\n"
"v_mfma_f32_16x16x32_bf16 a[16:19], v[64:67], v[104:107], a[16:19]\n"
"s_add_u32 m0, s42, 49152\n"
"ds_read_b64_tr_b16 v[34:35], v250 offset:1024\n"
"v_mfma_f32_16x16x32_bf16 a[24:27], v[64:67], v[108:111], a[24:27]\n"
"buffer_load_dwordx4 v240, s[24:27], s38 offen offset:0 lds\n"
"v_mfma_f32_16x16x32_bf16 a[32:35], v[64:67], v[112:115], a[32:35]\n"
"ds_read_b64_tr_b16 v[36:37], v251 offset:0\n"
"v_mfma_f32_16x16x32_bf16 a[40:43], v[64:67], v[116:119], a[40:43]\n"
"ds_read_b64_tr_b16 v[38:39], v251 offset:1024\n"
"v_mfma_f32_16x16x32_bf16 a[48:51], v[64:67], v[120:123], a[48:51]\n"
"v_mfma_f32_16x16x32_bf16 a[56:59], v[64:67], v[124:127], a[56:59]\n"
"ds_read_b64_tr_b16 v[40:41], v250 offset:256\n"
"v_mfma_f32_16x16x32_bf16 a[60:63], v[68:71], v[124:127], a[60:63]\n"
"ds_read_b64_tr_b16 v[42:43], v250 offset:1280\n"
"v_mfma_f32_16x16x32_bf16 a[52:55], v[68:71], v[120:123], a[52:55]\n"
"buffer_load_dwordx4 v241, s[24:27], s38 offen offset:1024 lds\n"
"v_mfma_f32_16x16x32_bf16 a[44:47], v[68:71], v[116:119], a[44:47]\n"
"ds_read_b64_tr_b16 v[44:45], v251 offset:256\n"
"v_mfma_f32_16x16x32_bf16 a[36:39], v[68:71], v[112:115], a[36:39]\n"
"ds_read_b64_tr_b16 v[46:47], v251 offset:1280\n"
"v_mfma_f32_16x16x32_bf16 a[28:31], v[68:71], v[108:111], a[28:31]\n"
"v_mfma_f32_16x16x32_bf16 a[20:23], v[68:71], v[104:107], a[20:23]\n"
"ds_read_b128 v[0:3], v254 offset:0\n"
"v_mfma_f32_16x16x32_bf16 a[12:15], v[68:71], v[100:103], a[12:15]\n"
"ds_read_b128 v[4:7], v254 offset:2048\n"
"v_mfma_f32_16x16x32_bf16 a[4:7], v[68:71], v[96:99], a[4:7]\n"
"buffer_load_dwordx4 v242, s[24:27], s38 offen offset:2048 lds\n"
"v_mfma_f32_16x16x32_bf16 a[64:67], v[72:75], v[96:99], a[64:67]\n"
"ds_read_b128 v[8:11], v254 offset:4096\n"
"v_mfma_f32_16x16x32_bf16 a[72:75], v[72:75], v[100:103], a[72:75]\n"
"ds_read_b128 v[12:15], v254 offset:6144\n"
"v_mfma_f32_16x16x32_bf16 a[80:83], v[72:75], v[104:107], a[80:83]\n"
"v_mfma_f32_16x16x32_bf16 a[88:91], v[72:75], v[108:111], a[88:91]\n"
"ds_read_b64_tr_b16 v[48:49], v250 offset:512\n"
"v_mfma_f32_16x16x32_bf16 a[96:99], v[72:75], v[112:115], a[96:99]\n"
"ds_read_b64_tr_b16 v[50:51], v250 offset:1536\n"
"v_mfma_f32_16x16x32_bf16 a[104:107], v[72:75], v[116:119], a[104:107]\n"
"buffer_load_dwordx4 v243, s[24:27], s38 offen offset:3072 lds\n"
"v_mfma_f32_16x16x32_bf16 a[112:115], v[72:75], v[120:123], a[112:115]\n"
"ds_read_b64_tr_b16 v[52:53], v251 offset:512\n"
"v_mfma_f32_16x16x32_bf16 a[120:123], v[72:75], v[124:127], a[120:123]\n"
"ds_read_b64_tr_b16 v[54:55], v251 offset:1536\n"
"v_mfma_f32_16x16x32_bf16 a[124:127], v[76:79], v[124:127], a[124:127]\n"
"v_mfma_f32_16x16x32_bf16 a[116:119], v[76:79], v[120:123], a[116:119]\n"
"ds_read_b64_tr_b16 v[56:57], v250 offset:768\n"
"v_mfma_f32_16x16x32_bf16 a[108:111], v[76:79], v[116:119], a[108:111]\n"
"s_add_u32 m0, s41, s39\n"
"ds_read_b64_tr_b16 v[58:59], v250 offset:1792\n"
"v_mfma_f32_16x16x32_bf16 a[100:103], v[76:79], v[112:115], a[100:103]\n"
"buffer_load_dwordx4 v232, s[20:23], s36 offen offset:0 lds\n"
"v_mfma_f32_16x16x32_bf16 a[92:95], v[76:79], v[108:111], a[92:95]\n"
"ds_read_b64_tr_b16 v[60:61], v251 offset:768\n"
"v_mfma_f32_16x16x32_bf16 a[84:87], v[76:79], v[104:107], a[84:87]\n"
"ds_read_b64_tr_b16 v[62:63], v251 offset:1792\n"
"v_mfma_f32_16x16x32_bf16 a[76:79], v[76:79], v[100:103], a[76:79]\n"
"v_mfma_f32_16x16x32_bf16 a[68:71], v[76:79], v[96:99], a[68:71]\n"
"ds_read_b128 v[16:19], v254 offset:8192\n"
"v_mfma_f32_16x16x32_bf16 a[128:131], v[80:83], v[96:99], a[128:131]\n"
"ds_read_b128 v[20:23], v254 offset:10240\n"
"v_mfma_f32_16x16x32_bf16 a[136:139], v[80:83], v[100:103], a[136:139]\n"
"buffer_load_dwordx4 v233, s[20:23], s36 offen offset:1024 lds\n"
"v_mfma_f32_16x16x32_bf16 a[144:147], v[80:83], v[104:107], a[144:147]\n"
"ds_read_b128 v[24:27], v254 offset:12288\n"
"v_mfma_f32_16x16x32_bf16 a[152:155], v[80:83], v[108:111], a[152:155]\n"
"ds_read_b128 v[28:31], v254 offset:14336\n"
"v_mfma_f32_16x16x32_bf16 a[160:163], v[80:83], v[112:115], a[160:163]\n"
"v_mfma_f32_16x16x32_bf16 a[168:171], v[80:83], v[116:119], a[168:171]\n"
"v_mfma_f32_16x16x32_bf16 a[176:179], v[80:83], v[120:123], a[176:179]\n"
"v_mfma_f32_16x16x32_bf16 a[184:187], v[80:83], v[124:127], a[184:187]\n"
"buffer_load_dwordx4 v234, s[20:23], s36 offen offset:2048 lds\n"
"v_mfma_f32_16x16x32_bf16 a[188:191], v[84:87], v[124:127], a[188:191]\n"
"v_mfma_f32_16x16x32_bf16 a[180:183], v[84:87], v[120:123], a[180:183]\n"
"v_mfma_f32_16x16x32_bf16 a[172:175], v[84:87], v[116:119], a[172:175]\n"
"v_mfma_f32_16x16x32_bf16 a[164:167], v[84:87], v[112:115], a[164:167]\n"
"v_mfma_f32_16x16x32_bf16 a[156:159], v[84:87], v[108:111], a[156:159]\n"
"v_mfma_f32_16x16x32_bf16 a[148:151], v[84:87], v[104:107], a[148:151]\n"
"buffer_load_dwordx4 v235, s[20:23], s36 offen offset:3072 lds\n"
"v_mfma_f32_16x16x32_bf16 a[140:143], v[84:87], v[100:103], a[140:143]\n"
"v_mfma_f32_16x16x32_bf16 a[132:135], v[84:87], v[96:99], a[132:135]\n"
"v_mfma_f32_16x16x32_bf16 a[192:195], v[88:91], v[96:99], a[192:195]\n"
"s_add_u32 s65, s65, 262144\n"
"s_xor_b32 s38, s65, s67\n"
"v_mfma_f32_16x16x32_bf16 a[200:203], v[88:91], v[100:103], a[200:203]\n"
"s_add_u32 s39, s39, 16384\n"
"v_mfma_f32_16x16x32_bf16 a[208:211], v[88:91], v[104:107], a[208:211]\n"
"s_cmp_eq_u32 s39, 98304\n"
"s_cselect_b32 s39, 0, s39\n"
"v_mfma_f32_16x16x32_bf16 a[216:219], v[88:91], v[108:111], a[216:219]\n"
"v_mfma_f32_16x16x32_bf16 a[224:227], v[88:91], v[112:115], a[224:227]\n"
"v_mfma_f32_16x16x32_bf16 a[232:235], v[88:91], v[116:119], a[232:235]\n"
"v_mfma_f32_16x16x32_bf16 a[240:243], v[88:91], v[120:123], a[240:243]\n"
"v_mfma_f32_16x16x32_bf16 a[248:251], v[88:91], v[124:127], a[248:251]\n"
"v_mfma_f32_16x16x32_bf16 a[252:255], v[92:95], v[124:127], a[252:255]\n"
"v_mfma_f32_16x16x32_bf16 a[244:247], v[92:95], v[120:123], a[244:247]\n"
"v_mfma_f32_16x16x32_bf16 a[236:239], v[92:95], v[116:119], a[236:239]\n"
"v_mfma_f32_16x16x32_bf16 a[228:231], v[92:95], v[112:115], a[228:231]\n"
"v_mfma_f32_16x16x32_bf16 a[220:223], v[92:95], v[108:111], a[220:223]\n"
"v_mfma_f32_16x16x32_bf16 a[212:215], v[92:95], v[104:107], a[212:215]\n"
"v_mfma_f32_16x16x32_bf16 a[204:207], v[92:95], v[100:103], a[204:207]\n"
"s_waitcnt vmcnt(16) lgkmcnt(0)\n"
"s_barrier\n"
"v_mfma_f32_16x16x32_bf16 a[196:199], v[92:95], v[96:99], a[196:199]\n"
"s_sub_u32 s43, s43, 1\n"
"s_cbranch_scc0 L_KV_V_loop\n"
"L_KV_stub:\n"
"s_add_u32 s54, s54, 1\n"
"s_cmp_eq_u32 s54, 4\n"
"s_cbranch_scc1 L_KV_end\n"
"s_cmp_eq_u32 s54, 3\n"
"s_cbranch_scc1 L_KV_sw\n"
"s_cmp_eq_u32 s54, 2\n"
"s_cbranch_scc1 L_KV_pu\n"
"L_KV_pu:\n"
"v_readlane_b32 s44, v255, 36\n"
"v_mov_b32 v228, 0x7ffffff0\n"
"v_writelane_b32 v228, 0, 0\n"
"v_mov_b32 v229, s57\n"
"s_nop 1\n"
"buffer_store_dword v229, v228, s[28:31], s44 offen offset:0 sc1\n"
"s_mov_b32 s43, 4\n"
"s_setpc_b64 s[72:73]\n"
"L_KV_sw:\n"
"s_getpc_b64 s[60:61]\n"
"L_ct_1:\n"
"s_sub_u32 s60, s60, L_ct_1-L_code_begin\n"
"s_subb_u32 s61, s61, 0\n"
"s_and_b32 s61, s61, 0xffff\n"
"s_mov_b32 s62, L_code_end-L_code_begin\n"
"s_mov_b32 s63, 0x20000\n"
"v_readlane_b32 s44, v255, 44\n"
"v_mbcnt_lo_u32_b32 v230, -1, 0\n"
"v_and_b32 v230, 3, v230\n"
"v_lshlrev_b32 v230, 7, v230\n"
"s_nop 1\n"
"buffer_load_dword v227, v230, s[60:63], s44 offen offset:0\n"
"v_readlane_b32 s20, v255, 0\n"
"v_readlane_b32 s21, v255, 1\n"
"v_readlane_b32 s24, v255, 2\n"
"v_readlane_b32 s25, v255, 3\n"
"v_readlane_b32 s26, v255, 4\n"
"s_mov_b32 s22, 114688\n"
"s_mov_b32 s36, 0\n"
"s_mov_b32 s38, 0\n"
"s_mov_b32 s64, 0\n"
"s_mov_b32 s65, 0\n"
"s_mov_b32 s37, 0\n"
"s_mov_b32 s67, 0\n"
"s_mov_b32 s43, 0\n"
"s_mov_b32 s62, 0\n"
"v_readlane_b32 s44, v255, 34\n"
"v_mbcnt_lo_u32_b32 v228, -1, 0\n"
"v_mbcnt_hi_u32_b32 v228, -1, v228\n"
"v_lshlrev_b32 v183, 2, v228\n"
"s_nop 1\n"
"buffer_load_dword v231, v183, s[28:31], s44 offen offset:0 sc1\n"
"v_readlane_b32 s68, v255, 40\n"
"v_readlane_b32 s69, v255, 41\n"
"v_readlane_b32 s70, v255, 42\n"
"s_mov_b32 s71, 0x20000\n"
"v_lshrrev_b32 v229, 3, v228\n"
"v_mul_u32_u24 v229, 896, v229\n"
"v_and_b32 v230, 7, v228\n"
"v_lshl_add_u32 v229, v230, 4, v229\n"
"s_add_u32 m0, s41, 32768\n"
"s_mov_b32 s44, 0\n"
"buffer_load_dwordx4 v229, s[68:71], s44 offen offset:0 lds\n"
"s_mov_b32 s44, 6144\n"
"buffer_load_dwordx4 v229, s[68:71], s44 offen offset:1024 lds\n"
"s_mov_b32 s44, 12288\n"
"buffer_load_dwordx4 v229, s[68:71], s44 offen offset:2048 lds\n"
"s_mov_b32 s44, 18432\n"
"buffer_load_dwordx4 v229, s[68:71], s44 offen offset:3072 lds\n"
"v_lshrrev_b32 v181, 2, v228\n"
"v_lshlrev_b32 v181, 7, v181\n"
"v_and_b32 v230, 3, v228\n"
"v_lshl_add_u32 v181, v230, 4, v181\n"
"v_add_u32 v181, s41, v181\n"
"v_lshlrev_b32 v182, 4, v228\n"
"v_mov_b32 v236, v244\n"
"v_add_u32 v232, 0x80, v244\n"
"v_mov_b32 v237, v245\n"
"v_add_u32 v233, 0x80, v245\n"
"v_mov_b32 v238, v246\n"
"v_add_u32 v234, 0x80, v246\n"
"v_mov_b32 v239, v247\n"
"v_add_u32 v235, 0x80, v247\n"
"s_nop 4\n"
"s_setpc_b64 s[72:73]\n"
"L_KV_end:\n"
"s_lshr_b32 s44, s46, 1\n"
"s_lshl_b32 s44, s44, 13\n"
"s_add_u32 s44, s44, 65536\n"
"v_subrev_u32 v248, s44, v248\n"
"v_subrev_u32 v249, s44, v249\n"
"v_cmp_eq_u32 vcc, s57, v231\n"
"s_nop 0\n"
"s_cmp_eq_u32 vcc_lo, -1\n"
"s_cbranch_scc1 L_u_ok\n"
"v_readlane_b32 s45, v255, 34\n"
"s_mov_b32 s55, 100000\n"
"s_nop 3\n"
"L_u_poll:\n"
"buffer_load_dword v231, v183, s[28:31], s45 offen sc1\n"
"s_waitcnt vmcnt(0)\n"
"v_cmp_eq_u32 vcc, s57, v231\n"
"s_nop 0\n"
"s_cmp_eq_u32 vcc_lo, -1\n"
"s_cbranch_scc1 L_u_ok\n"
"s_sleep 4\n"
"s_sub_u32 s55, s55, 1\n"
"s_cbranch_scc0 L_u_poll\n"
"L_u_ok:\n"
"s_add_u32 m0, s42, 0\n"
"s_mov_b32 s45, 1048576\n"
"buffer_load_dwordx4 v240, s[24:27], s45 offen offset:0 lds\n"
"buffer_load_dwordx4 v241, s[24:27], s45 offen offset:1024 lds\n"
"buffer_load_dwordx4 v242, s[24:27], s45 offen offset:2048 lds\n"
"buffer_load_dwordx4 v243, s[24:27], s45 offen offset:3072 lds\n"
"s_add_u32 m0, s42, 81920\n"
"s_mov_b32 s45, 1310720\n"
"buffer_load_dwordx4 v240, s[24:27], s45 offen offset:0 lds\n"
"buffer_load_dwordx4 v241, s[24:27], s45 offen offset:1024 lds\n"
"buffer_load_dwordx4 v242, s[24:27], s45 offen offset:2048 lds\n"
"buffer_load_dwordx4 v243, s[24:27], s45 offen offset:3072 lds\n"
"v_add_u32 v230, 114688, v248\n"
"ds_read_b128 v[0:3], v230 offset:0\n"
"ds_read_b128 v[4:7], v230 offset:2048\n"
"ds_read_b128 v[8:11], v230 offset:4096\n"
"ds_read_b128 v[12:15], v230 offset:6144\n"
"v_readlane_b32 s45, v255, 43\n"
"ds_read_b64_tr_b16 v[208:209], v181 offset:32768\n"
"ds_read_b64_tr_b16 v[210:211], v181 offset:34816\n"
"ds_read_b64_tr_b16 v[212:213], v181 offset:32776\n"
"ds_read_b64_tr_b16 v[214:215], v181 offset:34824\n"
"ds_read_b64_tr_b16 v[216:217], v181 offset:32832\n"
"ds_read_b64_tr_b16 v[218:219], v181 offset:34880\n"
"ds_read_b64_tr_b16 v[220:221], v181 offset:32840\n"
"ds_read_b64_tr_b16 v[222:223], v181 offset:34888\n"
"v_accvgpr_read_b32 v232, a0\n"
"v_accvgpr_read_b32 v233, a1\n"
"v_accvgpr_read_b32 v234, a2\n"
"v_accvgpr_read_b32 v235, a3\n"
"v_accvgpr_read_b32 v236, a4\n"
"v_accvgpr_read_b32 v237, a5\n"
"v_accvgpr_read_b32 v238, a6\n"
"v_accvgpr_read_b32 v239, a7\n"
"v_cvt_pk_bf16_f32 v232, v232, v233\n"
"v_cvt_pk_bf16_f32 v233, v234, v235\n"
"v_cvt_pk_bf16_f32 v234, v236, v237\n"
"v_cvt_pk_bf16_f32 v235, v238, v239\n"
"s_add_u32 s55, s50, 0\n"
"buffer_store_dwordx4 v[232:235], v252, s[28:31], s55 offen offset:0 sc1\n"
"s_waitcnt lgkmcnt(0)\n"
"s_add_u32 s44, s45, 0\n"
"buffer_store_dwordx4 v[208:211], v182, s[28:31], s44 offen offset:0 sc1\n"
"s_add_u32 s44, s45, 8192\n"
"buffer_store_dwordx4 v[212:215], v182, s[28:31], s44 offen offset:0 sc1\n"
"s_add_u32 s44, s45, 16384\n"
"buffer_store_dwordx4 v[216:219], v182, s[28:31], s44 offen offset:0 sc1\n"
"s_add_u32 s44, s45, 24576\n"
"buffer_store_dwordx4 v[220:223], v182, s[28:31], s44 offen offset:0 sc1\n"
"v_accvgpr_read_b32 v232, a8\n"
"v_accvgpr_read_b32 v233, a9\n"
"v_accvgpr_read_b32 v234, a10\n"
"v_accvgpr_read_b32 v235, a11\n"
"v_accvgpr_read_b32 v236, a12\n"
"v_accvgpr_read_b32 v237, a13\n"
"v_accvgpr_read_b32 v238, a14\n"
"v_accvgpr_read_b32 v239, a15\n"
"v_cvt_pk_bf16_f32 v232, v232, v233\n"
"v_cvt_pk_bf16_f32 v233, v234, v235\n"
"v_cvt_pk_bf16_f32 v234, v236, v237\n"
"v_cvt_pk_bf16_f32 v235, v238, v239\n"
"buffer_store_dwordx4 v[232:235], v252, s[28:31], s55 offen offset:1024 sc1\n"
"s_nop 1\n"
"v_accvgpr_read_b32 v232, a16\n"
"v_accvgpr_read_b32 v233, a17\n"
"v_accvgpr_read_b32 v234, a18\n"
"v_accvgpr_read_b32 v235, a19\n"
"v_accvgpr_read_b32 v236, a20\n"
"v_accvgpr_read_b32 v237, a21\n"
"v_accvgpr_read_b32 v238, a22\n"
"v_accvgpr_read_b32 v239, a23\n"
"v_cvt_pk_bf16_f32 v232, v232, v233\n"
"v_cvt_pk_bf16_f32 v233, v234, v235\n"
"v_cvt_pk_bf16_f32 v234, v236, v237\n"
"v_cvt_pk_bf16_f32 v235, v238, v239\n"
"buffer_store_dwordx4 v[232:235], v252, s[28:31], s55 offen offset:2048 sc1\n"
"s_nop 1\n"
"v_accvgpr_read_b32 v232, a24\n"
"v_accvgpr_read_b32 v233, a25\n"
"v_accvgpr_read_b32 v234, a26\n"
"v_accvgpr_read_b32 v235, a27\n"
"v_accvgpr_read_b32 v236, a28\n"
"v_accvgpr_read_b32 v237, a29\n"
"v_accvgpr_read_b32 v238, a30\n"
"v_accvgpr_read_b32 v239, a31\n"
"v_cvt_pk_bf16_f32 v232, v232, v233\n"
"v_cvt_pk_bf16_f32 v233, v234, v235\n"
"v_cvt_pk_bf16_f32 v234, v236, v237\n"
"v_cvt_pk_bf16_f32 v235, v238, v239\n"
"buffer_store_dwordx4 v[232:235], v252, s[28:31], s55 offen offset:3072 sc1\n"
"s_nop 1\n"
"v_accvgpr_read_b32 v232, a32\n"
"v_accvgpr_read_b32 v233, a33\n"
"v_accvgpr_read_b32 v234, a34\n"
"v_accvgpr_read_b32 v235, a35\n"
"v_accvgpr_read_b32 v236, a36\n"
"v_accvgpr_read_b32 v237, a37\n"
"v_accvgpr_read_b32 v238, a38\n"
"v_accvgpr_read_b32 v239, a39\n"
"v_cvt_pk_bf16_f32 v232, v232, v233\n"
"v_cvt_pk_bf16_f32 v233, v234, v235\n"
"v_cvt_pk_bf16_f32 v234, v236, v237\n"
"v_cvt_pk_bf16_f32 v235, v238, v239\n"
"s_add_u32 s55, s51, 0\n"
"buffer_store_dwordx4 v[232:235], v252, s[28:31], s55 offen offset:0 sc1\n"
"s_nop 1\n"
"v_accvgpr_read_b32 v232, a40\n"
"v_accvgpr_read_b32 v233, a41\n"
"v_accvgpr_read_b32 v234, a42\n"
"v_accvgpr_read_b32 v235, a43\n"
"v_accvgpr_read_b32 v236, a44\n"
"v_accvgpr_read_b32 v237, a45\n"
"v_accvgpr_read_b32 v238, a46\n"
"v_accvgpr_read_b32 v239, a47\n"
"v_cvt_pk_bf16_f32 v232, v232, v233\n"
"v_cvt_pk_bf16_f32 v233, v234, v235\n"
"v_cvt_pk_bf16_f32 v234, v236, v237\n"
"v_cvt_pk_bf16_f32 v235, v238, v239\n"
"buffer_store_dwordx4 v[232:235], v252, s[28:31], s55 offen offset:1024 sc1\n"
"s_waitcnt lgkmcnt(0)\n"
"v_mfma_f32_16x16x32_bf16 v[128:131], v[32:35], v[0:3], 0\n"
"v_add_u32 v228, 16384, v250\n"
"v_add_u32 v229, 16384, v251\n"
"v_accvgpr_read_b32 v232, a48\n"
"v_accvgpr_read_b32 v233, a49\n"
"v_mfma_f32_16x16x32_bf16 v[132:135], v[36:39], v[0:3], 0\n"
"v_add_u32 v230, 114688, v249\n"
"ds_read_b64_tr_b16 v[96:97], v228 offset:0\n"
"v_accvgpr_read_b32 v234, a50\n"
"v_accvgpr_read_b32 v235, a51\n"
"v_mfma_f32_16x16x32_bf16 v[136:139], v[40:43], v[0:3], 0\n"
"ds_read_b64_tr_b16 v[98:99], v228 offset:1024\n"
"v_accvgpr_read_b32 v236, a52\n"
"v_accvgpr_read_b32 v237, a53\n"
"v_mfma_f32_16x16x32_bf16 v[140:143], v[44:47], v[0:3], 0\n"
"ds_read_b64_tr_b16 v[100:101], v229 offset:0\n"
"v_accvgpr_read_b32 v238, a54\n"
"v_mfma_f32_16x16x32_bf16 v[144:147], v[48:51], v[0:3], 0\n"
"ds_read_b64_tr_b16 v[102:103], v229 offset:1024\n"
"v_accvgpr_read_b32 v239, a55\n"
"v_cvt_pk_bf16_f32 v232, v232, v233\n"
"v_mfma_f32_16x16x32_bf16 v[148:151], v[52:55], v[0:3], 0\n"
"ds_read_b64_tr_b16 v[104:105], v228 offset:256\n"
"v_cvt_pk_bf16_f32 v233, v234, v235\n"
"v_cvt_pk_bf16_f32 v234, v236, v237\n"
"v_mfma_f32_16x16x32_bf16 v[152:155], v[56:59], v[0:3], 0\n"
"ds_read_b64_tr_b16 v[106:107], v228 offset:1280\n"
"v_cvt_pk_bf16_f32 v235, v238, v239\n"
"v_mfma_f32_16x16x32_bf16 v[156:159], v[60:63], v[0:3], 0\n"
"ds_read_b64_tr_b16 v[108:109], v229 offset:256\n"
"buffer_store_dwordx4 v[232:235], v252, s[28:31], s55 offen offset:2048 sc1\n"
"s_nop 1\n"
"v_accvgpr_read_b32 v232, a56\n"
"v_mfma_f32_16x16x32_bf16 v[188:191], v[60:63], v[4:7], 0\n"
"ds_read_b64_tr_b16 v[110:111], v229 offset:1280\n"
"v_accvgpr_read_b32 v233, a57\n"
"v_accvgpr_read_b32 v234, a58\n"
"v_mfma_f32_16x16x32_bf16 v[184:187], v[56:59], v[4:7], 0\n"
"ds_read_b128 v[64:67], v230 offset:0\n"
"v_accvgpr_read_b32 v235, a59\n"
"v_accvgpr_read_b32 v236, a60\n"
"v_mfma_f32_16x16x32_bf16 v[180:183], v[52:55], v[4:7], 0\n"
"ds_read_b128 v[68:71], v230 offset:2048\n"
"v_accvgpr_read_b32 v237, a61\n"
"v_mfma_f32_16x16x32_bf16 v[176:179], v[48:51], v[4:7], 0\n"
"ds_read_b64_tr_b16 v[112:113], v228 offset:512\n"
"v_accvgpr_read_b32 v238, a62\n"
"v_accvgpr_read_b32 v239, a63\n"
"v_mfma_f32_16x16x32_bf16 v[172:175], v[44:47], v[4:7], 0\n"
"ds_read_b64_tr_b16 v[114:115], v228 offset:1536\n"
"v_cvt_pk_bf16_f32 v232, v232, v233\n"
"v_cvt_pk_bf16_f32 v233, v234, v235\n"
"v_mfma_f32_16x16x32_bf16 v[168:171], v[40:43], v[4:7], 0\n"
"ds_read_b64_tr_b16 v[116:117], v229 offset:512\n"
"v_cvt_pk_bf16_f32 v234, v236, v237\n"
"v_mfma_f32_16x16x32_bf16 v[164:167], v[36:39], v[4:7], 0\n"
"ds_read_b64_tr_b16 v[118:119], v229 offset:1536\n"
"v_cvt_pk_bf16_f32 v235, v238, v239\n"
"buffer_store_dwordx4 v[232:235], v252, s[28:31], s55 offen offset:3072 sc1\n"
"v_mfma_f32_16x16x32_bf16 v[160:163], v[32:35], v[4:7], 0\n"
"ds_read_b64_tr_b16 v[120:121], v228 offset:768\n"
"v_accvgpr_read_b32 v232, a64\n"
"v_accvgpr_read_b32 v233, a65\n"
"v_mfma_f32_16x16x32_bf16 v[192:195], v[32:35], v[8:11], 0\n"
"ds_read_b64_tr_b16 v[122:123], v228 offset:1792\n"
"v_accvgpr_read_b32 v234, a66\n"
"v_accvgpr_read_b32 v235, a67\n"
"v_mfma_f32_16x16x32_bf16 v[196:199], v[36:39], v[8:11], 0\n"
"ds_read_b64_tr_b16 v[124:125], v229 offset:768\n"
"v_accvgpr_read_b32 v236, a68\n"
"v_mfma_f32_16x16x32_bf16 v[200:203], v[40:43], v[8:11], 0\n"
"ds_read_b64_tr_b16 v[126:127], v229 offset:1792\n"
"v_accvgpr_read_b32 v237, a69\n"
"v_accvgpr_read_b32 v238, a70\n"
"v_mfma_f32_16x16x32_bf16 v[204:207], v[44:47], v[8:11], 0\n"
"ds_read_b128 v[72:75], v230 offset:4096\n"
"v_accvgpr_read_b32 v239, a71\n"
"v_cvt_pk_bf16_f32 v232, v232, v233\n"
"v_mfma_f32_16x16x32_bf16 v[208:211], v[48:51], v[8:11], 0\n"
"ds_read_b128 v[76:79], v230 offset:6144\n"
"v_cvt_pk_bf16_f32 v233, v234, v235\n"
"v_mfma_f32_16x16x32_bf16 v[212:215], v[52:55], v[8:11], 0\n"
"v_cvt_pk_bf16_f32 v234, v236, v237\n"
"v_cvt_pk_bf16_f32 v235, v238, v239\n"
"v_mfma_f32_16x16x32_bf16 v[216:219], v[56:59], v[8:11], 0\n"
"s_add_u32 s55, s52, 0\n"
"buffer_store_dwordx4 v[232:235], v252, s[28:31], s55 offen offset:0 sc1\n"
"v_mfma_f32_16x16x32_bf16 v[220:223], v[60:63], v[8:11], 0\n"
"s_nop 0\n"
"v_accvgpr_read_b32 v232, a72\n"
"v_accvgpr_read_b32 v233, a73\n"
"v_mfma_f32_16x16x32_bf16 v[88:91], v[60:63], v[12:15], 0\n"
"v_accvgpr_read_b32 v234, a74\n"
"v_mfma_f32_16x16x32_bf16 v[84:87], v[56:59], v[12:15], 0\n"
"v_accvgpr_read_b32 v235, a75\n"
"v_accvgpr_read_b32 v236, a76\n"
"v_mfma_f32_16x16x32_bf16 v[80:83], v[52:55], v[12:15], 0\n"
"v_accvgpr_read_b32 v237, a77\n"
"v_accvgpr_read_b32 v238, a78\n"
"v_mfma_f32_16x16x32_bf16 v[28:31], v[48:51], v[12:15], 0\n"
"v_accvgpr_read_b32 v239, a79\n"
"v_mfma_f32_16x16x32_bf16 v[24:27], v[44:47], v[12:15], 0\n"
"v_cvt_pk_bf16_f32 v232, v232, v233\n"
"v_cvt_pk_bf16_f32 v233, v234, v235\n"
"v_mfma_f32_16x16x32_bf16 v[20:23], v[40:43], v[12:15], 0\n"
"v_cvt_pk_bf16_f32 v234, v236, v237\n"
"v_cvt_pk_bf16_f32 v235, v238, v239\n"
"v_mfma_f32_16x16x32_bf16 v[16:19], v[36:39], v[12:15], 0\n"
"s_waitcnt vmcnt(29) lgkmcnt(0)\n"
"s_barrier\n"
"buffer_store_dwordx4 v[232:235], v252, s[28:31], s55 offen offset:1024 sc1\n"
"v_mfma_f32_16x16x32_bf16 v[224:227], v[32:35], v[12:15], 0\n"
"v_mfma_f32_16x16x32_bf16 v[128:131], v[96:99], v[64:67], v[128:131]\n"
"v_add_u32 v228, 32768, v250\n"
"v_add_u32 v229, 32768, v251\n"
"v_accvgpr_read_b32 v232, a80\n"
"v_accvgpr_read_b32 v233, a81\n"
"v_mfma_f32_16x16x32_bf16 v[132:135], v[100:103], v[64:67], v[132:135]\n"
"v_add_u32 v230, 147456, v248\n"
"ds_read_b64_tr_b16 v[32:33], v228 offset:0\n"
"v_accvgpr_read_b32 v234, a82\n"
"v_accvgpr_read_b32 v235, a83\n"
"v_mfma_f32_16x16x32_bf16 v[136:139], v[104:107], v[64:67], v[136:139]\n"
"ds_read_b64_tr_b16 v[34:35], v228 offset:1024\n"
"s_add_u32 m0, s42, 16384\n"
"v_accvgpr_read_b32 v236, a84\n"
"v_accvgpr_read_b32 v237, a85\n"
"v_mfma_f32_16x16x32_bf16 v[140:143], v[108:111], v[64:67], v[140:143]\n"
"ds_read_b64_tr_b16 v[36:37], v229 offset:0\n"
"s_mov_b32 s45, 1572864\n"
"v_accvgpr_read_b32 v238, a86\n"
"v_mfma_f32_16x16x32_bf16 v[144:147], v[112:115], v[64:67], v[144:147]\n"
"ds_read_b64_tr_b16 v[38:39], v229 offset:1024\n"
"buffer_load_dwordx4 v240, s[24:27], s45 offen offset:0 lds\n"
"v_accvgpr_read_b32 v239, a87\n"
"v_cvt_pk_bf16_f32 v232, v232, v233\n"
"v_mfma_f32_16x16x32_bf16 v[148:151], v[116:119], v[64:67], v[148:151]\n"
"ds_read_b64_tr_b16 v[40:41], v228 offset:256\n"
"v_cvt_pk_bf16_f32 v233, v234, v235\n"
"v_cvt_pk_bf16_f32 v234, v236, v237\n"
"v_mfma_f32_16x16x32_bf16 v[152:155], v[120:123], v[64:67], v[152:155]\n"
"ds_read_b64_tr_b16 v[42:43], v228 offset:1280\n"
"buffer_load_dwordx4 v241, s[24:27], s45 offen offset:1024 lds\n"
"v_cvt_pk_bf16_f32 v235, v238, v239\n"
"v_mfma_f32_16x16x32_bf16 v[156:159], v[124:127], v[64:67], v[156:159]\n"
"ds_read_b64_tr_b16 v[44:45], v229 offset:256\n"
"buffer_store_dwordx4 v[232:235], v252, s[28:31], s55 offen offset:2048 sc1\n"
"s_nop 1\n"
"v_accvgpr_read_b32 v232, a88\n"
"v_mfma_f32_16x16x32_bf16 v[188:191], v[124:127], v[68:71], v[188:191]\n"
"ds_read_b64_tr_b16 v[46:47], v229 offset:1280\n"
"buffer_load_dwordx4 v242, s[24:27], s45 offen offset:2048 lds\n"
"v_accvgpr_read_b32 v233, a89\n"
"v_accvgpr_read_b32 v234, a90\n"
"v_mfma_f32_16x16x32_bf16 v[184:187], v[120:123], v[68:71], v[184:187]\n"
"ds_read_b128 v[0:3], v230 offset:0\n"
"v_accvgpr_read_b32 v235, a91\n"
"v_accvgpr_read_b32 v236, a92\n"
"v_mfma_f32_16x16x32_bf16 v[180:183], v[116:119], v[68:71], v[180:183]\n"
"ds_read_b128 v[4:7], v230 offset:2048\n"
"buffer_load_dwordx4 v243, s[24:27], s45 offen offset:3072 lds\n"
"v_accvgpr_read_b32 v237, a93\n"
"v_mfma_f32_16x16x32_bf16 v[176:179], v[112:115], v[68:71], v[176:179]\n"
"ds_read_b64_tr_b16 v[48:49], v228 offset:512\n"
"s_add_u32 m0, s42, 98304\n"
"v_accvgpr_read_b32 v238, a94\n"
"v_accvgpr_read_b32 v239, a95\n"
"v_mfma_f32_16x16x32_bf16 v[172:175], v[108:111], v[68:71], v[172:175]\n"
"ds_read_b64_tr_b16 v[50:51], v228 offset:1536\n"
"s_mov_b32 s45, 1835008\n"
"v_cvt_pk_bf16_f32 v232, v232, v233\n"
"v_cvt_pk_bf16_f32 v233, v234, v235\n"
"v_mfma_f32_16x16x32_bf16 v[168:171], v[104:107], v[68:71], v[168:171]\n"
"ds_read_b64_tr_b16 v[52:53], v229 offset:512\n"
"buffer_load_dwordx4 v240, s[24:27], s45 offen offset:0 lds\n"
"v_cvt_pk_bf16_f32 v234, v236, v237\n"
"v_mfma_f32_16x16x32_bf16 v[164:167], v[100:103], v[68:71], v[164:167]\n"
"ds_read_b64_tr_b16 v[54:55], v229 offset:1536\n"
"v_cvt_pk_bf16_f32 v235, v238, v239\n"
"buffer_store_dwordx4 v[232:235], v252, s[28:31], s55 offen offset:3072 sc1\n"
"v_mfma_f32_16x16x32_bf16 v[160:163], v[96:99], v[68:71], v[160:163]\n"
"ds_read_b64_tr_b16 v[56:57], v228 offset:768\n"
"buffer_load_dwordx4 v241, s[24:27], s45 offen offset:1024 lds\n"
"v_accvgpr_read_b32 v232, a96\n"
"v_accvgpr_read_b32 v233, a97\n"
"v_mfma_f32_16x16x32_bf16 v[192:195], v[96:99], v[72:75], v[192:195]\n"
"ds_read_b64_tr_b16 v[58:59], v228 offset:1792\n"
"v_accvgpr_read_b32 v234, a98\n"
"v_accvgpr_read_b32 v235, a99\n"
"v_mfma_f32_16x16x32_bf16 v[196:199], v[100:103], v[72:75], v[196:199]\n"
"ds_read_b64_tr_b16 v[60:61], v229 offset:768\n"
"buffer_load_dwordx4 v242, s[24:27], s45 offen offset:2048 lds\n"
"v_accvgpr_read_b32 v236, a100\n"
"v_mfma_f32_16x16x32_bf16 v[200:203], v[104:107], v[72:75], v[200:203]\n"
"ds_read_b64_tr_b16 v[62:63], v229 offset:1792\n"
"v_accvgpr_read_b32 v237, a101\n"
"v_accvgpr_read_b32 v238, a102\n"
"v_mfma_f32_16x16x32_bf16 v[204:207], v[108:111], v[72:75], v[204:207]\n"
"ds_read_b128 v[8:11], v230 offset:4096\n"
"buffer_load_dwordx4 v243, s[24:27], s45 offen offset:3072 lds\n"
"v_accvgpr_read_b32 v239, a103\n"
"v_cvt_pk_bf16_f32 v232, v232, v233\n"
"v_mfma_f32_16x16x32_bf16 v[208:211], v[112:115], v[72:75], v[208:211]\n"
"ds_read_b128 v[12:15], v230 offset:6144\n"
"s_add_u32 m0, s41, 49152\n"
"v_cvt_pk_bf16_f32 v233, v234, v235\n"
"v_mfma_f32_16x16x32_bf16 v[212:215], v[116:119], v[72:75], v[212:215]\n"
"s_mov_b32 s45, 512\n"
"v_cvt_pk_bf16_f32 v234, v236, v237\n"
"v_cvt_pk_bf16_f32 v235, v238, v239\n"
"v_mfma_f32_16x16x32_bf16 v[216:219], v[120:123], v[72:75], v[216:219]\n"
"buffer_load_dwordx4 v244, s[20:23], s45 offen offset:0 lds\n"
"s_add_u32 s55, s53, 0\n"
"buffer_store_dwordx4 v[232:235], v252, s[28:31], s55 offen offset:0 sc1\n"
"v_mfma_f32_16x16x32_bf16 v[220:223], v[124:127], v[72:75], v[220:223]\n"
"s_nop 0\n"
"v_accvgpr_read_b32 v232, a104\n"
"v_accvgpr_read_b32 v233, a105\n"
"v_mfma_f32_16x16x32_bf16 v[88:91], v[124:127], v[76:79], v[88:91]\n"
"buffer_load_dwordx4 v245, s[20:23], s45 offen offset:1024 lds\n"
"v_accvgpr_read_b32 v234, a106\n"
"v_mfma_f32_16x16x32_bf16 v[84:87], v[120:123], v[76:79], v[84:87]\n"
"v_accvgpr_read_b32 v235, a107\n"
"v_accvgpr_read_b32 v236, a108\n"
"v_mfma_f32_16x16x32_bf16 v[80:83], v[116:119], v[76:79], v[80:83]\n"
"buffer_load_dwordx4 v246, s[20:23], s45 offen offset:2048 lds\n"
"v_accvgpr_read_b32 v237, a109\n"
"v_accvgpr_read_b32 v238, a110\n"
"v_mfma_f32_16x16x32_bf16 v[28:31], v[112:115], v[76:79], v[28:31]\n"
"v_accvgpr_read_b32 v239, a111\n"
"v_mfma_f32_16x16x32_bf16 v[24:27], v[108:111], v[76:79], v[24:27]\n"
"buffer_load_dwordx4 v247, s[20:23], s45 offen offset:3072 lds\n"
"v_cvt_pk_bf16_f32 v232, v232, v233\n"
"v_cvt_pk_bf16_f32 v233, v234, v235\n"
"v_mfma_f32_16x16x32_bf16 v[20:23], v[104:107], v[76:79], v[20:23]\n"
"v_cvt_pk_bf16_f32 v234, v236, v237\n"
"v_cvt_pk_bf16_f32 v235, v238, v239\n"
"v_mfma_f32_16x16x32_bf16 v[16:19], v[100:103], v[76:79], v[16:19]\n"
"s_waitcnt vmcnt(41) lgkmcnt(0)\n"
"s_barrier\n"
"buffer_store_dwordx4 v[232:235], v252, s[28:31], s55 offen offset:1024 sc1\n"
"v_mfma_f32_16x16x32_bf16 v[224:227], v[96:99], v[76:79], v[224:227]\n"
"v_mfma_f32_16x16x32_bf16 v[128:131], v[32:35], v[0:3], v[128:131]\n"
"v_add_u32 v228, 49152, v250\n"
"v_add_u32 v229, 49152, v251\n"
"v_accvgpr_read_b32 v232, a112\n"
"v_accvgpr_read_b32 v233, a113\n"
"v_mfma_f32_16x16x32_bf16 v[132:135], v[36:39], v[0:3], v[132:135]\n"
"v_add_u32 v230, 147456, v249\n"
"ds_read_b64_tr_b16 v[96:97], v228 offset:0\n"
"v_accvgpr_read_b32 v234, a114\n"
"v_accvgpr_read_b32 v235, a115\n"
"v_mfma_f32_16x16x32_bf16 v[136:139], v[40:43], v[0:3], v[136:139]\n"
"ds_read_b64_tr_b16 v[98:99], v228 offset:1024\n"
"s_add_u32 m0, s42, 32768\n"
"v_accvgpr_read_b32 v236, a116\n"
"v_accvgpr_read_b32 v237, a117\n"
"v_mfma_f32_16x16x32_bf16 v[140:143], v[44:47], v[0:3], v[140:143]\n"
"ds_read_b64_tr_b16 v[100:101], v229 offset:0\n"
"s_mov_b32 s45, 2097152\n"
"v_accvgpr_read_b32 v238, a118\n"
"v_mfma_f32_16x16x32_bf16 v[144:147], v[48:51], v[0:3], v[144:147]\n"
"ds_read_b64_tr_b16 v[102:103], v229 offset:1024\n"
"buffer_load_dwordx4 v240, s[24:27], s45 offen offset:0 lds\n"
"v_accvgpr_read_b32 v239, a119\n"
"v_cvt_pk_bf16_f32 v232, v232, v233\n"
"v_mfma_f32_16x16x32_bf16 v[148:151], v[52:55], v[0:3], v[148:151]\n"
"ds_read_b64_tr_b16 v[104:105], v228 offset:256\n"
"v_cvt_pk_bf16_f32 v233, v234, v235\n"
"v_cvt_pk_bf16_f32 v234, v236, v237\n"
"v_mfma_f32_16x16x32_bf16 v[152:155], v[56:59], v[0:3], v[152:155]\n"
"ds_read_b64_tr_b16 v[106:107], v228 offset:1280\n"
"buffer_load_dwordx4 v241, s[24:27], s45 offen offset:1024 lds\n"
"v_cvt_pk_bf16_f32 v235, v238, v239\n"
"v_mfma_f32_16x16x32_bf16 v[156:159], v[60:63], v[0:3], v[156:159]\n"
"ds_read_b64_tr_b16 v[108:109], v229 offset:256\n"
"buffer_store_dwordx4 v[232:235], v252, s[28:31], s55 offen offset:2048 sc1\n"
"s_nop 1\n"
"v_accvgpr_read_b32 v232, a120\n"
"v_mfma_f32_16x16x32_bf16 v[188:191], v[60:63], v[4:7], v[188:191]\n"
"ds_read_b64_tr_b16 v[110:111], v229 offset:1280\n"
"buffer_load_dwordx4 v242, s[24:27], s45 offen offset:2048 lds\n"
"v_accvgpr_read_b32 v233, a121\n"
"v_accvgpr_read_b32 v234, a122\n"
"v_mfma_f32_16x16x32_bf16 v[184:187], v[56:59], v[4:7], v[184:187]\n"
"ds_read_b128 v[64:67], v230 offset:0\n"
"v_accvgpr_read_b32 v235, a123\n"
"v_accvgpr_read_b32 v236, a124\n"
"v_mfma_f32_16x16x32_bf16 v[180:183], v[52:55], v[4:7], v[180:183]\n"
"ds_read_b128 v[68:71], v230 offset:2048\n"
"buffer_load_dwordx4 v243, s[24:27], s45 offen offset:3072 lds\n"
"v_accvgpr_read_b32 v237, a125\n"
"v_mfma_f32_16x16x32_bf16 v[176:179], v[48:51], v[4:7], v[176:179]\n"
"ds_read_b64_tr_b16 v[112:113], v228 offset:512\n"
"v_accvgpr_read_b32 v238, a126\n"
"v_accvgpr_read_b32 v239, a127\n"
"v_mfma_f32_16x16x32_bf16 v[172:175], v[44:47], v[4:7], v[172:175]\n"
"ds_read_b64_tr_b16 v[114:115], v228 offset:1536\n"
"v_cvt_pk_bf16_f32 v232, v232, v233\n"
"v_cvt_pk_bf16_f32 v233, v234, v235\n"
"v_mfma_f32_16x16x32_bf16 v[168:171], v[40:43], v[4:7], v[168:171]\n"
"ds_read_b64_tr_b16 v[116:117], v229 offset:512\n"
"v_cvt_pk_bf16_f32 v234, v236, v237\n"
"v_mfma_f32_16x16x32_bf16 v[164:167], v[36:39], v[4:7], v[164:167]\n"
"ds_read_b64_tr_b16 v[118:119], v229 offset:1536\n"
"v_cvt_pk_bf16_f32 v235, v238, v239\n"
"buffer_store_dwordx4 v[232:235], v252, s[28:31], s55 offen offset:3072 sc1\n"
"v_mfma_f32_16x16x32_bf16 v[160:163], v[32:35], v[4:7], v[160:163]\n"
"ds_read_b64_tr_b16 v[120:121], v228 offset:768\n"
"v_accvgpr_read_b32 v232, a128\n"
"v_accvgpr_read_b32 v233, a129\n"
"v_mfma_f32_16x16x32_bf16 v[192:195], v[32:35], v[8:11], v[192:195]\n"
"ds_read_b64_tr_b16 v[122:123], v228 offset:1792\n"
"v_accvgpr_read_b32 v234, a130\n"
"v_accvgpr_read_b32 v235, a131\n"
"v_mfma_f32_16x16x32_bf16 v[196:199], v[36:39], v[8:11], v[196:199]\n"
"ds_read_b64_tr_b16 v[124:125], v229 offset:768\n"
"v_accvgpr_read_b32 v236, a132\n"
"v_mfma_f32_16x16x32_bf16 v[200:203], v[40:43], v[8:11], v[200:203]\n"
"ds_read_b64_tr_b16 v[126:127], v229 offset:1792\n"
"v_accvgpr_read_b32 v237, a133\n"
"v_accvgpr_read_b32 v238, a134\n"
"v_mfma_f32_16x16x32_bf16 v[204:207], v[44:47], v[8:11], v[204:207]\n"
"ds_read_b128 v[72:75], v230 offset:4096\n"
"v_accvgpr_read_b32 v239, a135\n"
"v_cvt_pk_bf16_f32 v232, v232, v233\n"
"v_mfma_f32_16x16x32_bf16 v[208:211], v[48:51], v[8:11], v[208:211]\n"
"ds_read_b128 v[76:79], v230 offset:6144\n"
"v_cvt_pk_bf16_f32 v233, v234, v235\n"
"v_mfma_f32_16x16x32_bf16 v[212:215], v[52:55], v[8:11], v[212:215]\n"
"v_cvt_pk_bf16_f32 v234, v236, v237\n"
"v_cvt_pk_bf16_f32 v235, v238, v239\n"
"v_mfma_f32_16x16x32_bf16 v[216:219], v[56:59], v[8:11], v[216:219]\n"
"s_add_u32 s55, s50, 65536\n"
"buffer_store_dwordx4 v[232:235], v252, s[28:31], s55 offen offset:0 sc1\n"
"v_mfma_f32_16x16x32_bf16 v[220:223], v[60:63], v[8:11], v[220:223]\n"
"s_nop 0\n"
"v_accvgpr_read_b32 v232, a136\n"
"v_accvgpr_read_b32 v233, a137\n"
"v_mfma_f32_16x16x32_bf16 v[88:91], v[60:63], v[12:15], v[88:91]\n"
"v_accvgpr_read_b32 v234, a138\n"
"v_mfma_f32_16x16x32_bf16 v[84:87], v[56:59], v[12:15], v[84:87]\n"
"v_accvgpr_read_b32 v235, a139\n"
"v_accvgpr_read_b32 v236, a140\n"
"v_mfma_f32_16x16x32_bf16 v[80:83], v[52:55], v[12:15], v[80:83]\n"
"v_accvgpr_read_b32 v237, a141\n"
"v_accvgpr_read_b32 v238, a142\n"
"v_mfma_f32_16x16x32_bf16 v[28:31], v[48:51], v[12:15], v[28:31]\n"
"v_accvgpr_read_b32 v239, a143\n"
"v_mfma_f32_16x16x32_bf16 v[24:27], v[44:47], v[12:15], v[24:27]\n"
"v_cvt_pk_bf16_f32 v232, v232, v233\n"
"v_cvt_pk_bf16_f32 v233, v234, v235\n"
"v_mfma_f32_16x16x32_bf16 v[20:23], v[40:43], v[12:15], v[20:23]\n"
"v_cvt_pk_bf16_f32 v234, v236, v237\n"
"v_cvt_pk_bf16_f32 v235, v238, v239\n"
"v_mfma_f32_16x16x32_bf16 v[16:19], v[36:39], v[12:15], v[16:19]\n"
"s_waitcnt vmcnt(41) lgkmcnt(0)\n"
"s_barrier\n"
"buffer_store_dwordx4 v[232:235], v252, s[28:31], s55 offen offset:1024 sc1\n"
"v_mfma_f32_16x16x32_bf16 v[224:227], v[32:35], v[12:15], v[224:227]\n"
"v_mfma_f32_16x16x32_bf16 v[128:131], v[96:99], v[64:67], v[128:131]\n"
"v_add_u32 v228, 0, v250\n"
"v_add_u32 v229, 0, v251\n"
"v_accvgpr_read_b32 v232, a144\n"
"v_accvgpr_read_b32 v233, a145\n"
"v_mfma_f32_16x16x32_bf16 v[132:135], v[100:103], v[64:67], v[132:135]\n"
"v_add_u32 v230, 131072, v248\n"
"ds_read_b64_tr_b16 v[32:33], v228 offset:0\n"
"v_accvgpr_read_b32 v234, a146\n"
"v_accvgpr_read_b32 v235, a147\n"
"v_mfma_f32_16x16x32_bf16 v[136:139], v[104:107], v[64:67], v[136:139]\n"
"ds_read_b64_tr_b16 v[34:35], v228 offset:1024\n"
"s_add_u32 m0, s42, 49152\n"
"v_accvgpr_read_b32 v236, a148\n"
"v_accvgpr_read_b32 v237, a149\n"
"v_mfma_f32_16x16x32_bf16 v[140:143], v[108:111], v[64:67], v[140:143]\n"
"ds_read_b64_tr_b16 v[36:37], v229 offset:0\n"
"s_mov_b32 s45, 2359296\n"
"v_accvgpr_read_b32 v238, a150\n"
"v_mfma_f32_16x16x32_bf16 v[144:147], v[112:115], v[64:67], v[144:147]\n"
"ds_read_b64_tr_b16 v[38:39], v229 offset:1024\n"
"buffer_load_dwordx4 v240, s[24:27], s45 offen offset:0 lds\n"
"v_accvgpr_read_b32 v239, a151\n"
"v_cvt_pk_bf16_f32 v232, v232, v233\n"
"v_mfma_f32_16x16x32_bf16 v[148:151], v[116:119], v[64:67], v[148:151]\n"
"ds_read_b64_tr_b16 v[40:41], v228 offset:256\n"
"v_cvt_pk_bf16_f32 v233, v234, v235\n"
"v_cvt_pk_bf16_f32 v234, v236, v237\n"
"v_mfma_f32_16x16x32_bf16 v[152:155], v[120:123], v[64:67], v[152:155]\n"
"ds_read_b64_tr_b16 v[42:43], v228 offset:1280\n"
"buffer_load_dwordx4 v241, s[24:27], s45 offen offset:1024 lds\n"
"v_cvt_pk_bf16_f32 v235, v238, v239\n"
"v_mfma_f32_16x16x32_bf16 v[156:159], v[124:127], v[64:67], v[156:159]\n"
"ds_read_b64_tr_b16 v[44:45], v229 offset:256\n"
"buffer_store_dwordx4 v[232:235], v252, s[28:31], s55 offen offset:2048 sc1\n"
"s_nop 1\n"
"v_accvgpr_read_b32 v232, a152\n"
"v_mfma_f32_16x16x32_bf16 v[188:191], v[124:127], v[68:71], v[188:191]\n"
"ds_read_b64_tr_b16 v[46:47], v229 offset:1280\n"
"buffer_load_dwordx4 v242, s[24:27], s45 offen offset:2048 lds\n"
"v_accvgpr_read_b32 v233, a153\n"
"v_accvgpr_read_b32 v234, a154\n"
"v_mfma_f32_16x16x32_bf16 v[184:187], v[120:123], v[68:71], v[184:187]\n"
"ds_read_b128 v[0:3], v230 offset:0\n"
"v_accvgpr_read_b32 v235, a155\n"
"v_accvgpr_read_b32 v236, a156\n"
"v_mfma_f32_16x16x32_bf16 v[180:183], v[116:119], v[68:71], v[180:183]\n"
"ds_read_b128 v[4:7], v230 offset:2048\n"
"buffer_load_dwordx4 v243, s[24:27], s45 offen offset:3072 lds\n"
"v_accvgpr_read_b32 v237, a157\n"
"v_mfma_f32_16x16x32_bf16 v[176:179], v[112:115], v[68:71], v[176:179]\n"
"ds_read_b64_tr_b16 v[48:49], v228 offset:512\n"
"s_add_u32 m0, s41, 81920\n"
"v_accvgpr_read_b32 v238, a158\n"
"v_accvgpr_read_b32 v239, a159\n"
"v_mfma_f32_16x16x32_bf16 v[172:175], v[108:111], v[68:71], v[172:175]\n"
"ds_read_b64_tr_b16 v[50:51], v228 offset:1536\n"
"s_mov_b32 s45, 640\n"
"v_cvt_pk_bf16_f32 v232, v232, v233\n"
"v_cvt_pk_bf16_f32 v233, v234, v235\n"
"v_mfma_f32_16x16x32_bf16 v[168:171], v[104:107], v[68:71], v[168:171]\n"
"ds_read_b64_tr_b16 v[52:53], v229 offset:512\n"
"buffer_load_dwordx4 v244, s[20:23], s45 offen offset:0 lds\n"
"v_cvt_pk_bf16_f32 v234, v236, v237\n"
"v_mfma_f32_16x16x32_bf16 v[164:167], v[100:103], v[68:71], v[164:167]\n"
"ds_read_b64_tr_b16 v[54:55], v229 offset:1536\n"
"v_cvt_pk_bf16_f32 v235, v238, v239\n"
"buffer_store_dwordx4 v[232:235], v252, s[28:31], s55 offen offset:3072 sc1\n"
"v_mfma_f32_16x16x32_bf16 v[160:163], v[96:99], v[68:71], v[160:163]\n"
"ds_read_b64_tr_b16 v[56:57], v228 offset:768\n"
"buffer_load_dwordx4 v245, s[20:23], s45 offen offset:1024 lds\n"
"v_accvgpr_read_b32 v232, a160\n"
"v_accvgpr_read_b32 v233, a161\n"
"v_mfma_f32_16x16x32_bf16 v[192:195], v[96:99], v[72:75], v[192:195]\n"
"ds_read_b64_tr_b16 v[58:59], v228 offset:1792\n"
"v_accvgpr_read_b32 v234, a162\n"
"v_accvgpr_read_b32 v235, a163\n"
"v_mfma_f32_16x16x32_bf16 v[196:199], v[100:103], v[72:75], v[196:199]\n"
"ds_read_b64_tr_b16 v[60:61], v229 offset:768\n"
"buffer_load_dwordx4 v246, s[20:23], s45 offen offset:2048 lds\n"
"v_accvgpr_read_b32 v236, a164\n"
"v_mfma_f32_16x16x32_bf16 v[200:203], v[104:107], v[72:75], v[200:203]\n"
"ds_read_b64_tr_b16 v[62:63], v229 offset:1792\n"
"v_accvgpr_read_b32 v237, a165\n"
"v_accvgpr_read_b32 v238, a166\n"
"v_mfma_f32_16x16x32_bf16 v[204:207], v[108:111], v[72:75], v[204:207]\n"
"ds_read_b128 v[8:11], v230 offset:4096\n"
"buffer_load_dwordx4 v247, s[20:23], s45 offen offset:3072 lds\n"
"v_accvgpr_read_b32 v239, a167\n"
"v_cvt_pk_bf16_f32 v232, v232, v233\n"
"v_mfma_f32_16x16x32_bf16 v[208:211], v[112:115], v[72:75], v[208:211]\n"
"ds_read_b128 v[12:15], v230 offset:6144\n"
"v_cvt_pk_bf16_f32 v233, v234, v235\n"
"v_mfma_f32_16x16x32_bf16 v[212:215], v[116:119], v[72:75], v[212:215]\n"
"v_cvt_pk_bf16_f32 v234, v236, v237\n"
"v_cvt_pk_bf16_f32 v235, v238, v239\n"
"v_mfma_f32_16x16x32_bf16 v[216:219], v[120:123], v[72:75], v[216:219]\n"
"s_add_u32 s55, s51, 65536\n"
"buffer_store_dwordx4 v[232:235], v252, s[28:31], s55 offen offset:0 sc1\n"
"v_mfma_f32_16x16x32_bf16 v[220:223], v[124:127], v[72:75], v[220:223]\n"
"s_nop 0\n"
"v_accvgpr_read_b32 v232, a168\n"
"v_accvgpr_read_b32 v233, a169\n"
"v_mfma_f32_16x16x32_bf16 v[88:91], v[124:127], v[76:79], v[88:91]\n"
"v_accvgpr_read_b32 v234, a170\n"
"v_mfma_f32_16x16x32_bf16 v[84:87], v[120:123], v[76:79], v[84:87]\n"
"v_accvgpr_read_b32 v235, a171\n"
"v_accvgpr_read_b32 v236, a172\n"
"v_mfma_f32_16x16x32_bf16 v[80:83], v[116:119], v[76:79], v[80:83]\n"
"v_accvgpr_read_b32 v237, a173\n"
"v_accvgpr_read_b32 v238, a174\n"
"v_mfma_f32_16x16x32_bf16 v[28:31], v[112:115], v[76:79], v[28:31]\n"
"v_accvgpr_read_b32 v239, a175\n"
"v_mfma_f32_16x16x32_bf16 v[24:27], v[108:111], v[76:79], v[24:27]\n"
"v_cvt_pk_bf16_f32 v232, v232, v233\n"
"v_cvt_pk_bf16_f32 v233, v234, v235\n"
"v_mfma_f32_16x16x32_bf16 v[20:23], v[104:107], v[76:79], v[20:23]\n"
"v_cvt_pk_bf16_f32 v234, v236, v237\n"
"v_cvt_pk_bf16_f32 v235, v238, v239\n"
"v_mfma_f32_16x16x32_bf16 v[16:19], v[100:103], v[76:79], v[16:19]\n"
"s_waitcnt vmcnt(49) lgkmcnt(0)\n"
"s_barrier\n"
"buffer_store_dwordx4 v[232:235], v252, s[28:31], s55 offen offset:1024 sc1\n"
"v_mfma_f32_16x16x32_bf16 v[224:227], v[96:99], v[76:79], v[224:227]\n"
"v_mfma_f32_16x16x32_bf16 v[128:131], v[32:35], v[0:3], v[128:131]\n"
"v_add_u32 v228, 81920, v250\n"
"v_add_u32 v229, 81920, v251\n"
"v_accvgpr_read_b32 v232, a176\n"
"v_accvgpr_read_b32 v233, a177\n"
"v_mfma_f32_16x16x32_bf16 v[132:135], v[36:39], v[0:3], v[132:135]\n"
"v_add_u32 v230, 131072, v249\n"
"ds_read_b64_tr_b16 v[96:97], v228 offset:0\n"
"v_accvgpr_read_b32 v234, a178\n"
"v_accvgpr_read_b32 v235, a179\n"
"v_mfma_f32_16x16x32_bf16 v[136:139], v[40:43], v[0:3], v[136:139]\n"
"ds_read_b64_tr_b16 v[98:99], v228 offset:1024\n"
"s_add_u32 m0, s42, 0\n"
"v_accvgpr_read_b32 v236, a180\n"
"v_accvgpr_read_b32 v237, a181\n"
"v_mfma_f32_16x16x32_bf16 v[140:143], v[44:47], v[0:3], v[140:143]\n"
"ds_read_b64_tr_b16 v[100:101], v229 offset:0\n"
"s_mov_b32 s45, 2621440\n"
"v_accvgpr_read_b32 v238, a182\n"
"v_mfma_f32_16x16x32_bf16 v[144:147], v[48:51], v[0:3], v[144:147]\n"
"ds_read_b64_tr_b16 v[102:103], v229 offset:1024\n"
"buffer_load_dwordx4 v240, s[24:27], s45 offen offset:0 lds\n"
"v_accvgpr_read_b32 v239, a183\n"
"v_cvt_pk_bf16_f32 v232, v232, v233\n"
"v_mfma_f32_16x16x32_bf16 v[148:151], v[52:55], v[0:3], v[148:151]\n"
"ds_read_b64_tr_b16 v[104:105], v228 offset:256\n"
"v_cvt_pk_bf16_f32 v233, v234, v235\n"
"v_cvt_pk_bf16_f32 v234, v236, v237\n"
"v_mfma_f32_16x16x32_bf16 v[152:155], v[56:59], v[0:3], v[152:155]\n"
"ds_read_b64_tr_b16 v[106:107], v228 offset:1280\n"
"buffer_load_dwordx4 v241, s[24:27], s45 offen offset:1024 lds\n"
"v_cvt_pk_bf16_f32 v235, v238, v239\n"
"v_mfma_f32_16x16x32_bf16 v[156:159], v[60:63], v[0:3], v[156:159]\n"
"ds_read_b64_tr_b16 v[108:109], v229 offset:256\n"
"buffer_store_dwordx4 v[232:235], v252, s[28:31], s55 offen offset:2048 sc1\n"
"s_nop 1\n"
"v_accvgpr_read_b32 v232, a184\n"
"v_mfma_f32_16x16x32_bf16 v[188:191], v[60:63], v[4:7], v[188:191]\n"
"ds_read_b64_tr_b16 v[110:111], v229 offset:1280\n"
"buffer_load_dwordx4 v242, s[24:27], s45 offen offset:2048 lds\n"
"v_accvgpr_read_b32 v233, a185\n"
"v_accvgpr_read_b32 v234, a186\n"
"v_mfma_f32_16x16x32_bf16 v[184:187], v[56:59], v[4:7], v[184:187]\n"
"ds_read_b128 v[64:67], v230 offset:0\n"
"v_accvgpr_read_b32 v235, a187\n"
"v_accvgpr_read_b32 v236, a188\n"
"v_mfma_f32_16x16x32_bf16 v[180:183], v[52:55], v[4:7], v[180:183]\n"
"ds_read_b128 v[68:71], v230 offset:2048\n"
"buffer_load_dwordx4 v243, s[24:27], s45 offen offset:3072 lds\n"
"v_accvgpr_read_b32 v237, a189\n"
"v_mfma_f32_16x16x32_bf16 v[176:179], v[48:51], v[4:7], v[176:179]\n"
"ds_read_b64_tr_b16 v[112:113], v228 offset:512\n"
"v_accvgpr_read_b32 v238, a190\n"
"v_accvgpr_read_b32 v239, a191\n"
"v_mfma_f32_16x16x32_bf16 v[172:175], v[44:47], v[4:7], v[172:175]\n"
"ds_read_b64_tr_b16 v[114:115], v228 offset:1536\n"
"v_cvt_pk_bf16_f32 v232, v232, v233\n"
"v_cvt_pk_bf16_f32 v233, v234, v235\n"
"v_mfma_f32_16x16x32_bf16 v[168:171], v[40:43], v[4:7], v[168:171]\n"
"ds_read_b64_tr_b16 v[116:117], v229 offset:512\n"
"v_cvt_pk_bf16_f32 v234, v236, v237\n"
"v_mfma_f32_16x16x32_bf16 v[164:167], v[36:39], v[4:7], v[164:167]\n"
"ds_read_b64_tr_b16 v[118:119], v229 offset:1536\n"
"v_cvt_pk_bf16_f32 v235, v238, v239\n"
"buffer_store_dwordx4 v[232:235], v252, s[28:31], s55 offen offset:3072 sc1\n"
"v_mfma_f32_16x16x32_bf16 v[160:163], v[32:35], v[4:7], v[160:163]\n"
"ds_read_b64_tr_b16 v[120:121], v228 offset:768\n"
"v_accvgpr_read_b32 v232, a192\n"
"v_accvgpr_read_b32 v233, a193\n"
"v_mfma_f32_16x16x32_bf16 v[192:195], v[32:35], v[8:11], v[192:195]\n"
"ds_read_b64_tr_b16 v[122:123], v228 offset:1792\n"
"v_accvgpr_read_b32 v234, a194\n"
"v_accvgpr_read_b32 v235, a195\n"
"v_mfma_f32_16x16x32_bf16 v[196:199], v[36:39], v[8:11], v[196:199]\n"
"ds_read_b64_tr_b16 v[124:125], v229 offset:768\n"
"v_accvgpr_read_b32 v236, a196\n"
"v_mfma_f32_16x16x32_bf16 v[200:203], v[40:43], v[8:11], v[200:203]\n"
"ds_read_b64_tr_b16 v[126:127], v229 offset:1792\n"
"v_accvgpr_read_b32 v237, a197\n"
"v_accvgpr_read_b32 v238, a198\n"
"v_mfma_f32_16x16x32_bf16 v[204:207], v[44:47], v[8:11], v[204:207]\n"
"ds_read_b128 v[72:75], v230 offset:4096\n"
"v_accvgpr_read_b32 v239, a199\n"
"v_cvt_pk_bf16_f32 v232, v232, v233\n"
"v_mfma_f32_16x16x32_bf16 v[208:211], v[48:51], v[8:11], v[208:211]\n"
"ds_read_b128 v[76:79], v230 offset:6144\n"
"v_cvt_pk_bf16_f32 v233, v234, v235\n"
"v_mfma_f32_16x16x32_bf16 v[212:215], v[52:55], v[8:11], v[212:215]\n"
"v_cvt_pk_bf16_f32 v234, v236, v237\n"
"v_cvt_pk_bf16_f32 v235, v238, v239\n"
"v_mfma_f32_16x16x32_bf16 v[216:219], v[56:59], v[8:11], v[216:219]\n"
"s_add_u32 s55, s52, 65536\n"
"buffer_store_dwordx4 v[232:235], v252, s[28:31], s55 offen offset:0 sc1\n"
"v_mfma_f32_16x16x32_bf16 v[220:223], v[60:63], v[8:11], v[220:223]\n"
"s_nop 0\n"
"v_accvgpr_read_b32 v232, a200\n"
"v_accvgpr_read_b32 v233, a201\n"
"v_mfma_f32_16x16x32_bf16 v[88:91], v[60:63], v[12:15], v[88:91]\n"
"v_accvgpr_read_b32 v234, a202\n"
"v_mfma_f32_16x16x32_bf16 v[84:87], v[56:59], v[12:15], v[84:87]\n"
"v_accvgpr_read_b32 v235, a203\n"
"v_accvgpr_read_b32 v236, a204\n"
"v_mfma_f32_16x16x32_bf16 v[80:83], v[52:55], v[12:15], v[80:83]\n"
"v_accvgpr_read_b32 v237, a205\n"
"v_accvgpr_read_b32 v238, a206\n"
"v_mfma_f32_16x16x32_bf16 v[28:31], v[48:51], v[12:15], v[28:31]\n"
"v_accvgpr_read_b32 v239, a207\n"
"v_mfma_f32_16x16x32_bf16 v[24:27], v[44:47], v[12:15], v[24:27]\n"
"v_cvt_pk_bf16_f32 v232, v232, v233\n"
"v_cvt_pk_bf16_f32 v233, v234, v235\n"
"v_mfma_f32_16x16x32_bf16 v[20:23], v[40:43], v[12:15], v[20:23]\n"
"v_cvt_pk_bf16_f32 v234, v236, v237\n"
"v_cvt_pk_bf16_f32 v235, v238, v239\n"
"v_mfma_f32_16x16x32_bf16 v[16:19], v[36:39], v[12:15], v[16:19]\n"
"s_waitcnt vmcnt(38) lgkmcnt(0)\n"
"s_barrier\n"
"buffer_store_dwordx4 v[232:235], v252, s[28:31], s55 offen offset:1024 sc1\n"
"v_mfma_f32_16x16x32_bf16 v[224:227], v[32:35], v[12:15], v[224:227]\n"
"v_mfma_f32_16x16x32_bf16 v[128:131], v[96:99], v[64:67], v[128:131]\n"
"v_add_u32 v228, 16384, v250\n"
"v_add_u32 v229, 16384, v251\n"
"v_accvgpr_read_b32 v232, a208\n"
"v_accvgpr_read_b32 v233, a209\n"
"v_mfma_f32_16x16x32_bf16 v[132:135], v[100:103], v[64:67], v[132:135]\n"
"v_add_u32 v230, 65536, v248\n"
"ds_read_b64_tr_b16 v[32:33], v228 offset:0\n"
"v_accvgpr_read_b32 v234, a210\n"
"v_mfma_f32_16x16x32_bf16 v[136:139], v[104:107], v[64:67], v[136:139]\n"
"ds_read_b64_tr_b16 v[34:35], v228 offset:1024\n"
"s_add_u32 m0, s42, 131072\n"
"v_accvgpr_read_b32 v235, a211\n"
"v_mfma_f32_16x16x32_bf16 v[140:143], v[108:111], v[64:67], v[140:143]\n"
"ds_read_b64_tr_b16 v[36:37], v229 offset:0\n"
"s_mov_b32 s45, 2883584\n"
"v_accvgpr_read_b32 v236, a212\n"
"v_accvgpr_read_b32 v237, a213\n"
"v_mfma_f32_16x16x32_bf16 v[144:147], v[112:115], v[64:67], v[144:147]\n"
"ds_read_b64_tr_b16 v[38:39], v229 offset:1024\n"
"buffer_load_dwordx4 v240, s[24:27], s45 offen offset:0 lds\n"
"v_accvgpr_read_b32 v238, a214\n"
"v_mfma_f32_16x16x32_bf16 v[148:151], v[116:119], v[64:67], v[148:151]\n"
"ds_read_b64_tr_b16 v[40:41], v228 offset:256\n"
"v_accvgpr_read_b32 v239, a215\n"
"v_mfma_f32_16x16x32_bf16 v[152:155], v[120:123], v[64:67], v[152:155]\n"
"ds_read_b64_tr_b16 v[42:43], v228 offset:1280\n"
"buffer_load_dwordx4 v241, s[24:27], s45 offen offset:1024 lds\n"
"v_cvt_pk_bf16_f32 v232, v232, v233\n"
"v_cvt_pk_bf16_f32 v233, v234, v235\n"
"v_mfma_f32_16x16x32_bf16 v[156:159], v[124:127], v[64:67], v[156:159]\n"
"ds_read_b64_tr_b16 v[44:45], v229 offset:256\n"
"v_cvt_pk_bf16_f32 v234, v236, v237\n"
"v_mfma_f32_16x16x32_bf16 v[188:191], v[124:127], v[68:71], v[188:191]\n"
"ds_read_b64_tr_b16 v[46:47], v229 offset:1280\n"
"buffer_load_dwordx4 v242, s[24:27], s45 offen offset:2048 lds\n"
"v_cvt_pk_bf16_f32 v235, v238, v239\n"
"v_mfma_f32_16x16x32_bf16 v[184:187], v[120:123], v[68:71], v[184:187]\n"
"ds_read_b128 v[0:3], v230 offset:0\n"
"buffer_store_dwordx4 v[232:235], v252, s[28:31], s55 offen offset:2048 sc1\n"
"v_mfma_f32_16x16x32_bf16 v[180:183], v[116:119], v[68:71], v[180:183]\n"
"ds_read_b128 v[4:7], v230 offset:2048\n"
"buffer_load_dwordx4 v243, s[24:27], s45 offen offset:3072 lds\n"
"v_accvgpr_read_b32 v232, a216\n"
"v_accvgpr_read_b32 v233, a217\n"
"v_mfma_f32_16x16x32_bf16 v[176:179], v[112:115], v[68:71], v[176:179]\n"
"ds_read_b64_tr_b16 v[48:49], v228 offset:512\n"
"s_add_u32 m0, s41, 16384\n"
"v_accvgpr_read_b32 v234, a218\n"
"v_mfma_f32_16x16x32_bf16 v[172:175], v[108:111], v[68:71], v[172:175]\n"
"ds_read_b64_tr_b16 v[50:51], v228 offset:1536\n"
"s_mov_b32 s45, 768\n"
"v_accvgpr_read_b32 v235, a219\n"
"v_mfma_f32_16x16x32_bf16 v[168:171], v[104:107], v[68:71], v[168:171]\n"
"ds_read_b64_tr_b16 v[52:53], v229 offset:512\n"
"buffer_load_dwordx4 v244, s[20:23], s45 offen offset:0 lds\n"
"v_accvgpr_read_b32 v236, a220\n"
"v_accvgpr_read_b32 v237, a221\n"
"v_mfma_f32_16x16x32_bf16 v[164:167], v[100:103], v[68:71], v[164:167]\n"
"ds_read_b64_tr_b16 v[54:55], v229 offset:1536\n"
"v_accvgpr_read_b32 v238, a222\n"
"v_mfma_f32_16x16x32_bf16 v[160:163], v[96:99], v[68:71], v[160:163]\n"
"ds_read_b64_tr_b16 v[56:57], v228 offset:768\n"
"buffer_load_dwordx4 v245, s[20:23], s45 offen offset:1024 lds\n"
"v_accvgpr_read_b32 v239, a223\n"
"v_mfma_f32_16x16x32_bf16 v[192:195], v[96:99], v[72:75], v[192:195]\n"
"ds_read_b64_tr_b16 v[58:59], v228 offset:1792\n"
"v_cvt_pk_bf16_f32 v232, v232, v233\n"
"v_mfma_f32_16x16x32_bf16 v[196:199], v[100:103], v[72:75], v[196:199]\n"
"ds_read_b64_tr_b16 v[60:61], v229 offset:768\n"
"buffer_load_dwordx4 v246, s[20:23], s45 offen offset:2048 lds\n"
"v_cvt_pk_bf16_f32 v233, v234, v235\n"
"v_cvt_pk_bf16_f32 v234, v236, v237\n"
"v_mfma_f32_16x16x32_bf16 v[200:203], v[104:107], v[72:75], v[200:203]\n"
"ds_read_b64_tr_b16 v[62:63], v229 offset:1792\n"
"v_cvt_pk_bf16_f32 v235, v238, v239\n"
"v_mfma_f32_16x16x32_bf16 v[204:207], v[108:111], v[72:75], v[204:207]\n"
"ds_read_b128 v[8:11], v230 offset:4096\n"
"buffer_load_dwordx4 v247, s[20:23], s45 offen offset:3072 lds\n"
"buffer_store_dwordx4 v[232:235], v252, s[28:31], s55 offen offset:3072 sc1\n"
"v_mfma_f32_16x16x32_bf16 v[208:211], v[112:115], v[72:75], v[208:211]\n"
"ds_read_b128 v[12:15], v230 offset:6144\n"
"v_accvgpr_read_b32 v232, a224\n"
"v_accvgpr_read_b32 v233, a225\n"
"v_mfma_f32_16x16x32_bf16 v[212:215], v[116:119], v[72:75], v[212:215]\n"
"v_accvgpr_read_b32 v234, a226\n"
"v_mfma_f32_16x16x32_bf16 v[216:219], v[120:123], v[72:75], v[216:219]\n"
"v_accvgpr_read_b32 v235, a227\n"
"v_mfma_f32_16x16x32_bf16 v[220:223], v[124:127], v[72:75], v[220:223]\n"
"v_accvgpr_read_b32 v236, a228\n"
"v_mfma_f32_16x16x32_bf16 v[88:91], v[124:127], v[76:79], v[88:91]\n"
"v_accvgpr_read_b32 v237, a229\n"
"v_accvgpr_read_b32 v238, a230\n"
"v_mfma_f32_16x16x32_bf16 v[84:87], v[120:123], v[76:79], v[84:87]\n"
"v_accvgpr_read_b32 v239, a231\n"
"v_mfma_f32_16x16x32_bf16 v[80:83], v[116:119], v[76:79], v[80:83]\n"
"v_cvt_pk_bf16_f32 v232, v232, v233\n"
"v_mfma_f32_16x16x32_bf16 v[28:31], v[112:115], v[76:79], v[28:31]\n"
"v_cvt_pk_bf16_f32 v233, v234, v235\n"
"v_cvt_pk_bf16_f32 v234, v236, v237\n"
"v_mfma_f32_16x16x32_bf16 v[24:27], v[108:111], v[76:79], v[24:27]\n"
"v_cvt_pk_bf16_f32 v235, v238, v239\n"
"v_mfma_f32_16x16x32_bf16 v[20:23], v[104:107], v[76:79], v[20:23]\n"
"s_add_u32 s55, s53, 65536\n"
"v_mfma_f32_16x16x32_bf16 v[16:19], v[100:103], v[76:79], v[16:19]\n"
"s_waitcnt vmcnt(44) lgkmcnt(0)\n"
"s_barrier\n"
"buffer_store_dwordx4 v[232:235], v252, s[28:31], s55 offen offset:0 sc1\n"
"v_mfma_f32_16x16x32_bf16 v[224:227], v[96:99], v[76:79], v[224:227]\n"
"v_mfma_f32_16x16x32_bf16 v[128:131], v[32:35], v[0:3], v[128:131]\n"
"v_add_u32 v228, 98304, v250\n"
"v_add_u32 v229, 98304, v251\n"
"v_mfma_f32_16x16x32_bf16 v[132:135], v[36:39], v[0:3], v[132:135]\n"
"v_add_u32 v230, 65536, v249\n"
"ds_read_b64_tr_b16 v[96:97], v228 offset:0\n"
"v_mfma_f32_16x16x32_bf16 v[136:139], v[40:43], v[0:3], v[136:139]\n"
"ds_read_b64_tr_b16 v[98:99], v228 offset:1024\n"
"v_accvgpr_read_b32 v232, a232\n"
"v_mfma_f32_16x16x32_bf16 v[140:143], v[44:47], v[0:3], v[140:143]\n"
"ds_read_b64_tr_b16 v[100:101], v229 offset:0\n"
"v_accvgpr_read_b32 v233, a233\n"
"v_mfma_f32_16x16x32_bf16 v[144:147], v[48:51], v[0:3], v[144:147]\n"
"ds_read_b64_tr_b16 v[102:103], v229 offset:1024\n"
"v_accvgpr_read_b32 v234, a234\n"
"v_mfma_f32_16x16x32_bf16 v[148:151], v[52:55], v[0:3], v[148:151]\n"
"ds_read_b64_tr_b16 v[104:105], v228 offset:256\n"
"v_accvgpr_read_b32 v235, a235\n"
"v_mfma_f32_16x16x32_bf16 v[152:155], v[56:59], v[0:3], v[152:155]\n"
"ds_read_b64_tr_b16 v[106:107], v228 offset:1280\n"
"v_accvgpr_read_b32 v236, a236\n"
"v_mfma_f32_16x16x32_bf16 v[156:159], v[60:63], v[0:3], v[156:159]\n"
"ds_read_b64_tr_b16 v[108:109], v229 offset:256\n"
"v_accvgpr_read_b32 v237, a237\n"
"v_mfma_f32_16x16x32_bf16 v[188:191], v[60:63], v[4:7], v[188:191]\n"
"ds_read_b64_tr_b16 v[110:111], v229 offset:1280\n"
"v_accvgpr_read_b32 v238, a238\n"
"v_mfma_f32_16x16x32_bf16 v[184:187], v[56:59], v[4:7], v[184:187]\n"
"ds_read_b128 v[64:67], v230 offset:0\n"
"v_accvgpr_read_b32 v239, a239\n"
"v_mfma_f32_16x16x32_bf16 v[180:183], v[52:55], v[4:7], v[180:183]\n"
"ds_read_b128 v[68:71], v230 offset:2048\n"
"v_cvt_pk_bf16_f32 v232, v232, v233\n"
"v_mfma_f32_16x16x32_bf16 v[176:179], v[48:51], v[4:7], v[176:179]\n"
"ds_read_b64_tr_b16 v[112:113], v228 offset:512\n"
"v_cvt_pk_bf16_f32 v233, v234, v235\n"
"v_mfma_f32_16x16x32_bf16 v[172:175], v[44:47], v[4:7], v[172:175]\n"
"ds_read_b64_tr_b16 v[114:115], v228 offset:1536\n"
"v_cvt_pk_bf16_f32 v234, v236, v237\n"
"v_mfma_f32_16x16x32_bf16 v[168:171], v[40:43], v[4:7], v[168:171]\n"
"ds_read_b64_tr_b16 v[116:117], v229 offset:512\n"
"v_cvt_pk_bf16_f32 v235, v238, v239\n"
"v_mfma_f32_16x16x32_bf16 v[164:167], v[36:39], v[4:7], v[164:167]\n"
"ds_read_b64_tr_b16 v[118:119], v229 offset:1536\n"
"buffer_store_dwordx4 v[232:235], v252, s[28:31], s55 offen offset:1024 sc1\n"
"v_mfma_f32_16x16x32_bf16 v[160:163], v[32:35], v[4:7], v[160:163]\n"
"ds_read_b64_tr_b16 v[120:121], v228 offset:768\n"
"v_accvgpr_read_b32 v232, a240\n"
"v_mfma_f32_16x16x32_bf16 v[192:195], v[32:35], v[8:11], v[192:195]\n"
"ds_read_b64_tr_b16 v[122:123], v228 offset:1792\n"
"v_accvgpr_read_b32 v233, a241\n"
"v_mfma_f32_16x16x32_bf16 v[196:199], v[36:39], v[8:11], v[196:199]\n"
"ds_read_b64_tr_b16 v[124:125], v229 offset:768\n"
"v_accvgpr_read_b32 v234, a242\n"
"v_mfma_f32_16x16x32_bf16 v[200:203], v[40:43], v[8:11], v[200:203]\n"
"ds_read_b64_tr_b16 v[126:127], v229 offset:1792\n"
"v_accvgpr_read_b32 v235, a243\n"
"v_mfma_f32_16x16x32_bf16 v[204:207], v[44:47], v[8:11], v[204:207]\n"
"ds_read_b128 v[72:75], v230 offset:4096\n"
"v_accvgpr_read_b32 v236, a244\n"
"v_mfma_f32_16x16x32_bf16 v[208:211], v[48:51], v[8:11], v[208:211]\n"
"ds_read_b128 v[76:79], v230 offset:6144\n"
"v_accvgpr_read_b32 v237, a245\n"
"v_mfma_f32_16x16x32_bf16 v[212:215], v[52:55], v[8:11], v[212:215]\n"
"v_accvgpr_read_b32 v238, a246\n"
"v_accvgpr_read_b32 v239, a247\n"
"v_mfma_f32_16x16x32_bf16 v[216:219], v[56:59], v[8:11], v[216:219]\n"
"v_cvt_pk_bf16_f32 v232, v232, v233\n"
"v_cvt_pk_bf16_f32 v233, v234, v235\n"
"v_mfma_f32_16x16x32_bf16 v[220:223], v[60:63], v[8:11], v[220:223]\n"
"v_cvt_pk_bf16_f32 v234, v236, v237\n"
"v_cvt_pk_bf16_f32 v235, v238, v239\n"
"v_mfma_f32_16x16x32_bf16 v[88:91], v[60:63], v[12:15], v[88:91]\n"
"buffer_store_dwordx4 v[232:235], v252, s[28:31], s55 offen offset:2048 sc1\n"
"s_nop 1\n"
"v_accvgpr_read_b32 v232, a248\n"
"v_mfma_f32_16x16x32_bf16 v[84:87], v[56:59], v[12:15], v[84:87]\n"
"v_accvgpr_read_b32 v233, a249\n"
"v_accvgpr_read_b32 v234, a250\n"
"v_mfma_f32_16x16x32_bf16 v[80:83], v[52:55], v[12:15], v[80:83]\n"
"v_accvgpr_read_b32 v235, a251\n"
"v_accvgpr_read_b32 v236, a252\n"
"v_mfma_f32_16x16x32_bf16 v[28:31], v[48:51], v[12:15], v[28:31]\n"
"v_accvgpr_read_b32 v237, a253\n"
"v_accvgpr_read_b32 v238, a254\n"
"v_mfma_f32_16x16x32_bf16 v[24:27], v[44:47], v[12:15], v[24:27]\n"
"v_accvgpr_read_b32 v239, a255\n"
"v_cvt_pk_bf16_f32 v232, v232, v233\n"
"v_mfma_f32_16x16x32_bf16 v[20:23], v[40:43], v[12:15], v[20:23]\n"
"v_cvt_pk_bf16_f32 v233, v234, v235\n"
"v_cvt_pk_bf16_f32 v234, v236, v237\n"
"v_mfma_f32_16x16x32_bf16 v[16:19], v[36:39], v[12:15], v[16:19]\n"
"s_waitcnt vmcnt(36) lgkmcnt(0)\n"
"s_barrier\n"
"v_mfma_f32_16x16x32_bf16 v[224:227], v[32:35], v[12:15], v[224:227]\n"
"v_cvt_pk_bf16_f32 v235, v238, v239\n"
"buffer_store_dwordx4 v[232:235], v252, s[28:31], s55 offen offset:3072 sc1\n"
"v_mfma_f32_16x16x32_bf16 v[128:131], v[96:99], v[64:67], v[128:131]\n"
"v_add_u32 v228, 32768, v250\n"
"v_add_u32 v229, 32768, v251\n"
"v_mfma_f32_16x16x32_bf16 v[132:135], v[100:103], v[64:67], v[132:135]\n"
"v_add_u32 v230, 114688, v248\n"
"ds_read_b64_tr_b16 v[32:33], v228 offset:0\n"
"v_mfma_f32_16x16x32_bf16 v[136:139], v[104:107], v[64:67], v[136:139]\n"
"ds_read_b64_tr_b16 v[34:35], v228 offset:1024\n"
"s_add_u32 m0, s42, 98304\n"
"v_mfma_f32_16x16x32_bf16 v[140:143], v[108:111], v[64:67], v[140:143]\n"
"ds_read_b64_tr_b16 v[36:37], v229 offset:0\n"
"s_mov_b32 s45, 3145728\n"
"v_mfma_f32_16x16x32_bf16 v[144:147], v[112:115], v[64:67], v[144:147]\n"
"ds_read_b64_tr_b16 v[38:39], v229 offset:1024\n"
"buffer_load_dwordx4 v240, s[24:27], s45 offen offset:0 lds\n"
"v_mfma_f32_16x16x32_bf16 v[148:151], v[116:119], v[64:67], v[148:151]\n"
"ds_read_b64_tr_b16 v[40:41], v228 offset:256\n"
"v_mfma_f32_16x16x32_bf16 v[152:155], v[120:123], v[64:67], v[152:155]\n"
"ds_read_b64_tr_b16 v[42:43], v228 offset:1280\n"
"buffer_load_dwordx4 v241, s[24:27], s45 offen offset:1024 lds\n"
"v_mfma_f32_16x16x32_bf16 v[156:159], v[124:127], v[64:67], v[156:159]\n"
"ds_read_b64_tr_b16 v[44:45], v229 offset:256\n"
"v_mfma_f32_16x16x32_bf16 v[188:191], v[124:127], v[68:71], v[188:191]\n"
"ds_read_b64_tr_b16 v[46:47], v229 offset:1280\n"
"buffer_load_dwordx4 v242, s[24:27], s45 offen offset:2048 lds\n"
"v_mfma_f32_16x16x32_bf16 v[184:187], v[120:123], v[68:71], v[184:187]\n"
"ds_read_b128 v[0:3], v230 offset:0\n"
"v_mfma_f32_16x16x32_bf16 v[180:183], v[116:119], v[68:71], v[180:183]\n"
"ds_read_b128 v[4:7], v230 offset:2048\n"
"buffer_load_dwordx4 v243, s[24:27], s45 offen offset:3072 lds\n"
"v_mfma_f32_16x16x32_bf16 v[176:179], v[112:115], v[68:71], v[176:179]\n"
"ds_read_b64_tr_b16 v[48:49], v228 offset:512\n"
"s_add_u32 m0, s42, 65536\n"
"v_mfma_f32_16x16x32_bf16 v[172:175], v[108:111], v[68:71], v[172:175]\n"
"ds_read_b64_tr_b16 v[50:51], v228 offset:1536\n"
"s_mov_b32 s45, 3407872\n"
"v_mfma_f32_16x16x32_bf16 v[168:171], v[104:107], v[68:71], v[168:171]\n"
"ds_read_b64_tr_b16 v[52:53], v229 offset:512\n"
"buffer_load_dwordx4 v240, s[24:27], s45 offen offset:0 lds\n"
"v_mfma_f32_16x16x32_bf16 v[164:167], v[100:103], v[68:71], v[164:167]\n"
"ds_read_b64_tr_b16 v[54:55], v229 offset:1536\n"
"v_mfma_f32_16x16x32_bf16 v[160:163], v[96:99], v[68:71], v[160:163]\n"
"ds_read_b64_tr_b16 v[56:57], v228 offset:768\n"
"buffer_load_dwordx4 v241, s[24:27], s45 offen offset:1024 lds\n"
"v_mfma_f32_16x16x32_bf16 v[192:195], v[96:99], v[72:75], v[192:195]\n"
"ds_read_b64_tr_b16 v[58:59], v228 offset:1792\n"
"v_mfma_f32_16x16x32_bf16 v[196:199], v[100:103], v[72:75], v[196:199]\n"
"ds_read_b64_tr_b16 v[60:61], v229 offset:768\n"
"buffer_load_dwordx4 v242, s[24:27], s45 offen offset:2048 lds\n"
"v_mfma_f32_16x16x32_bf16 v[200:203], v[104:107], v[72:75], v[200:203]\n"
"ds_read_b64_tr_b16 v[62:63], v229 offset:1792\n"
"v_mfma_f32_16x16x32_bf16 v[204:207], v[108:111], v[72:75], v[204:207]\n"
"ds_read_b128 v[8:11], v230 offset:4096\n"
"buffer_load_dwordx4 v243, s[24:27], s45 offen offset:3072 lds\n"
"v_mfma_f32_16x16x32_bf16 v[208:211], v[112:115], v[72:75], v[208:211]\n"
"ds_read_b128 v[12:15], v230 offset:6144\n"
"v_mfma_f32_16x16x32_bf16 v[212:215], v[116:119], v[72:75], v[212:215]\n"
"v_mfma_f32_16x16x32_bf16 v[216:219], v[120:123], v[72:75], v[216:219]\n"
"v_mfma_f32_16x16x32_bf16 v[220:223], v[124:127], v[72:75], v[220:223]\n"
"v_mfma_f32_16x16x32_bf16 v[88:91], v[124:127], v[76:79], v[88:91]\n"
"v_mfma_f32_16x16x32_bf16 v[84:87], v[120:123], v[76:79], v[84:87]\n"
"v_mfma_f32_16x16x32_bf16 v[80:83], v[116:119], v[76:79], v[80:83]\n"
"v_mfma_f32_16x16x32_bf16 v[28:31], v[112:115], v[76:79], v[28:31]\n"
"v_mfma_f32_16x16x32_bf16 v[24:27], v[108:111], v[76:79], v[24:27]\n"
"v_mfma_f32_16x16x32_bf16 v[20:23], v[104:107], v[76:79], v[20:23]\n"
"v_mfma_f32_16x16x32_bf16 v[16:19], v[100:103], v[76:79], v[16:19]\n"
"s_waitcnt vmcnt(37) lgkmcnt(0)\n"
"s_barrier\n"
"v_mfma_f32_16x16x32_bf16 v[224:227], v[96:99], v[76:79], v[224:227]\n"
"v_mfma_f32_16x16x32_bf16 v[128:131], v[32:35], v[0:3], v[128:131]\n"
"v_add_u32 v228, 49152, v250\n"
"v_add_u32 v229, 49152, v251\n"
"v_mfma_f32_16x16x32_bf16 v[132:135], v[36:39], v[0:3], v[132:135]\n"
"v_add_u32 v230, 114688, v249\n"
"ds_read_b64_tr_b16 v[96:97], v228 offset:0\n"
"v_mfma_f32_16x16x32_bf16 v[136:139], v[40:43], v[0:3], v[136:139]\n"
"ds_read_b64_tr_b16 v[98:99], v228 offset:1024\n"
"v_mfma_f32_16x16x32_bf16 v[140:143], v[44:47], v[0:3], v[140:143]\n"
"ds_read_b64_tr_b16 v[100:101], v229 offset:0\n"
"v_mfma_f32_16x16x32_bf16 v[144:147], v[48:51], v[0:3], v[144:147]\n"
"ds_read_b64_tr_b16 v[102:103], v229 offset:1024\n"
"v_mfma_f32_16x16x32_bf16 v[148:151], v[52:55], v[0:3], v[148:151]\n"
"ds_read_b64_tr_b16 v[104:105], v228 offset:256\n"
"v_mfma_f32_16x16x32_bf16 v[152:155], v[56:59], v[0:3], v[152:155]\n"
"ds_read_b64_tr_b16 v[106:107], v228 offset:1280\n"
"v_mfma_f32_16x16x32_bf16 v[156:159], v[60:63], v[0:3], v[156:159]\n"
"ds_read_b64_tr_b16 v[108:109], v229 offset:256\n"
"v_mfma_f32_16x16x32_bf16 v[188:191], v[60:63], v[4:7], v[188:191]\n"
"ds_read_b64_tr_b16 v[110:111], v229 offset:1280\n"
"v_mfma_f32_16x16x32_bf16 v[184:187], v[56:59], v[4:7], v[184:187]\n"
"ds_read_b128 v[64:67], v230 offset:0\n"
"v_mfma_f32_16x16x32_bf16 v[180:183], v[52:55], v[4:7], v[180:183]\n"
"ds_read_b128 v[68:71], v230 offset:2048\n"
"v_mfma_f32_16x16x32_bf16 v[176:179], v[48:51], v[4:7], v[176:179]\n"
"ds_read_b64_tr_b16 v[112:113], v228 offset:512\n"
"v_mfma_f32_16x16x32_bf16 v[172:175], v[44:47], v[4:7], v[172:175]\n"
"ds_read_b64_tr_b16 v[114:115], v228 offset:1536\n"
"v_mfma_f32_16x16x32_bf16 v[168:171], v[40:43], v[4:7], v[168:171]\n"
"ds_read_b64_tr_b16 v[116:117], v229 offset:512\n"
"v_mfma_f32_16x16x32_bf16 v[164:167], v[36:39], v[4:7], v[164:167]\n"
"ds_read_b64_tr_b16 v[118:119], v229 offset:1536\n"
"v_mfma_f32_16x16x32_bf16 v[160:163], v[32:35], v[4:7], v[160:163]\n"
"ds_read_b64_tr_b16 v[120:121], v228 offset:768\n"
"v_mfma_f32_16x16x32_bf16 v[192:195], v[32:35], v[8:11], v[192:195]\n"
"ds_read_b64_tr_b16 v[122:123], v228 offset:1792\n"
"v_mfma_f32_16x16x32_bf16 v[196:199], v[36:39], v[8:11], v[196:199]\n"
"ds_read_b64_tr_b16 v[124:125], v229 offset:768\n"
"v_mfma_f32_16x16x32_bf16 v[200:203], v[40:43], v[8:11], v[200:203]\n"
"ds_read_b64_tr_b16 v[126:127], v229 offset:1792\n"
"v_mfma_f32_16x16x32_bf16 v[204:207], v[44:47], v[8:11], v[204:207]\n"
"ds_read_b128 v[72:75], v230 offset:4096\n"
"v_mfma_f32_16x16x32_bf16 v[208:211], v[48:51], v[8:11], v[208:211]\n"
"ds_read_b128 v[76:79], v230 offset:6144\n"
"v_mfma_f32_16x16x32_bf16 v[212:215], v[52:55], v[8:11], v[212:215]\n"
"v_mfma_f32_16x16x32_bf16 v[216:219], v[56:59], v[8:11], v[216:219]\n"
"v_mfma_f32_16x16x32_bf16 v[220:223], v[60:63], v[8:11], v[220:223]\n"
"v_mfma_f32_16x16x32_bf16 v[88:91], v[60:63], v[12:15], v[88:91]\n"
"v_mfma_f32_16x16x32_bf16 v[84:87], v[56:59], v[12:15], v[84:87]\n"
"v_mfma_f32_16x16x32_bf16 v[80:83], v[52:55], v[12:15], v[80:83]\n"
"v_mfma_f32_16x16x32_bf16 v[28:31], v[48:51], v[12:15], v[28:31]\n"
"v_mfma_f32_16x16x32_bf16 v[24:27], v[44:47], v[12:15], v[24:27]\n"
"v_mfma_f32_16x16x32_bf16 v[20:23], v[40:43], v[12:15], v[20:23]\n"
"v_mfma_f32_16x16x32_bf16 v[16:19], v[36:39], v[12:15], v[16:19]\n"
"s_waitcnt vmcnt(25) lgkmcnt(0)\n"
"s_barrier\n"
"v_mfma_f32_16x16x32_bf16 v[224:227], v[32:35], v[12:15], v[224:227]\n"
"v_mfma_f32_16x16x32_bf16 v[128:131], v[96:99], v[64:67], v[128:131]\n"
"v_add_u32 v228, 0, v250\n"
"v_add_u32 v229, 0, v251\n"
"v_mfma_f32_16x16x32_bf16 v[132:135], v[100:103], v[64:67], v[132:135]\n"
"v_add_u32 v230, 147456, v248\n"
"ds_read_b64_tr_b16 v[32:33], v228 offset:0\n"
"v_mfma_f32_16x16x32_bf16 v[136:139], v[104:107], v[64:67], v[136:139]\n"
"ds_read_b64_tr_b16 v[34:35], v228 offset:1024\n"
"v_readlane_b32 s68, v255, 28\n"
"v_mfma_f32_16x16x32_bf16 v[140:143], v[108:111], v[64:67], v[140:143]\n"
"ds_read_b64_tr_b16 v[36:37], v229 offset:0\n"
"v_readlane_b32 s69, v255, 29\n"
"v_mfma_f32_16x16x32_bf16 v[144:147], v[112:115], v[64:67], v[144:147]\n"
"ds_read_b64_tr_b16 v[38:39], v229 offset:1024\n"
"v_readlane_b32 s70, v255, 30\n"
"v_mfma_f32_16x16x32_bf16 v[148:151], v[116:119], v[64:67], v[148:151]\n"
"ds_read_b64_tr_b16 v[40:41], v228 offset:256\n"
"v_readlane_b32 s72, v255, 31\n"
"v_mfma_f32_16x16x32_bf16 v[152:155], v[120:123], v[64:67], v[152:155]\n"
"ds_read_b64_tr_b16 v[42:43], v228 offset:1280\n"
"s_mov_b32 s71, 0x20000\n"
"v_mfma_f32_16x16x32_bf16 v[156:159], v[124:127], v[64:67], v[156:159]\n"
"ds_read_b64_tr_b16 v[44:45], v229 offset:256\n"
"s_lshr_b32 s73, s46, 1\n"
"v_mfma_f32_16x16x32_bf16 v[188:191], v[124:127], v[68:71], v[188:191]\n"
"ds_read_b64_tr_b16 v[46:47], v229 offset:1280\n"
"s_mul_i32 s58, s73, 27648\n"
"v_mfma_f32_16x16x32_bf16 v[184:187], v[120:123], v[68:71], v[184:187]\n"
"ds_read_b128 v[0:3], v230 offset:0\n"
"s_add_u32 s58, s58, s48\n"
"v_mfma_f32_16x16x32_bf16 v[180:183], v[116:119], v[68:71], v[180:183]\n"
"ds_read_b128 v[4:7], v230 offset:2048\n"
"s_and_b32 s75, s46, 1\n"
"v_mfma_f32_16x16x32_bf16 v[176:179], v[112:115], v[68:71], v[176:179]\n"
"ds_read_b64_tr_b16 v[48:49], v228 offset:512\n"
"v_mbcnt_lo_u32_b32 v246, -1, 0\n"
"v_mfma_f32_16x16x32_bf16 v[172:175], v[108:111], v[68:71], v[172:175]\n"
"ds_read_b64_tr_b16 v[50:51], v228 offset:1536\n"
"v_mbcnt_hi_u32_b32 v246, -1, v246\n"
"v_mfma_f32_16x16x32_bf16 v[168:171], v[104:107], v[68:71], v[168:171]\n"
"ds_read_b64_tr_b16 v[52:53], v229 offset:512\n"
"v_lshlrev_b32 v245, 6, v246\n"
"v_mfma_f32_16x16x32_bf16 v[164:167], v[100:103], v[68:71], v[164:167]\n"
"ds_read_b64_tr_b16 v[54:55], v229 offset:1536\n"
"v_sub_u32 v245, 4032, v245\n"
"v_mfma_f32_16x16x32_bf16 v[160:163], v[96:99], v[68:71], v[160:163]\n"
"ds_read_b64_tr_b16 v[56:57], v228 offset:768\n"
"s_mul_i32 s59, s75, 5120\n"
"v_mfma_f32_16x16x32_bf16 v[192:195], v[96:99], v[72:75], v[192:195]\n"
"ds_read_b64_tr_b16 v[58:59], v228 offset:1792\n"
"s_add_u32 s59, s59, s58\n"
"v_mfma_f32_16x16x32_bf16 v[196:199], v[100:103], v[72:75], v[196:199]\n"
"ds_read_b64_tr_b16 v[60:61], v229 offset:768\n"
"s_add_u32 s59, s59, 17408\n"
"v_mfma_f32_16x16x32_bf16 v[200:203], v[104:107], v[72:75], v[200:203]\n"
"ds_read_b64_tr_b16 v[62:63], v229 offset:1792\n"
"s_lshl_b32 s73, s75, 11\n"
"v_mfma_f32_16x16x32_bf16 v[204:207], v[108:111], v[72:75], v[204:207]\n"
"ds_read_b128 v[8:11], v230 offset:4096\n"
"v_lshl_add_u32 v244, v246, 2, s73\n"
"v_mfma_f32_16x16x32_bf16 v[208:211], v[112:115], v[72:75], v[208:211]\n"
"ds_read_b128 v[12:15], v230 offset:6144\n"
"v_subrev_u32 v244, 60, v244\n"
"v_mfma_f32_16x16x32_bf16 v[212:215], v[116:119], v[72:75], v[212:215]\n"
"v_mov_b32 v246, 0\n"
"s_lshl_b32 s73, s75, 13\n"
"v_mfma_f32_16x16x32_bf16 v[216:219], v[120:123], v[72:75], v[216:219]\n"
"s_add_u32 s73, s73, s58\n"
"v_mfma_f32_16x16x32_bf16 v[220:223], v[124:127], v[72:75], v[220:223]\n"
"v_mfma_f32_16x16x32_bf16 v[88:91], v[124:127], v[76:79], v[88:91]\n"
"v_mfma_f32_16x16x32_bf16 v[84:87], v[120:123], v[76:79], v[84:87]\n"
"v_mfma_f32_16x16x32_bf16 v[80:83], v[116:119], v[76:79], v[80:83]\n"
"v_mfma_f32_16x16x32_bf16 v[28:31], v[112:115], v[76:79], v[28:31]\n"
"v_mfma_f32_16x16x32_bf16 v[24:27], v[108:111], v[76:79], v[24:27]\n"
"v_mfma_f32_16x16x32_bf16 v[20:23], v[104:107], v[76:79], v[20:23]\n"
"v_mfma_f32_16x16x32_bf16 v[16:19], v[100:103], v[76:79], v[16:19]\n"
"s_waitcnt vmcnt(17) lgkmcnt(0)\n"
"s_barrier\n"
"v_mfma_f32_16x16x32_bf16 v[224:227], v[96:99], v[76:79], v[224:227]\n"
"v_mfma_f32_16x16x32_bf16 v[128:131], v[32:35], v[0:3], v[128:131]\n"
"v_add_u32 v228, 131072, v250\n"
"v_add_u32 v229, 131072, v251\n"
"v_mfma_f32_16x16x32_bf16 v[132:135], v[36:39], v[0:3], v[132:135]\n"
"v_add_u32 v230, 147456, v249\n"
"ds_read_b64_tr_b16 v[96:97], v228 offset:0\n"
"v_mfma_f32_16x16x32_bf16 v[136:139], v[40:43], v[0:3], v[136:139]\n"
"ds_read_b64_tr_b16 v[98:99], v228 offset:1024\n"
"s_add_u32 m0, s59, 256\n"
"v_mfma_f32_16x16x32_bf16 v[140:143], v[44:47], v[0:3], v[140:143]\n"
"ds_read_b64_tr_b16 v[100:101], v229 offset:0\n"
"s_add_u32 s74, s72, 48\n"
"v_mfma_f32_16x16x32_bf16 v[144:147], v[48:51], v[0:3], v[144:147]\n"
"ds_read_b64_tr_b16 v[102:103], v229 offset:1024\n"
"buffer_load_dwordx4 v245, s[28:31], s74 offen offset:0 sc1 lds\n"
"v_mfma_f32_16x16x32_bf16 v[148:151], v[52:55], v[0:3], v[148:151]\n"
"ds_read_b64_tr_b16 v[104:105], v228 offset:256\n"
"s_add_u32 m0, s59, 1536\n"
"v_mfma_f32_16x16x32_bf16 v[152:155], v[56:59], v[0:3], v[152:155]\n"
"ds_read_b64_tr_b16 v[106:107], v228 offset:1280\n"
"s_add_u32 s74, s72, 32\n"
"v_mfma_f32_16x16x32_bf16 v[156:159], v[60:63], v[0:3], v[156:159]\n"
"ds_read_b64_tr_b16 v[108:109], v229 offset:256\n"
"buffer_load_dwordx4 v245, s[28:31], s74 offen offset:0 sc1 lds\n"
"v_mfma_f32_16x16x32_bf16 v[188:191], v[60:63], v[4:7], v[188:191]\n"
"ds_read_b64_tr_b16 v[110:111], v229 offset:1280\n"
"s_add_u32 m0, s59, 2816\n"
"v_mfma_f32_16x16x32_bf16 v[184:187], v[56:59], v[4:7], v[184:187]\n"
"ds_read_b128 v[64:67], v230 offset:0\n"
"s_add_u32 s74, s72, 16\n"
"v_mfma_f32_16x16x32_bf16 v[180:183], v[52:55], v[4:7], v[180:183]\n"
"ds_read_b128 v[68:71], v230 offset:2048\n"
"buffer_load_dwordx4 v245, s[28:31], s74 offen offset:0 sc1 lds\n"
"v_mfma_f32_16x16x32_bf16 v[176:179], v[48:51], v[4:7], v[176:179]\n"
"ds_read_b64_tr_b16 v[112:113], v228 offset:512\n"
"s_add_u32 m0, s59, 4096\n"
"v_mfma_f32_16x16x32_bf16 v[172:175], v[44:47], v[4:7], v[172:175]\n"
"ds_read_b64_tr_b16 v[114:115], v228 offset:1536\n"
"s_add_u32 s74, s72, 0\n"
"v_mfma_f32_16x16x32_bf16 v[168:171], v[40:43], v[4:7], v[168:171]\n"
"ds_read_b64_tr_b16 v[116:117], v229 offset:512\n"
"buffer_load_dwordx4 v245, s[28:31], s74 offen offset:0 sc1 lds\n"
"v_mfma_f32_16x16x32_bf16 v[164:167], v[36:39], v[4:7], v[164:167]\n"
"ds_read_b64_tr_b16 v[118:119], v229 offset:1536\n"
"s_add_u32 m0, s73, 0\n"
"v_mfma_f32_16x16x32_bf16 v[160:163], v[32:35], v[4:7], v[160:163]\n"
"ds_read_b64_tr_b16 v[120:121], v228 offset:768\n"
"s_nop 0\n"
"v_mfma_f32_16x16x32_bf16 v[192:195], v[32:35], v[8:11], v[192:195]\n"
"ds_read_b64_tr_b16 v[122:123], v228 offset:1792\n"
"buffer_load_dwordx4 v244, s[68:71], 0 offen offset:0 lds\n"
"v_mfma_f32_16x16x32_bf16 v[196:199], v[36:39], v[8:11], v[196:199]\n"
"ds_read_b64_tr_b16 v[124:125], v229 offset:768\n"
"s_add_u32 m0, s73, 1024\n"
"v_mfma_f32_16x16x32_bf16 v[200:203], v[40:43], v[8:11], v[200:203]\n"
"ds_read_b64_tr_b16 v[126:127], v229 offset:1792\n"
"v_add_u32 v244, 256, v244\n"
"v_mfma_f32_16x16x32_bf16 v[204:207], v[44:47], v[8:11], v[204:207]\n"
"ds_read_b128 v[72:75], v230 offset:4096\n"
"buffer_load_dwordx4 v244, s[68:71], 0 offen offset:0 lds\n"
"v_mfma_f32_16x16x32_bf16 v[208:211], v[48:51], v[8:11], v[208:211]\n"
"ds_read_b128 v[76:79], v230 offset:6144\n"
"s_add_u32 m0, s73, 2048\n"
"v_mfma_f32_16x16x32_bf16 v[212:215], v[52:55], v[8:11], v[212:215]\n"
"v_add_u32 v244, 256, v244\n"
"buffer_load_dwordx4 v244, s[68:71], 0 offen offset:0 lds\n"
"v_mfma_f32_16x16x32_bf16 v[216:219], v[56:59], v[8:11], v[216:219]\n"
"v_mfma_f32_16x16x32_bf16 v[220:223], v[60:63], v[8:11], v[220:223]\n"
"v_mfma_f32_16x16x32_bf16 v[88:91], v[60:63], v[12:15], v[88:91]\n"
"v_mfma_f32_16x16x32_bf16 v[84:87], v[56:59], v[12:15], v[84:87]\n"
"v_mfma_f32_16x16x32_bf16 v[80:83], v[52:55], v[12:15], v[80:83]\n"
"v_mfma_f32_16x16x32_bf16 v[28:31], v[48:51], v[12:15], v[28:31]\n"
"v_mfma_f32_16x16x32_bf16 v[24:27], v[44:47], v[12:15], v[24:27]\n"
"v_mfma_f32_16x16x32_bf16 v[20:23], v[40:43], v[12:15], v[20:23]\n"
"v_mfma_f32_16x16x32_bf16 v[16:19], v[36:39], v[12:15], v[16:19]\n"
"s_waitcnt vmcnt(11) lgkmcnt(0)\n"
"s_barrier\n"
"v_mfma_f32_16x16x32_bf16 v[224:227], v[32:35], v[12:15], v[224:227]\n"
"v_mfma_f32_16x16x32_bf16 v[128:131], v[96:99], v[64:67], v[128:131]\n"
"v_add_u32 v228, 98304, v250\n"
"v_add_u32 v229, 98304, v251\n"
"v_mfma_f32_16x16x32_bf16 v[132:135], v[100:103], v[64:67], v[132:135]\n"
"v_add_u32 v230, 81920, v248\n"
"ds_read_b64_tr_b16 v[32:33], v228 offset:0\n"
"v_mfma_f32_16x16x32_bf16 v[136:139], v[104:107], v[64:67], v[136:139]\n"
"ds_read_b64_tr_b16 v[34:35], v228 offset:1024\n"
"s_add_u32 m0, s73, 3072\n"
"v_mfma_f32_16x16x32_bf16 v[140:143], v[108:111], v[64:67], v[140:143]\n"
"ds_read_b64_tr_b16 v[36:37], v229 offset:0\n"
"v_add_u32 v244, 256, v244\n"
"v_mfma_f32_16x16x32_bf16 v[144:147], v[112:115], v[64:67], v[144:147]\n"
"ds_read_b64_tr_b16 v[38:39], v229 offset:1024\n"
"buffer_load_dwordx4 v244, s[68:71], 0 offen offset:0 lds\n"
"v_mfma_f32_16x16x32_bf16 v[148:151], v[116:119], v[64:67], v[148:151]\n"
"ds_read_b64_tr_b16 v[40:41], v228 offset:256\n"
"s_add_u32 m0, s73, 4096\n"
"v_mfma_f32_16x16x32_bf16 v[152:155], v[120:123], v[64:67], v[152:155]\n"
"ds_read_b64_tr_b16 v[42:43], v228 offset:1280\n"
"v_add_u32 v244, 256, v244\n"
"v_mfma_f32_16x16x32_bf16 v[156:159], v[124:127], v[64:67], v[156:159]\n"
"ds_read_b64_tr_b16 v[44:45], v229 offset:256\n"
"buffer_load_dwordx4 v244, s[68:71], 0 offen offset:0 lds\n"
"v_mfma_f32_16x16x32_bf16 v[188:191], v[124:127], v[68:71], v[188:191]\n"
"ds_read_b64_tr_b16 v[46:47], v229 offset:1280\n"
"s_add_u32 m0, s73, 5120\n"
"v_mfma_f32_16x16x32_bf16 v[184:187], v[120:123], v[68:71], v[184:187]\n"
"ds_read_b128 v[0:3], v230 offset:0\n"
"v_add_u32 v244, 256, v244\n"
"v_mfma_f32_16x16x32_bf16 v[180:183], v[116:119], v[68:71], v[180:183]\n"
"ds_read_b128 v[4:7], v230 offset:2048\n"
"buffer_load_dwordx4 v244, s[68:71], 0 offen offset:0 lds\n"
"v_mfma_f32_16x16x32_bf16 v[176:179], v[112:115], v[68:71], v[176:179]\n"
"ds_read_b64_tr_b16 v[48:49], v228 offset:512\n"
"s_add_u32 m0, s73, 6144\n"
"v_mfma_f32_16x16x32_bf16 v[172:175], v[108:111], v[68:71], v[172:175]\n"
"ds_read_b64_tr_b16 v[50:51], v228 offset:1536\n"
"v_add_u32 v244, 256, v244\n"
"v_mfma_f32_16x16x32_bf16 v[168:171], v[104:107], v[68:71], v[168:171]\n"
"ds_read_b64_tr_b16 v[52:53], v229 offset:512\n"
"buffer_load_dwordx4 v244, s[68:71], 0 offen offset:0 lds\n"
"v_mfma_f32_16x16x32_bf16 v[164:167], v[100:103], v[68:71], v[164:167]\n"
"ds_read_b64_tr_b16 v[54:55], v229 offset:1536\n"
"s_add_u32 m0, s73, 7168\n"
"v_mfma_f32_16x16x32_bf16 v[160:163], v[96:99], v[68:71], v[160:163]\n"
"ds_read_b64_tr_b16 v[56:57], v228 offset:768\n"
"v_add_u32 v244, 256, v244\n"
"v_mfma_f32_16x16x32_bf16 v[192:195], v[96:99], v[72:75], v[192:195]\n"
"ds_read_b64_tr_b16 v[58:59], v228 offset:1792\n"
"buffer_load_dwordx4 v244, s[68:71], 0 offen offset:0 lds\n"
"v_mfma_f32_16x16x32_bf16 v[196:199], v[100:103], v[72:75], v[196:199]\n"
"ds_read_b64_tr_b16 v[60:61], v229 offset:768\n"
"s_add_u32 m0, s73, 8192\n"
"v_mfma_f32_16x16x32_bf16 v[200:203], v[104:107], v[72:75], v[200:203]\n"
"ds_read_b64_tr_b16 v[62:63], v229 offset:1792\n"
"v_add_u32 v244, 256, v244\n"
"v_mfma_f32_16x16x32_bf16 v[204:207], v[108:111], v[72:75], v[204:207]\n"
"ds_read_b128 v[8:11], v230 offset:4096\n"
"buffer_load_dwordx4 v244, s[68:71], 0 offen offset:0 lds\n"
"v_mfma_f32_16x16x32_bf16 v[208:211], v[112:115], v[72:75], v[208:211]\n"
"ds_read_b128 v[12:15], v230 offset:6144\n"
"buffer_load_dwordx4 v[92:95], v246, s[68:71], 0 offen offset:0\n"
"v_mfma_f32_16x16x32_bf16 v[212:215], v[116:119], v[72:75], v[212:215]\n"
"v_mfma_f32_16x16x32_bf16 v[216:219], v[120:123], v[72:75], v[216:219]\n"
"v_mfma_f32_16x16x32_bf16 v[220:223], v[124:127], v[72:75], v[220:223]\n"
"v_mfma_f32_16x16x32_bf16 v[88:91], v[124:127], v[76:79], v[88:91]\n"
"v_mfma_f32_16x16x32_bf16 v[84:87], v[120:123], v[76:79], v[84:87]\n"
"v_mfma_f32_16x16x32_bf16 v[80:83], v[116:119], v[76:79], v[80:83]\n"
"v_mfma_f32_16x16x32_bf16 v[28:31], v[112:115], v[76:79], v[28:31]\n"
"v_mfma_f32_16x16x32_bf16 v[24:27], v[108:111], v[76:79], v[24:27]\n"
"v_mfma_f32_16x16x32_bf16 v[20:23], v[104:107], v[76:79], v[20:23]\n"
"v_mfma_f32_16x16x32_bf16 v[16:19], v[100:103], v[76:79], v[16:19]\n"
"s_waitcnt vmcnt(14) lgkmcnt(0)\n"
"s_barrier\n"
"v_mfma_f32_16x16x32_bf16 v[224:227], v[96:99], v[76:79], v[224:227]\n"
"v_mfma_f32_16x16x32_bf16 v[128:131], v[32:35], v[0:3], v[128:131]\n"
"v_add_u32 v228, 65536, v250\n"
"v_add_u32 v229, 65536, v251\n"
"v_mfma_f32_16x16x32_bf16 v[132:135], v[36:39], v[0:3], v[132:135]\n"
"v_add_u32 v230, 81920, v249\n"
"ds_read_b64_tr_b16 v[96:97], v228 offset:0\n"
"v_mfma_f32_16x16x32_bf16 v[136:139], v[40:43], v[0:3], v[136:139]\n"
"ds_read_b64_tr_b16 v[98:99], v228 offset:1024\n"
"v_mfma_f32_16x16x32_bf16 v[140:143], v[44:47], v[0:3], v[140:143]\n"
"ds_read_b64_tr_b16 v[100:101], v229 offset:0\n"
"v_mfma_f32_16x16x32_bf16 v[144:147], v[48:51], v[0:3], v[144:147]\n"
"ds_read_b64_tr_b16 v[102:103], v229 offset:1024\n"
"v_mfma_f32_16x16x32_bf16 v[148:151], v[52:55], v[0:3], v[148:151]\n"
"ds_read_b64_tr_b16 v[104:105], v228 offset:256\n"
"v_mfma_f32_16x16x32_bf16 v[152:155], v[56:59], v[0:3], v[152:155]\n"
"ds_read_b64_tr_b16 v[106:107], v228 offset:1280\n"
"v_mfma_f32_16x16x32_bf16 v[156:159], v[60:63], v[0:3], v[156:159]\n"
"ds_read_b64_tr_b16 v[108:109], v229 offset:256\n"
"v_mfma_f32_16x16x32_bf16 v[188:191], v[60:63], v[4:7], v[188:191]\n"
"ds_read_b64_tr_b16 v[110:111], v229 offset:1280\n"
"v_mfma_f32_16x16x32_bf16 v[184:187], v[56:59], v[4:7], v[184:187]\n"
"ds_read_b128 v[64:67], v230 offset:0\n"
"v_mfma_f32_16x16x32_bf16 v[180:183], v[52:55], v[4:7], v[180:183]\n"
"ds_read_b128 v[68:71], v230 offset:2048\n"
"v_mfma_f32_16x16x32_bf16 v[176:179], v[48:51], v[4:7], v[176:179]\n"
"ds_read_b64_tr_b16 v[112:113], v228 offset:512\n"
"v_mfma_f32_16x16x32_bf16 v[172:175], v[44:47], v[4:7], v[172:175]\n"
"ds_read_b64_tr_b16 v[114:115], v228 offset:1536\n"
"v_mfma_f32_16x16x32_bf16 v[168:171], v[40:43], v[4:7], v[168:171]\n"
"ds_read_b64_tr_b16 v[116:117], v229 offset:512\n"
"v_mfma_f32_16x16x32_bf16 v[164:167], v[36:39], v[4:7], v[164:167]\n"
"ds_read_b64_tr_b16 v[118:119], v229 offset:1536\n"
"v_mfma_f32_16x16x32_bf16 v[160:163], v[32:35], v[4:7], v[160:163]\n"
"ds_read_b64_tr_b16 v[120:121], v228 offset:768\n"
"v_mfma_f32_16x16x32_bf16 v[192:195], v[32:35], v[8:11], v[192:195]\n"
"ds_read_b64_tr_b16 v[122:123], v228 offset:1792\n"
"v_readlane_b32 s55, v255, 11\n"
"v_mfma_f32_16x16x32_bf16 v[196:199], v[36:39], v[8:11], v[196:199]\n"
"ds_read_b64_tr_b16 v[124:125], v229 offset:768\n"
"v_mov_b32 v232, 0x7ffffff0\n"
"v_mfma_f32_16x16x32_bf16 v[200:203], v[40:43], v[8:11], v[200:203]\n"
"ds_read_b64_tr_b16 v[126:127], v229 offset:1792\n"
"v_writelane_b32 v232, 0, 0\n"
"v_mfma_f32_16x16x32_bf16 v[204:207], v[44:47], v[8:11], v[204:207]\n"
"ds_read_b128 v[72:75], v230 offset:4096\n"
"v_mov_b32 v233, s57\n"
"v_mfma_f32_16x16x32_bf16 v[208:211], v[48:51], v[8:11], v[208:211]\n"
"ds_read_b128 v[76:79], v230 offset:6144\n"
"v_mfma_f32_16x16x32_bf16 v[212:215], v[52:55], v[8:11], v[212:215]\n"
"v_mfma_f32_16x16x32_bf16 v[216:219], v[56:59], v[8:11], v[216:219]\n"
"v_mfma_f32_16x16x32_bf16 v[220:223], v[60:63], v[8:11], v[220:223]\n"
"v_mfma_f32_16x16x32_bf16 v[88:91], v[60:63], v[12:15], v[88:91]\n"
"v_mfma_f32_16x16x32_bf16 v[84:87], v[56:59], v[12:15], v[84:87]\n"
"v_mfma_f32_16x16x32_bf16 v[80:83], v[52:55], v[12:15], v[80:83]\n"
"v_mfma_f32_16x16x32_bf16 v[28:31], v[48:51], v[12:15], v[28:31]\n"
"v_mfma_f32_16x16x32_bf16 v[24:27], v[44:47], v[12:15], v[24:27]\n"
"v_mfma_f32_16x16x32_bf16 v[20:23], v[40:43], v[12:15], v[20:23]\n"
"v_mfma_f32_16x16x32_bf16 v[16:19], v[36:39], v[12:15], v[16:19]\n"
"s_waitcnt vmcnt(63) lgkmcnt(0)\n"
"s_barrier\n"
"v_mfma_f32_16x16x32_bf16 v[224:227], v[32:35], v[12:15], v[224:227]\n"
"s_waitcnt vmcnt(22)\n"
"buffer_store_dword v233, v232, s[28:31], s55 offen offset:0 sc1\n"
"v_mfma_f32_16x16x32_bf16 v[128:131], v[96:99], v[64:67], v[128:131]\n"
"v_mfma_f32_16x16x32_bf16 v[132:135], v[100:103], v[64:67], v[132:135]\n"
"v_mfma_f32_16x16x32_bf16 v[136:139], v[104:107], v[64:67], v[136:139]\n"
"v_mfma_f32_16x16x32_bf16 v[140:143], v[108:111], v[64:67], v[140:143]\n"
"v_mfma_f32_16x16x32_bf16 v[144:147], v[112:115], v[64:67], v[144:147]\n"
"v_mfma_f32_16x16x32_bf16 v[148:151], v[116:119], v[64:67], v[148:151]\n"
"v_mfma_f32_16x16x32_bf16 v[152:155], v[120:123], v[64:67], v[152:155]\n"
"v_mfma_f32_16x16x32_bf16 v[156:159], v[124:127], v[64:67], v[156:159]\n"
"v_mfma_f32_16x16x32_bf16 v[188:191], v[124:127], v[68:71], v[188:191]\n"
"v_mfma_f32_16x16x32_bf16 v[184:187], v[120:123], v[68:71], v[184:187]\n"
"v_mfma_f32_16x16x32_bf16 v[180:183], v[116:119], v[68:71], v[180:183]\n"
"v_mfma_f32_16x16x32_bf16 v[176:179], v[112:115], v[68:71], v[176:179]\n"
"v_mfma_f32_16x16x32_bf16 v[172:175], v[108:111], v[68:71], v[172:175]\n"
"v_mfma_f32_16x16x32_bf16 v[168:171], v[104:107], v[68:71], v[168:171]\n"
"v_mfma_f32_16x16x32_bf16 v[164:167], v[100:103], v[68:71], v[164:167]\n"
"v_mfma_f32_16x16x32_bf16 v[160:163], v[96:99], v[68:71], v[160:163]\n"
"v_mfma_f32_16x16x32_bf16 v[192:195], v[96:99], v[72:75], v[192:195]\n"
"v_mfma_f32_16x16x32_bf16 v[196:199], v[100:103], v[72:75], v[196:199]\n"
"v_mfma_f32_16x16x32_bf16 v[200:203], v[104:107], v[72:75], v[200:203]\n"
"v_mfma_f32_16x16x32_bf16 v[204:207], v[108:111], v[72:75], v[204:207]\n"
"v_mfma_f32_16x16x32_bf16 v[208:211], v[112:115], v[72:75], v[208:211]\n"
"v_mfma_f32_16x16x32_bf16 v[212:215], v[116:119], v[72:75], v[212:215]\n"
"v_mfma_f32_16x16x32_bf16 v[216:219], v[120:123], v[72:75], v[216:219]\n"
"v_mfma_f32_16x16x32_bf16 v[220:223], v[124:127], v[72:75], v[220:223]\n"
"v_mfma_f32_16x16x32_bf16 v[88:91], v[124:127], v[76:79], v[88:91]\n"
"v_mfma_f32_16x16x32_bf16 v[84:87], v[120:123], v[76:79], v[84:87]\n"
"v_mfma_f32_16x16x32_bf16 v[80:83], v[116:119], v[76:79], v[80:83]\n"
"v_mfma_f32_16x16x32_bf16 v[28:31], v[112:115], v[76:79], v[28:31]\n"
"v_mfma_f32_16x16x32_bf16 v[24:27], v[108:111], v[76:79], v[24:27]\n"
"v_mfma_f32_16x16x32_bf16 v[20:23], v[104:107], v[76:79], v[20:23]\n"
"v_mfma_f32_16x16x32_bf16 v[16:19], v[100:103], v[76:79], v[16:19]\n"
"v_mfma_f32_16x16x32_bf16 v[224:227], v[96:99], v[76:79], v[224:227]\n"
"s_mov_b32 s50, 0x3db8aa3b\n"
"s_mov_b32 s51, 0x3db8aa3b\n"
"v_add_u32 v228, s48, v253\n"
"v_add_u32 v228, 98304, v228\n"
"v_readlane_b32 s56, v255, 9\n"
"s_nop 0\n"
"s_add_u32 s44, s56, 0\n"
"v_pk_mul_f32 v[128:129], v[128:129], s[50:51]\n"
"v_pk_mul_f32 v[130:131], v[130:131], s[50:51]\n"
"v_pk_mul_f32 v[132:133], v[132:133], s[50:51]\n"
"v_pk_mul_f32 v[134:135], v[134:135], s[50:51]\n"
"v_cvt_pk_bf16_f32 v128, v128, v129\n"
"v_cvt_pk_bf16_f32 v129, v130, v131\n"
"v_cvt_pk_bf16_f32 v130, v132, v133\n"
"v_cvt_pk_bf16_f32 v131, v134, v135\n"
"ds_write_b128 v228, v[128:131] offset:0\n"
"buffer_store_dwordx4 v[128:131], v253, s[28:31], s44 offen offset:0 sc1\n"
"v_pk_mul_f32 v[136:137], v[136:137], s[50:51]\n"
"v_pk_mul_f32 v[138:139], v[138:139], s[50:51]\n"
"v_pk_mul_f32 v[140:141], v[140:141], s[50:51]\n"
"v_pk_mul_f32 v[142:143], v[142:143], s[50:51]\n"
"v_cvt_pk_bf16_f32 v136, v136, v137\n"
"v_cvt_pk_bf16_f32 v137, v138, v139\n"
"v_cvt_pk_bf16_f32 v138, v140, v141\n"
"v_cvt_pk_bf16_f32 v139, v142, v143\n"
"ds_write_b128 v228, v[136:139] offset:1024\n"
"buffer_store_dwordx4 v[136:139], v253, s[28:31], s44 offen offset:1024 sc1\n"
"v_pk_mul_f32 v[144:145], v[144:145], s[50:51]\n"
"v_pk_mul_f32 v[146:147], v[146:147], s[50:51]\n"
"v_pk_mul_f32 v[148:149], v[148:149], s[50:51]\n"
"v_pk_mul_f32 v[150:151], v[150:151], s[50:51]\n"
"v_cvt_pk_bf16_f32 v144, v144, v145\n"
"v_cvt_pk_bf16_f32 v145, v146, v147\n"
"v_cvt_pk_bf16_f32 v146, v148, v149\n"
"v_cvt_pk_bf16_f32 v147, v150, v151\n"
"ds_write_b128 v228, v[144:147] offset:2048\n"
"buffer_store_dwordx4 v[144:147], v253, s[28:31], s44 offen offset:2048 sc1\n"
"v_pk_mul_f32 v[152:153], v[152:153], s[50:51]\n"
"v_pk_mul_f32 v[154:155], v[154:155], s[50:51]\n"
"v_pk_mul_f32 v[156:157], v[156:157], s[50:51]\n"
"v_pk_mul_f32 v[158:159], v[158:159], s[50:51]\n"
"v_cvt_pk_bf16_f32 v152, v152, v153\n"
"v_cvt_pk_bf16_f32 v153, v154, v155\n"
"v_cvt_pk_bf16_f32 v154, v156, v157\n"
"v_cvt_pk_bf16_f32 v155, v158, v159\n"
"ds_write_b128 v228, v[152:155] offset:3072\n"
"buffer_store_dwordx4 v[152:155], v253, s[28:31], s44 offen offset:3072 sc1\n"
"s_waitcnt lgkmcnt(4)\n"
"s_add_u32 s44, s56, 8192\n"
"v_pk_mul_f32 v[160:161], v[160:161], s[50:51]\n"
"v_pk_mul_f32 v[162:163], v[162:163], s[50:51]\n"
"v_pk_mul_f32 v[164:165], v[164:165], s[50:51]\n"
"v_pk_mul_f32 v[166:167], v[166:167], s[50:51]\n"
"v_cvt_pk_bf16_f32 v160, v160, v161\n"
"v_cvt_pk_bf16_f32 v161, v162, v163\n"
"v_cvt_pk_bf16_f32 v162, v164, v165\n"
"v_cvt_pk_bf16_f32 v163, v166, v167\n"
"ds_write_b128 v228, v[160:163] offset:8192\n"
"buffer_store_dwordx4 v[160:163], v253, s[28:31], s44 offen offset:0 sc1\n"
"v_pk_mul_f32 v[168:169], v[168:169], s[50:51]\n"
"v_pk_mul_f32 v[170:171], v[170:171], s[50:51]\n"
"v_pk_mul_f32 v[172:173], v[172:173], s[50:51]\n"
"v_pk_mul_f32 v[174:175], v[174:175], s[50:51]\n"
"v_cvt_pk_bf16_f32 v168, v168, v169\n"
"v_cvt_pk_bf16_f32 v169, v170, v171\n"
"v_cvt_pk_bf16_f32 v170, v172, v173\n"
"v_cvt_pk_bf16_f32 v171, v174, v175\n"
"ds_write_b128 v228, v[168:171] offset:9216\n"
"buffer_store_dwordx4 v[168:171], v253, s[28:31], s44 offen offset:1024 sc1\n"
"v_pk_mul_f32 v[176:177], v[176:177], s[50:51]\n"
"v_pk_mul_f32 v[178:179], v[178:179], s[50:51]\n"
"v_pk_mul_f32 v[180:181], v[180:181], s[50:51]\n"
"v_pk_mul_f32 v[182:183], v[182:183], s[50:51]\n"
"v_cvt_pk_bf16_f32 v176, v176, v177\n"
"v_cvt_pk_bf16_f32 v177, v178, v179\n"
"v_cvt_pk_bf16_f32 v178, v180, v181\n"
"v_cvt_pk_bf16_f32 v179, v182, v183\n"
"ds_write_b128 v228, v[176:179] offset:10240\n"
"buffer_store_dwordx4 v[176:179], v253, s[28:31], s44 offen offset:2048 sc1\n"
"v_pk_mul_f32 v[184:185], v[184:185], s[50:51]\n"
"v_pk_mul_f32 v[186:187], v[186:187], s[50:51]\n"
"v_pk_mul_f32 v[188:189], v[188:189], s[50:51]\n"
"v_pk_mul_f32 v[190:191], v[190:191], s[50:51]\n"
"v_cvt_pk_bf16_f32 v184, v184, v185\n"
"v_cvt_pk_bf16_f32 v185, v186, v187\n"
"v_cvt_pk_bf16_f32 v186, v188, v189\n"
"v_cvt_pk_bf16_f32 v187, v190, v191\n"
"ds_write_b128 v228, v[184:187] offset:11264\n"
"buffer_store_dwordx4 v[184:187], v253, s[28:31], s44 offen offset:3072 sc1\n"
"s_waitcnt lgkmcnt(4)\n"
"s_add_u32 s44, s56, 16384\n"
"v_pk_mul_f32 v[192:193], v[192:193], s[50:51]\n"
"v_pk_mul_f32 v[194:195], v[194:195], s[50:51]\n"
"v_pk_mul_f32 v[196:197], v[196:197], s[50:51]\n"
"v_pk_mul_f32 v[198:199], v[198:199], s[50:51]\n"
"v_cvt_pk_bf16_f32 v192, v192, v193\n"
"v_cvt_pk_bf16_f32 v193, v194, v195\n"
"v_cvt_pk_bf16_f32 v194, v196, v197\n"
"v_cvt_pk_bf16_f32 v195, v198, v199\n"
"ds_write_b128 v228, v[192:195] offset:16384\n"
"buffer_store_dwordx4 v[192:195], v253, s[28:31], s44 offen offset:0 sc1\n"
"v_pk_mul_f32 v[200:201], v[200:201], s[50:51]\n"
"v_pk_mul_f32 v[202:203], v[202:203], s[50:51]\n"
"v_pk_mul_f32 v[204:205], v[204:205], s[50:51]\n"
"v_pk_mul_f32 v[206:207], v[206:207], s[50:51]\n"
"v_cvt_pk_bf16_f32 v200, v200, v201\n"
"v_cvt_pk_bf16_f32 v201, v202, v203\n"
"v_cvt_pk_bf16_f32 v202, v204, v205\n"
"v_cvt_pk_bf16_f32 v203, v206, v207\n"
"ds_write_b128 v228, v[200:203] offset:17408\n"
"buffer_store_dwordx4 v[200:203], v253, s[28:31], s44 offen offset:1024 sc1\n"
"v_pk_mul_f32 v[208:209], v[208:209], s[50:51]\n"
"v_pk_mul_f32 v[210:211], v[210:211], s[50:51]\n"
"v_pk_mul_f32 v[212:213], v[212:213], s[50:51]\n"
"v_pk_mul_f32 v[214:215], v[214:215], s[50:51]\n"
"v_cvt_pk_bf16_f32 v208, v208, v209\n"
"v_cvt_pk_bf16_f32 v209, v210, v211\n"
"v_cvt_pk_bf16_f32 v210, v212, v213\n"
"v_cvt_pk_bf16_f32 v211, v214, v215\n"
"ds_write_b128 v228, v[208:211] offset:18432\n"
"buffer_store_dwordx4 v[208:211], v253, s[28:31], s44 offen offset:2048 sc1\n"
"v_pk_mul_f32 v[216:217], v[216:217], s[50:51]\n"
"v_pk_mul_f32 v[218:219], v[218:219], s[50:51]\n"
"v_pk_mul_f32 v[220:221], v[220:221], s[50:51]\n"
"v_pk_mul_f32 v[222:223], v[222:223], s[50:51]\n"
"v_cvt_pk_bf16_f32 v216, v216, v217\n"
"v_cvt_pk_bf16_f32 v217, v218, v219\n"
"v_cvt_pk_bf16_f32 v218, v220, v221\n"
"v_cvt_pk_bf16_f32 v219, v222, v223\n"
"ds_write_b128 v228, v[216:219] offset:19456\n"
"buffer_store_dwordx4 v[216:219], v253, s[28:31], s44 offen offset:3072 sc1\n"
"s_waitcnt lgkmcnt(4)\n"
"s_add_u32 s44, s56, 24576\n"
"v_pk_mul_f32 v[224:225], v[224:225], s[50:51]\n"
"v_pk_mul_f32 v[226:227], v[226:227], s[50:51]\n"
"v_pk_mul_f32 v[16:17], v[16:17], s[50:51]\n"
"v_pk_mul_f32 v[18:19], v[18:19], s[50:51]\n"
"v_cvt_pk_bf16_f32 v224, v224, v225\n"
"v_cvt_pk_bf16_f32 v225, v226, v227\n"
"v_cvt_pk_bf16_f32 v226, v16, v17\n"
"v_cvt_pk_bf16_f32 v227, v18, v19\n"
"ds_write_b128 v228, v[224:227] offset:24576\n"
"buffer_store_dwordx4 v[224:227], v253, s[28:31], s44 offen offset:0 sc1\n"
"v_pk_mul_f32 v[20:21], v[20:21], s[50:51]\n"
"v_pk_mul_f32 v[22:23], v[22:23], s[50:51]\n"
"v_pk_mul_f32 v[24:25], v[24:25], s[50:51]\n"
"v_pk_mul_f32 v[26:27], v[26:27], s[50:51]\n"
"v_cvt_pk_bf16_f32 v20, v20, v21\n"
"v_cvt_pk_bf16_f32 v21, v22, v23\n"
"v_cvt_pk_bf16_f32 v22, v24, v25\n"
"v_cvt_pk_bf16_f32 v23, v26, v27\n"
"ds_write_b128 v228, v[20:23] offset:25600\n"
"buffer_store_dwordx4 v[20:23], v253, s[28:31], s44 offen offset:1024 sc1\n"
"v_pk_mul_f32 v[28:29], v[28:29], s[50:51]\n"
"v_pk_mul_f32 v[30:31], v[30:31], s[50:51]\n"
"v_pk_mul_f32 v[80:81], v[80:81], s[50:51]\n"
"v_pk_mul_f32 v[82:83], v[82:83], s[50:51]\n"
"v_cvt_pk_bf16_f32 v28, v28, v29\n"
"v_cvt_pk_bf16_f32 v29, v30, v31\n"
"v_cvt_pk_bf16_f32 v30, v80, v81\n"
"v_cvt_pk_bf16_f32 v31, v82, v83\n"
"ds_write_b128 v228, v[28:31] offset:26624\n"
"buffer_store_dwordx4 v[28:31], v253, s[28:31], s44 offen offset:2048 sc1\n"
"v_pk_mul_f32 v[84:85], v[84:85], s[50:51]\n"
"v_pk_mul_f32 v[86:87], v[86:87], s[50:51]\n"
"v_pk_mul_f32 v[88:89], v[88:89], s[50:51]\n"
"v_pk_mul_f32 v[90:91], v[90:91], s[50:51]\n"
"v_cvt_pk_bf16_f32 v84, v84, v85\n"
"v_cvt_pk_bf16_f32 v85, v86, v87\n"
"v_cvt_pk_bf16_f32 v86, v88, v89\n"
"v_cvt_pk_bf16_f32 v87, v90, v91\n"
"ds_write_b128 v228, v[84:87] offset:27648\n"
"buffer_store_dwordx4 v[84:87], v253, s[28:31], s44 offen offset:3072 sc1\n"
"s_waitcnt lgkmcnt(0)\n"
"s_waitcnt vmcnt(17)\n"
"v_mbcnt_lo_u32_b32 v206, -1, 0\n"
"v_mbcnt_hi_u32_b32 v206, -1, v206\n"
"s_lshl_b32 s44, s46, 13\n"
"s_lshl_b32 s45, s46, 11\n"
"v_lshlrev_b32 v225, 4, v206\n"
"v_lshlrev_b32 v248, 2, v206\n"
"v_subrev_u32 v218, s44, v225\n"
"v_add_u32 v219, s44, v225\n"
"v_add_u32 v220, 4096, v219\n"
"s_add_u32 s40, s44, s48\n"
"s_add_u32 s39, s40, 163840\n"
"s_lshl_b32 s45, s46, 4\n"
"v_and_b32 v221, 15, v206\n"
"v_lshrrev_b32 v207, 4, v206\n"
"v_lshlrev_b32 v207, 2, v207\n"
"v_sub_u32 v221, v221, v207\n"
"v_subrev_u32 v223, s45, v221\n"
"v_add_u32 v223, 48, v223\n"
"v_add_u32 v221, s45, v221\n"
"v_mov_b32 v222, 0xff800000\n"
"v_readlane_b32 s44, v255, 13\n"
"s_mov_b32 s22, 2326528\n"
"v_readlane_b32 s51, v255, 15\n"
"v_readlane_b32 s47, v255, 16\n"
"v_readlane_b32 s70, v255, 17\n"
"v_readlane_b32 s71, v255, 18\n"
"v_readlane_b32 s49, v255, 19\n"
"v_readlane_b32 s50, v255, 20\n"
"v_readlane_b32 s73, v255, 23\n"
"s_add_u32 s20, s28, s44\n"
"s_addc_u32 s21, s29, 0\n"
"s_mov_b32 s23, 0x20000\n"
"v_readlane_b32 s38, v255, 14\n"
"s_add_u32 s36, s40, 65536\n"
"s_add_u32 s37, s40, 98304\n"
"v_add_u32 v216, s36, v218\n"
"v_add_u32 v217, s37, v218\n"
"v_mov_b32 v132, 0\n"
"v_mov_b32 v133, 0\n"
"v_mov_b32 v134, 0\n"
"v_mov_b32 v135, 0\n"
"v_and_b32 v136, 15, v206\n"
"v_lshrrev_b32 v137, 4, v206\n"
"v_lshlrev_b32 v129, 5, v136\n"
"v_lshl_add_u32 v129, v137, 3, v129\n"
"v_mul_u32_u24 v131, 1280, v137\n"
"v_lshl_add_u32 v131, v136, 4, v131\n"
"v_add_u32 v131, s59, v131\n"
"ds_write_b128 v131, v[132:135] offset:0\n"
"v_lshlrev_b32 v137, 2, v137\n"
"v_sub_u32 v130, v136, v137\n"
"v_lshlrev_b32 v130, 4, v130\n"
"v_add_u32 v130, s58, v130\n"
"v_add_u32 v130, 192, v130\n"
"v_add_u32 v131, 16, v131\n"
"v_mov_b32 v138, s58\n"
"s_cmp_eq_u32 s75, 1\n"
"s_cbranch_scc1 L_s2_nofix\n"
"ds_write_b32 v138, v92 offset:204\n"
"ds_write_b32 v138, v92 offset:216\n"
"ds_write_b32 v138, v93 offset:220\n"
"ds_write_b32 v138, v92 offset:228\n"
"ds_write_b32 v138, v93 offset:232\n"
"ds_write_b32 v138, v94 offset:236\n"
"L_s2_nofix:\n"
"s_lshl_b32 s44, s46, 13\n"
"s_add_u32 s45, s48, 98304\n"
"v_add_u32 v209, s45, v225\n"
"v_add_u32 v208, s44, v209\n"
"v_subrev_u32 v209, s44, v209\n"
"s_waitcnt lgkmcnt(0)\n"
"s_barrier\n"
"ds_read_b128 a[0:3], v208 offset:0\n"
"ds_read_b128 a[4:7], v208 offset:1024\n"
"ds_read_b128 a[8:11], v208 offset:2048\n"
"ds_read_b128 a[12:15], v208 offset:3072\n"
"ds_read_b128 a[16:19], v208 offset:4096\n"
"ds_read_b128 a[20:23], v208 offset:5120\n"
"ds_read_b128 a[24:27], v208 offset:6144\n"
"ds_read_b128 a[28:31], v208 offset:7168\n"
"s_waitcnt lgkmcnt(4)\n"
"ds_read_b128 a[32:35], v209 offset:57344\n"
"ds_read_b128 a[36:39], v209 offset:58368\n"
"ds_read_b128 a[40:43], v209 offset:59392\n"
"ds_read_b128 a[44:47], v209 offset:60416\n"
"ds_read_b128 a[48:51], v209 offset:61440\n"
"ds_read_b128 a[52:55], v209 offset:62464\n"
"ds_read_b128 a[56:59], v209 offset:63488\n"
"ds_read_b128 a[60:63], v209 offset:64512\n"
"v_readlane_b32 s56, v255, 33\n"
"v_readlane_b32 s72, v255, 32\n"
"ds_read_b128 v[32:35], v131 offset:240\n"
"ds_read_b128 v[16:19], v130 offset:0\n"
"ds_read_b128 v[36:39], v131 offset:496\n"
"ds_read_b128 v[20:23], v130 offset:4096\n"
"ds_read_b128 v[40:43], v131 offset:752\n"
"ds_read_b128 v[24:27], v130 offset:8192\n"
"ds_read_b128 v[44:47], v131 offset:1008\n"
"ds_read_b128 v[28:31], v130 offset:12288\n"
"ds_read_b128 v[64:67], v131 offset:224\n"
"ds_read_b128 v[48:51], v130 offset:256\n"
"ds_read_b128 v[68:71], v131 offset:480\n"
"ds_read_b128 v[52:55], v130 offset:4352\n"
"ds_read_b128 v[72:75], v131 offset:736\n"
"ds_read_b128 v[56:59], v130 offset:8448\n"
"ds_read_b128 v[76:79], v131 offset:992\n"
"ds_read_b128 v[60:63], v130 offset:12544\n"
"s_waitcnt lgkmcnt(15)\n"
"v_lshlrev_b32 v0, 16, v35\n"
"v_mul_f32 v0, s56, v0\n"
"v_lshlrev_b32 v1, 16, v34\n"
"v_mul_f32 v1, s56, v1\n"
"v_lshlrev_b32 v2, 16, v33\n"
"v_mul_f32 v2, s56, v2\n"
"v_lshlrev_b32 v3, 16, v32\n"
"v_mul_f32 v3, s56, v3\n"
"s_waitcnt lgkmcnt(13)\n"
"v_lshlrev_b32 v4, 16, v39\n"
"v_mul_f32 v4, s56, v4\n"
"v_lshlrev_b32 v5, 16, v38\n"
"v_mul_f32 v5, s56, v5\n"
"v_lshlrev_b32 v6, 16, v37\n"
"v_mul_f32 v6, s56, v6\n"
"v_lshlrev_b32 v7, 16, v36\n"
"v_mul_f32 v7, s56, v7\n"
"s_waitcnt lgkmcnt(11)\n"
"v_lshlrev_b32 v8, 16, v43\n"
"v_mul_f32 v8, s56, v8\n"
"v_lshlrev_b32 v9, 16, v42\n"
"v_mul_f32 v9, s56, v9\n"
"v_lshlrev_b32 v10, 16, v41\n"
"v_mul_f32 v10, s56, v10\n"
"v_lshlrev_b32 v11, 16, v40\n"
"v_mul_f32 v11, s56, v11\n"
"s_waitcnt lgkmcnt(9)\n"
"v_lshlrev_b32 v12, 16, v47\n"
"v_mul_f32 v12, s56, v12\n"
"v_lshlrev_b32 v13, 16, v46\n"
"v_mul_f32 v13, s56, v13\n"
"v_lshlrev_b32 v14, 16, v45\n"
"v_mul_f32 v14, s56, v14\n"
"v_lshlrev_b32 v15, 16, v44\n"
"v_mul_f32 v15, s56, v15\n"
"v_mfma_f32_16x16x32_bf16 v[0:3], v[16:19], v[32:35], v[0:3]\n"
"v_mfma_f32_16x16x32_bf16 v[4:7], v[16:19], v[36:39], v[4:7]\n"
"ds_read_b128 v[96:99], v131 offset:208\n"
"v_mfma_f32_16x16x32_bf16 v[4:7], v[20:23], v[32:35], v[4:7]\n"
"ds_read_b128 v[80:83], v130 offset:512\n"
"v_mfma_f32_16x16x32_bf16 v[8:11], v[16:19], v[40:43], v[8:11]\n"
"ds_read_b128 v[100:103], v131 offset:464\n"
"v_mfma_f32_16x16x32_bf16 v[8:11], v[20:23], v[36:39], v[8:11]\n"
"ds_read_b128 v[84:87], v130 offset:4608\n"
"v_mfma_f32_16x16x32_bf16 v[8:11], v[24:27], v[32:35], v[8:11]\n"
"ds_read_b128 v[104:107], v131 offset:720\n"
"v_mfma_f32_16x16x32_bf16 v[12:15], v[16:19], v[44:47], v[12:15]\n"
"ds_read_b128 v[88:91], v130 offset:8704\n"
"v_mfma_f32_16x16x32_bf16 v[12:15], v[20:23], v[40:43], v[12:15]\n"
"ds_read_b128 v[108:111], v131 offset:976\n"
"v_mfma_f32_16x16x32_bf16 v[12:15], v[24:27], v[36:39], v[12:15]\n"
"ds_read_b128 v[92:95], v130 offset:12800\n"
"s_waitcnt lgkmcnt(8)\n"
"v_mfma_f32_16x16x32_bf16 v[12:15], v[28:31], v[32:35], v[12:15]\n"
"v_mfma_f32_16x16x32_bf16 v[0:3], v[48:51], v[64:67], v[0:3]\n"
"v_mfma_f32_16x16x32_bf16 v[4:7], v[48:51], v[68:71], v[4:7]\n"
"ds_read_b128 v[32:35], v131 offset:192\n"
"v_mfma_f32_16x16x32_bf16 v[4:7], v[52:55], v[64:67], v[4:7]\n"
"ds_read_b128 v[16:19], v130 offset:768\n"
"v_mfma_f32_16x16x32_bf16 v[8:11], v[48:51], v[72:75], v[8:11]\n"
"ds_read_b128 v[36:39], v131 offset:448\n"
"v_mfma_f32_16x16x32_bf16 v[8:11], v[52:55], v[68:71], v[8:11]\n"
"ds_read_b128 v[20:23], v130 offset:4864\n"
"v_mfma_f32_16x16x32_bf16 v[8:11], v[56:59], v[64:67], v[8:11]\n"
"ds_read_b128 v[40:43], v131 offset:704\n"
"v_mfma_f32_16x16x32_bf16 v[12:15], v[48:51], v[76:79], v[12:15]\n"
"ds_read_b128 v[24:27], v130 offset:8960\n"
"v_mfma_f32_16x16x32_bf16 v[12:15], v[52:55], v[72:75], v[12:15]\n"
"ds_read_b128 v[44:47], v131 offset:960\n"
"v_mfma_f32_16x16x32_bf16 v[12:15], v[56:59], v[68:71], v[12:15]\n"
"ds_read_b128 v[28:31], v130 offset:13056\n"
"v_mfma_f32_16x16x32_bf16 v[12:15], v[60:63], v[64:67], v[12:15]\n"
"s_waitcnt lgkmcnt(7)\n"
"v_mfma_f32_16x16x32_bf16 v[0:3], v[80:83], v[96:99], v[0:3]\n"
"v_mfma_f32_16x16x32_bf16 v[4:7], v[80:83], v[100:103], v[4:7]\n"
"ds_read_b128 v[64:67], v131 offset:176\n"
"v_mfma_f32_16x16x32_bf16 v[4:7], v[84:87], v[96:99], v[4:7]\n"
"ds_read_b128 v[48:51], v130 offset:1024\n"
"v_mfma_f32_16x16x32_bf16 v[8:11], v[80:83], v[104:107], v[8:11]\n"
"ds_read_b128 v[68:71], v131 offset:432\n"
"v_mfma_f32_16x16x32_bf16 v[8:11], v[84:87], v[100:103], v[8:11]\n"
"ds_read_b128 v[52:55], v130 offset:5120\n"
"v_mfma_f32_16x16x32_bf16 v[8:11], v[88:91], v[96:99], v[8:11]\n"
"ds_read_b128 v[72:75], v131 offset:688\n"
"v_mfma_f32_16x16x32_bf16 v[12:15], v[80:83], v[108:111], v[12:15]\n"
"ds_read_b128 v[56:59], v130 offset:9216\n"
"v_mfma_f32_16x16x32_bf16 v[12:15], v[84:87], v[104:107], v[12:15]\n"
"ds_read_b128 v[76:79], v131 offset:944\n"
"v_mfma_f32_16x16x32_bf16 v[12:15], v[88:91], v[100:103], v[12:15]\n"
"ds_read_b128 v[60:63], v130 offset:13312\n"
"v_mfma_f32_16x16x32_bf16 v[12:15], v[92:95], v[96:99], v[12:15]\n"
"s_waitcnt lgkmcnt(7)\n"
"v_mfma_f32_16x16x32_bf16 v[0:3], v[16:19], v[32:35], v[0:3]\n"
"v_mfma_f32_16x16x32_bf16 v[4:7], v[16:19], v[36:39], v[4:7]\n"
"ds_read_b128 v[96:99], v131 offset:160\n"
"v_mfma_f32_16x16x32_bf16 v[4:7], v[20:23], v[32:35], v[4:7]\n"
"ds_read_b128 v[80:83], v130 offset:1280\n"
"v_mfma_f32_16x16x32_bf16 v[8:11], v[16:19], v[40:43], v[8:11]\n"
"ds_read_b128 v[100:103], v131 offset:416\n"
"v_mfma_f32_16x16x32_bf16 v[8:11], v[20:23], v[36:39], v[8:11]\n"
"ds_read_b128 v[84:87], v130 offset:5376\n"
"v_mfma_f32_16x16x32_bf16 v[8:11], v[24:27], v[32:35], v[8:11]\n"
"ds_read_b128 v[104:107], v131 offset:672\n"
"v_mfma_f32_16x16x32_bf16 v[12:15], v[16:19], v[44:47], v[12:15]\n"
"ds_read_b128 v[88:91], v130 offset:9472\n"
"v_mfma_f32_16x16x32_bf16 v[12:15], v[20:23], v[40:43], v[12:15]\n"
"ds_read_b128 v[108:111], v131 offset:928\n"
"v_mfma_f32_16x16x32_bf16 v[12:15], v[24:27], v[36:39], v[12:15]\n"
"ds_read_b128 v[92:95], v130 offset:13568\n"
"v_mfma_f32_16x16x32_bf16 v[12:15], v[28:31], v[32:35], v[12:15]\n"
"v_readlane_b32 s74, v255, 24\n"
"s_mov_b32 s66, 100000\n"
"s_mov_b32 s60, 4294967295\n"
"s_mov_b32 s61, 4294967295\n"
"s_nop 2\n"
"L_poll_kv:\n"
"buffer_load_dword v246, v248, s[28:31], s74 offen offset:0 sc1\n"
"s_waitcnt vmcnt(0)\n"
"v_cmp_eq_u32 vcc, s57, v246\n"
"s_nop 0\n"
"s_and_b64 s[62:63], vcc, s[60:61]\n"
"s_cmp_eq_u64 s[62:63], s[60:61]\n"
"s_cbranch_scc1 L_got_kv\n"
"s_sleep 4\n"
"s_sub_u32 s66, s66, 1\n"
"s_cbranch_scc0 L_poll_kv\n"
"L_got_kv:\n"
"v_readlane_b32 s44, v255, 12\n"
"v_mov_b32 v246, 0x7ffffff0\n"
"v_writelane_b32 v246, 0, 0\n"
"v_mov_b32 v247, s57\n"
"s_nop 1\n"
"buffer_store_dword v247, v246, s[28:31], s44 offen offset:0 sc1\n"
"s_barrier\n"
"s_waitcnt lgkmcnt(7)\n"
"v_mfma_f32_16x16x32_bf16 v[0:3], v[48:51], v[64:67], v[0:3]\n"
"s_add_u32 m0, s40, 65536\n"
"s_nop 0\n"
"buffer_load_dwordx4 v219, s[20:23], s38 offen offset:0 sc1 lds\n"
"v_mfma_f32_16x16x32_bf16 v[4:7], v[48:51], v[68:71], v[4:7]\n"
"ds_read_b128 v[32:35], v131 offset:144\n"
"v_mfma_f32_16x16x32_bf16 v[4:7], v[52:55], v[64:67], v[4:7]\n"
"ds_read_b128 v[16:19], v130 offset:1536\n"
"v_mfma_f32_16x16x32_bf16 v[8:11], v[48:51], v[72:75], v[8:11]\n"
"ds_read_b128 v[36:39], v131 offset:400\n"
"buffer_load_dwordx4 v219, s[20:23], s38 offen offset:1024 sc1 lds\n"
"v_mfma_f32_16x16x32_bf16 v[8:11], v[52:55], v[68:71], v[8:11]\n"
"ds_read_b128 v[20:23], v130 offset:5632\n"
"v_mfma_f32_16x16x32_bf16 v[8:11], v[56:59], v[64:67], v[8:11]\n"
"ds_read_b128 v[40:43], v131 offset:656\n"
"v_mfma_f32_16x16x32_bf16 v[12:15], v[48:51], v[76:79], v[12:15]\n"
"ds_read_b128 v[24:27], v130 offset:9728\n"
"buffer_load_dwordx4 v219, s[20:23], s38 offen offset:2048 sc1 lds\n"
"v_mfma_f32_16x16x32_bf16 v[12:15], v[52:55], v[72:75], v[12:15]\n"
"ds_read_b128 v[44:47], v131 offset:912\n"
"v_mfma_f32_16x16x32_bf16 v[12:15], v[56:59], v[68:71], v[12:15]\n"
"ds_read_b128 v[28:31], v130 offset:13824\n"
"v_mfma_f32_16x16x32_bf16 v[12:15], v[60:63], v[64:67], v[12:15]\n"
"buffer_load_dwordx4 v219, s[20:23], s38 offen offset:3072 sc1 lds\n"
"s_waitcnt lgkmcnt(7)\n"
"v_mfma_f32_16x16x32_bf16 v[0:3], v[80:83], v[96:99], v[0:3]\n"
"s_add_u32 m0, s40, 69632\n"
"s_nop 0\n"
"buffer_load_dwordx4 v220, s[20:23], s38 offen offset:0 sc1 lds\n"
"v_mfma_f32_16x16x32_bf16 v[4:7], v[80:83], v[100:103], v[4:7]\n"
"ds_read_b128 v[64:67], v131 offset:128\n"
"v_mfma_f32_16x16x32_bf16 v[4:7], v[84:87], v[96:99], v[4:7]\n"
"ds_read_b128 v[48:51], v130 offset:1792\n"
"v_mfma_f32_16x16x32_bf16 v[8:11], v[80:83], v[104:107], v[8:11]\n"
"ds_read_b128 v[68:71], v131 offset:384\n"
"buffer_load_dwordx4 v220, s[20:23], s38 offen offset:1024 sc1 lds\n"
"v_mfma_f32_16x16x32_bf16 v[8:11], v[84:87], v[100:103], v[8:11]\n"
"ds_read_b128 v[52:55], v130 offset:5888\n"
"v_mfma_f32_16x16x32_bf16 v[8:11], v[88:91], v[96:99], v[8:11]\n"
"ds_read_b128 v[72:75], v131 offset:640\n"
"v_mfma_f32_16x16x32_bf16 v[12:15], v[80:83], v[108:111], v[12:15]\n"
"ds_read_b128 v[56:59], v130 offset:9984\n"
"buffer_load_dwordx4 v220, s[20:23], s38 offen offset:2048 sc1 lds\n"
"v_mfma_f32_16x16x32_bf16 v[12:15], v[84:87], v[104:107], v[12:15]\n"
"ds_read_b128 v[76:79], v131 offset:896\n"
"v_mfma_f32_16x16x32_bf16 v[12:15], v[88:91], v[100:103], v[12:15]\n"
"ds_read_b128 v[60:63], v130 offset:14080\n"
"v_mfma_f32_16x16x32_bf16 v[12:15], v[92:95], v[96:99], v[12:15]\n"
"buffer_load_dwordx4 v220, s[20:23], s38 offen offset:3072 sc1 lds\n"
"s_waitcnt lgkmcnt(7)\n"
"v_mfma_f32_16x16x32_bf16 v[0:3], v[16:19], v[32:35], v[0:3]\n"
"s_add_u32 s38, s38, 32768\n"
"s_add_u32 m0, s40, 98304\n"
"s_nop 0\n"
"buffer_load_dwordx4 v219, s[20:23], s38 offen offset:0 sc1 lds\n"
"v_mfma_f32_16x16x32_bf16 v[4:7], v[16:19], v[36:39], v[4:7]\n"
"ds_read_b128 v[96:99], v131 offset:112\n"
"v_mfma_f32_16x16x32_bf16 v[4:7], v[20:23], v[32:35], v[4:7]\n"
"ds_read_b128 v[80:83], v130 offset:2048\n"
"v_mfma_f32_16x16x32_bf16 v[8:11], v[16:19], v[40:43], v[8:11]\n"
"ds_read_b128 v[100:103], v131 offset:368\n"
"buffer_load_dwordx4 v219, s[20:23], s38 offen offset:1024 sc1 lds\n"
"v_mfma_f32_16x16x32_bf16 v[8:11], v[20:23], v[36:39], v[8:11]\n"
"ds_read_b128 v[84:87], v130 offset:6144\n"
"v_mfma_f32_16x16x32_bf16 v[8:11], v[24:27], v[32:35], v[8:11]\n"
"ds_read_b128 v[104:107], v131 offset:624\n"
"v_mfma_f32_16x16x32_bf16 v[12:15], v[16:19], v[44:47], v[12:15]\n"
"ds_read_b128 v[88:91], v130 offset:10240\n"
"buffer_load_dwordx4 v219, s[20:23], s38 offen offset:2048 sc1 lds\n"
"v_mfma_f32_16x16x32_bf16 v[12:15], v[20:23], v[40:43], v[12:15]\n"
"ds_read_b128 v[108:111], v131 offset:880\n"
"v_mfma_f32_16x16x32_bf16 v[12:15], v[24:27], v[36:39], v[12:15]\n"
"ds_read_b128 v[92:95], v130 offset:14336\n"
"v_mfma_f32_16x16x32_bf16 v[12:15], v[28:31], v[32:35], v[12:15]\n"
"buffer_load_dwordx4 v219, s[20:23], s38 offen offset:3072 sc1 lds\n"
"s_waitcnt lgkmcnt(7)\n"
"v_mfma_f32_16x16x32_bf16 v[0:3], v[48:51], v[64:67], v[0:3]\n"
"s_add_u32 m0, s40, 102400\n"
"s_nop 0\n"
"buffer_load_dwordx4 v220, s[20:23], s38 offen offset:0 sc1 lds\n"
"v_mfma_f32_16x16x32_bf16 v[4:7], v[48:51], v[68:71], v[4:7]\n"
"ds_read_b128 v[32:35], v131 offset:96\n"
"v_mfma_f32_16x16x32_bf16 v[4:7], v[52:55], v[64:67], v[4:7]\n"
"ds_read_b128 v[16:19], v130 offset:2304\n"
"v_mfma_f32_16x16x32_bf16 v[8:11], v[48:51], v[72:75], v[8:11]\n"
"ds_read_b128 v[36:39], v131 offset:352\n"
"buffer_load_dwordx4 v220, s[20:23], s38 offen offset:1024 sc1 lds\n"
"v_mfma_f32_16x16x32_bf16 v[8:11], v[52:55], v[68:71], v[8:11]\n"
"ds_read_b128 v[20:23], v130 offset:6400\n"
"v_mfma_f32_16x16x32_bf16 v[8:11], v[56:59], v[64:67], v[8:11]\n"
"ds_read_b128 v[40:43], v131 offset:608\n"
"v_mfma_f32_16x16x32_bf16 v[12:15], v[48:51], v[76:79], v[12:15]\n"
"ds_read_b128 v[24:27], v130 offset:10496\n"
"buffer_load_dwordx4 v220, s[20:23], s38 offen offset:2048 sc1 lds\n"
"v_mfma_f32_16x16x32_bf16 v[12:15], v[52:55], v[72:75], v[12:15]\n"
"ds_read_b128 v[44:47], v131 offset:864\n"
"v_mfma_f32_16x16x32_bf16 v[12:15], v[56:59], v[68:71], v[12:15]\n"
"ds_read_b128 v[28:31], v130 offset:14592\n"
"v_mfma_f32_16x16x32_bf16 v[12:15], v[60:63], v[64:67], v[12:15]\n"
"buffer_load_dwordx4 v220, s[20:23], s38 offen offset:3072 sc1 lds\n"
"s_waitcnt lgkmcnt(7)\n"
"v_mfma_f32_16x16x32_bf16 v[0:3], v[80:83], v[96:99], v[0:3]\n"
"s_add_u32 s38, s38, 32768\n"
"s_cmp_eq_u32 s38, s49\n"
"s_cselect_b32 s38, 0, s38\n"
"s_cmp_eq_u32 s38, s50\n"
"s_cselect_b32 s38, 2097152, s38\n"
"s_add_u32 m0, s40, 131072\n"
"s_nop 0\n"
"buffer_load_dwordx4 v219, s[20:23], s38 offen offset:0 sc1 lds\n"
"v_mfma_f32_16x16x32_bf16 v[4:7], v[80:83], v[100:103], v[4:7]\n"
"ds_read_b128 v[64:67], v131 offset:80\n"
"v_mfma_f32_16x16x32_bf16 v[4:7], v[84:87], v[96:99], v[4:7]\n"
"ds_read_b128 v[48:51], v130 offset:2560\n"
"v_mfma_f32_16x16x32_bf16 v[8:11], v[80:83], v[104:107], v[8:11]\n"
"ds_read_b128 v[68:71], v131 offset:336\n"
"buffer_load_dwordx4 v219, s[20:23], s38 offen offset:1024 sc1 lds\n"
"v_mfma_f32_16x16x32_bf16 v[8:11], v[84:87], v[100:103], v[8:11]\n"
"ds_read_b128 v[52:55], v130 offset:6656\n"
"v_mfma_f32_16x16x32_bf16 v[8:11], v[88:91], v[96:99], v[8:11]\n"
"ds_read_b128 v[72:75], v131 offset:592\n"
"v_mfma_f32_16x16x32_bf16 v[12:15], v[80:83], v[108:111], v[12:15]\n"
"ds_read_b128 v[56:59], v130 offset:10752\n"
"buffer_load_dwordx4 v219, s[20:23], s38 offen offset:2048 sc1 lds\n"
"v_mfma_f32_16x16x32_bf16 v[12:15], v[84:87], v[104:107], v[12:15]\n"
"ds_read_b128 v[76:79], v131 offset:848\n"
"v_mfma_f32_16x16x32_bf16 v[12:15], v[88:91], v[100:103], v[12:15]\n"
"ds_read_b128 v[60:63], v130 offset:14848\n"
"v_mfma_f32_16x16x32_bf16 v[12:15], v[92:95], v[96:99], v[12:15]\n"
"buffer_load_dwordx4 v219, s[20:23], s38 offen offset:3072 sc1 lds\n"
"s_waitcnt lgkmcnt(7)\n"
"v_mfma_f32_16x16x32_bf16 v[0:3], v[16:19], v[32:35], v[0:3]\n"
"s_add_u32 m0, s40, 135168\n"
"s_nop 0\n"
"buffer_load_dwordx4 v220, s[20:23], s38 offen offset:0 sc1 lds\n"
"v_mfma_f32_16x16x32_bf16 v[4:7], v[16:19], v[36:39], v[4:7]\n"
"ds_read_b128 v[96:99], v131 offset:64\n"
"v_mfma_f32_16x16x32_bf16 v[4:7], v[20:23], v[32:35], v[4:7]\n"
"ds_read_b128 v[80:83], v130 offset:2816\n"
"v_mfma_f32_16x16x32_bf16 v[8:11], v[16:19], v[40:43], v[8:11]\n"
"ds_read_b128 v[100:103], v131 offset:320\n"
"buffer_load_dwordx4 v220, s[20:23], s38 offen offset:1024 sc1 lds\n"
"v_mfma_f32_16x16x32_bf16 v[8:11], v[20:23], v[36:39], v[8:11]\n"
"ds_read_b128 v[84:87], v130 offset:6912\n"
"v_mfma_f32_16x16x32_bf16 v[8:11], v[24:27], v[32:35], v[8:11]\n"
"ds_read_b128 v[104:107], v131 offset:576\n"
"v_mfma_f32_16x16x32_bf16 v[12:15], v[16:19], v[44:47], v[12:15]\n"
"ds_read_b128 v[88:91], v130 offset:11008\n"
"buffer_load_dwordx4 v220, s[20:23], s38 offen offset:2048 sc1 lds\n"
"v_mfma_f32_16x16x32_bf16 v[12:15], v[20:23], v[40:43], v[12:15]\n"
"ds_read_b128 v[108:111], v131 offset:832\n"
"v_mfma_f32_16x16x32_bf16 v[12:15], v[24:27], v[36:39], v[12:15]\n"
"ds_read_b128 v[92:95], v130 offset:15104\n"
"v_mfma_f32_16x16x32_bf16 v[12:15], v[28:31], v[32:35], v[12:15]\n"
"buffer_load_dwordx4 v220, s[20:23], s38 offen offset:3072 sc1 lds\n"
"s_waitcnt lgkmcnt(7)\n"
"v_mfma_f32_16x16x32_bf16 v[0:3], v[48:51], v[64:67], v[0:3]\n"
"s_add_u32 s38, s38, 32768\n"
"v_mfma_f32_16x16x32_bf16 v[4:7], v[48:51], v[68:71], v[4:7]\n"
"ds_read_b128 v[32:35], v131 offset:48\n"
"v_mfma_f32_16x16x32_bf16 v[4:7], v[52:55], v[64:67], v[4:7]\n"
"ds_read_b128 v[16:19], v130 offset:3072\n"
"v_mfma_f32_16x16x32_bf16 v[8:11], v[48:51], v[72:75], v[8:11]\n"
"ds_read_b128 v[36:39], v131 offset:304\n"
"v_mfma_f32_16x16x32_bf16 v[8:11], v[52:55], v[68:71], v[8:11]\n"
"ds_read_b128 v[20:23], v130 offset:7168\n"
"v_mfma_f32_16x16x32_bf16 v[8:11], v[56:59], v[64:67], v[8:11]\n"
"ds_read_b128 v[40:43], v131 offset:560\n"
"v_mfma_f32_16x16x32_bf16 v[12:15], v[48:51], v[76:79], v[12:15]\n"
"ds_read_b128 v[24:27], v130 offset:11264\n"
"v_mfma_f32_16x16x32_bf16 v[12:15], v[52:55], v[72:75], v[12:15]\n"
"ds_read_b128 v[44:47], v131 offset:816\n"
"v_mfma_f32_16x16x32_bf16 v[12:15], v[56:59], v[68:71], v[12:15]\n"
"ds_read_b128 v[28:31], v130 offset:15360\n"
"v_mfma_f32_16x16x32_bf16 v[12:15], v[60:63], v[64:67], v[12:15]\n"
"s_waitcnt lgkmcnt(7)\n"
"v_mfma_f32_16x16x32_bf16 v[0:3], v[80:83], v[96:99], v[0:3]\n"
"v_mfma_f32_16x16x32_bf16 v[4:7], v[80:83], v[100:103], v[4:7]\n"
"ds_read_b128 v[64:67], v131 offset:32\n"
"v_mfma_f32_16x16x32_bf16 v[4:7], v[84:87], v[96:99], v[4:7]\n"
"ds_read_b128 v[48:51], v130 offset:3328\n"
"v_mfma_f32_16x16x32_bf16 v[8:11], v[80:83], v[104:107], v[8:11]\n"
"ds_read_b128 v[68:71], v131 offset:288\n"
"v_mfma_f32_16x16x32_bf16 v[8:11], v[84:87], v[100:103], v[8:11]\n"
"ds_read_b128 v[52:55], v130 offset:7424\n"
"v_mfma_f32_16x16x32_bf16 v[8:11], v[88:91], v[96:99], v[8:11]\n"
"ds_read_b128 v[72:75], v131 offset:544\n"
"v_mfma_f32_16x16x32_bf16 v[12:15], v[80:83], v[108:111], v[12:15]\n"
"ds_read_b128 v[56:59], v130 offset:11520\n"
"v_mfma_f32_16x16x32_bf16 v[12:15], v[84:87], v[104:107], v[12:15]\n"
"ds_read_b128 v[76:79], v131 offset:800\n"
"v_mfma_f32_16x16x32_bf16 v[12:15], v[88:91], v[100:103], v[12:15]\n"
"ds_read_b128 v[60:63], v130 offset:15616\n"
"v_mfma_f32_16x16x32_bf16 v[12:15], v[92:95], v[96:99], v[12:15]\n"
"s_waitcnt lgkmcnt(7)\n"
"v_mfma_f32_16x16x32_bf16 v[0:3], v[16:19], v[32:35], v[0:3]\n"
"v_mfma_f32_16x16x32_bf16 v[4:7], v[16:19], v[36:39], v[4:7]\n"
"ds_read_b128 v[96:99], v131 offset:16\n"
"v_mfma_f32_16x16x32_bf16 v[4:7], v[20:23], v[32:35], v[4:7]\n"
"ds_read_b128 v[80:83], v130 offset:3584\n"
"v_mfma_f32_16x16x32_bf16 v[8:11], v[16:19], v[40:43], v[8:11]\n"
"ds_read_b128 v[100:103], v131 offset:272\n"
"v_mfma_f32_16x16x32_bf16 v[8:11], v[20:23], v[36:39], v[8:11]\n"
"ds_read_b128 v[84:87], v130 offset:7680\n"
"v_mfma_f32_16x16x32_bf16 v[8:11], v[24:27], v[32:35], v[8:11]\n"
"ds_read_b128 v[104:107], v131 offset:528\n"
"v_mfma_f32_16x16x32_bf16 v[12:15], v[16:19], v[44:47], v[12:15]\n"
"ds_read_b128 v[88:91], v130 offset:11776\n"
"v_mfma_f32_16x16x32_bf16 v[12:15], v[20:23], v[40:43], v[12:15]\n"
"ds_read_b128 v[108:111], v131 offset:784\n"
"v_mfma_f32_16x16x32_bf16 v[12:15], v[24:27], v[36:39], v[12:15]\n"
"ds_read_b128 v[92:95], v130 offset:15872\n"
"v_mfma_f32_16x16x32_bf16 v[12:15], v[28:31], v[32:35], v[12:15]\n"
"s_waitcnt lgkmcnt(7)\n"
"v_mfma_f32_16x16x32_bf16 v[0:3], v[48:51], v[64:67], v[0:3]\n"
"v_mfma_f32_16x16x32_bf16 v[4:7], v[48:51], v[68:71], v[4:7]\n"
"ds_read_b128 v[32:35], v131 offset:0\n"
"v_mfma_f32_16x16x32_bf16 v[4:7], v[52:55], v[64:67], v[4:7]\n"
"ds_read_b128 v[16:19], v130 offset:3840\n"
"v_mfma_f32_16x16x32_bf16 v[8:11], v[48:51], v[72:75], v[8:11]\n"
"ds_read_b128 v[36:39], v131 offset:256\n"
"v_mfma_f32_16x16x32_bf16 v[8:11], v[52:55], v[68:71], v[8:11]\n"
"ds_read_b128 v[20:23], v130 offset:7936\n"
"v_mfma_f32_16x16x32_bf16 v[8:11], v[56:59], v[64:67], v[8:11]\n"
"ds_read_b128 v[40:43], v131 offset:512\n"
"v_mfma_f32_16x16x32_bf16 v[12:15], v[48:51], v[76:79], v[12:15]\n"
"ds_read_b128 v[24:27], v130 offset:12032\n"
"v_mfma_f32_16x16x32_bf16 v[12:15], v[52:55], v[72:75], v[12:15]\n"
"ds_read_b128 v[44:47], v131 offset:768\n"
"v_mfma_f32_16x16x32_bf16 v[12:15], v[56:59], v[68:71], v[12:15]\n"
"ds_read_b128 v[28:31], v130 offset:16128\n"
"v_mfma_f32_16x16x32_bf16 v[12:15], v[60:63], v[64:67], v[12:15]\n"
"s_waitcnt lgkmcnt(7)\n"
"v_mfma_f32_16x16x32_bf16 v[0:3], v[80:83], v[96:99], v[0:3]\n"
"v_mfma_f32_16x16x32_bf16 v[4:7], v[80:83], v[100:103], v[4:7]\n"
"v_mfma_f32_16x16x32_bf16 v[4:7], v[84:87], v[96:99], v[4:7]\n"
"v_mfma_f32_16x16x32_bf16 v[8:11], v[80:83], v[104:107], v[8:11]\n"
"v_mfma_f32_16x16x32_bf16 v[8:11], v[84:87], v[100:103], v[8:11]\n"
"v_mfma_f32_16x16x32_bf16 v[8:11], v[88:91], v[96:99], v[8:11]\n"
"v_mfma_f32_16x16x32_bf16 v[12:15], v[80:83], v[108:111], v[12:15]\n"
"v_mfma_f32_16x16x32_bf16 v[12:15], v[84:87], v[104:107], v[12:15]\n"
"v_mfma_f32_16x16x32_bf16 v[12:15], v[88:91], v[100:103], v[12:15]\n"
"v_mfma_f32_16x16x32_bf16 v[12:15], v[92:95], v[96:99], v[12:15]\n"
"s_waitcnt lgkmcnt(0)\n"
"v_mfma_f32_16x16x32_bf16 v[0:3], v[16:19], v[32:35], v[0:3]\n"
"v_mfma_f32_16x16x32_bf16 v[4:7], v[16:19], v[36:39], v[4:7]\n"
"v_mfma_f32_16x16x32_bf16 v[4:7], v[20:23], v[32:35], v[4:7]\n"
"v_mfma_f32_16x16x32_bf16 v[8:11], v[16:19], v[40:43], v[8:11]\n"
"v_mfma_f32_16x16x32_bf16 v[8:11], v[20:23], v[36:39], v[8:11]\n"
"v_mfma_f32_16x16x32_bf16 v[8:11], v[24:27], v[32:35], v[8:11]\n"
"v_mfma_f32_16x16x32_bf16 v[12:15], v[16:19], v[44:47], v[12:15]\n"
"v_mfma_f32_16x16x32_bf16 v[12:15], v[20:23], v[40:43], v[12:15]\n"
"v_mfma_f32_16x16x32_bf16 v[12:15], v[24:27], v[36:39], v[12:15]\n"
"v_mfma_f32_16x16x32_bf16 v[12:15], v[28:31], v[32:35], v[12:15]\n"
"v_cvt_pk_bf16_f32 v140, v0, v1\n"
"v_cvt_pk_bf16_f32 v141, v2, v3\n"
"buffer_store_dwordx2 v[140:141], v129, s[28:31], s72 offen offset:0 sc1\n"
"v_cvt_pk_bf16_f32 v142, v4, v5\n"
"v_cvt_pk_bf16_f32 v143, v6, v7\n"
"buffer_store_dwordx2 v[142:143], v129, s[28:31], s72 offen offset:512 sc1\n"
"v_cvt_pk_bf16_f32 v144, v8, v9\n"
"v_cvt_pk_bf16_f32 v145, v10, v11\n"
"buffer_store_dwordx2 v[144:145], v129, s[28:31], s72 offen offset:1024 sc1\n"
"v_cvt_pk_bf16_f32 v146, v12, v13\n"
"v_cvt_pk_bf16_f32 v147, v14, v15\n"
"buffer_store_dwordx2 v[146:147], v129, s[28:31], s72 offen offset:1536 sc1\n"
"s_cmp_lg_u32 s70, 1\n"
"s_cbranch_scc1 L_no_hq\n"
"v_readlane_b32 s74, v255, 25\n"
"s_mov_b32 s66, 100000\n"
"s_mov_b32 s60, 15\n"
"s_mov_b32 s61, 0\n"
"s_nop 2\n"
"L_poll_q:\n"
"buffer_load_dword v246, v248, s[28:31], s74 offen offset:0 sc1\n"
"s_waitcnt vmcnt(0)\n"
"v_cmp_eq_u32 vcc, s57, v246\n"
"s_nop 0\n"
"s_and_b64 s[62:63], vcc, s[60:61]\n"
"s_cmp_eq_u64 s[62:63], s[60:61]\n"
"s_cbranch_scc1 L_got_q\n"
"s_sleep 8\n"
"s_sub_u32 s66, s66, 1\n"
"s_cbranch_scc0 L_poll_q\n"
"L_got_q:\n"
"v_readlane_b32 s45, v255, 21\n"
"s_lshl_b32 s74, s46, 13\n"
"s_add_u32 s44, s45, s74\n"
"buffer_load_dwordx4 a[0:3], v225, s[28:31], s44 offen offset:0 sc1\n"
"buffer_load_dwordx4 a[4:7], v225, s[28:31], s44 offen offset:1024 sc1\n"
"buffer_load_dwordx4 a[8:11], v225, s[28:31], s44 offen offset:2048 sc1\n"
"buffer_load_dwordx4 a[12:15], v225, s[28:31], s44 offen offset:3072 sc1\n"
"s_add_u32 s44, s44, 4096\n"
"buffer_load_dwordx4 a[16:19], v225, s[28:31], s44 offen offset:0 sc1\n"
"buffer_load_dwordx4 a[20:23], v225, s[28:31], s44 offen offset:1024 sc1\n"
"buffer_load_dwordx4 a[24:27], v225, s[28:31], s44 offen offset:2048 sc1\n"
"buffer_load_dwordx4 a[28:31], v225, s[28:31], s44 offen offset:3072 sc1\n"
"s_sub_u32 s44, s45, s74\n"
"s_add_u32 s44, s44, 57344\n"
"buffer_load_dwordx4 a[32:35], v225, s[28:31], s44 offen offset:0 sc1\n"
"buffer_load_dwordx4 a[36:39], v225, s[28:31], s44 offen offset:1024 sc1\n"
"buffer_load_dwordx4 a[40:43], v225, s[28:31], s44 offen offset:2048 sc1\n"
"buffer_load_dwordx4 a[44:47], v225, s[28:31], s44 offen offset:3072 sc1\n"
"s_add_u32 s44, s44, 4096\n"
"buffer_load_dwordx4 a[48:51], v225, s[28:31], s44 offen offset:0 sc1\n"
"buffer_load_dwordx4 a[52:55], v225, s[28:31], s44 offen offset:1024 sc1\n"
"buffer_load_dwordx4 a[56:59], v225, s[28:31], s44 offen offset:2048 sc1\n"
"buffer_load_dwordx4 a[60:63], v225, s[28:31], s44 offen offset:3072 sc1\n"
"L_no_hq:\n"
"v_mov_b32 v176, 0\n"
"v_mov_b32 v177, 0\n"
"v_mov_b32 v178, 0\n"
"v_mov_b32 v179, 0\n"
"v_mov_b32 v180, 0\n"
"v_mov_b32 v181, 0\n"
"v_mov_b32 v182, 0\n"
"v_mov_b32 v183, 0\n"
"s_nop 7\n"
"v_mfma_f32_32x32x16_bf16 v[0:15], v[176:179], v[176:179], 0\n"
"v_mfma_f32_32x32x16_bf16 v[16:31], v[176:179], v[176:179], 0\n"
"v_mfma_f32_32x32x16_bf16 v[32:47], v[176:179], v[176:179], 0\n"
"v_mfma_f32_32x32x16_bf16 v[48:63], v[176:179], v[176:179], 0\n"
"v_mfma_f32_32x32x16_bf16 v[64:79], v[176:179], v[176:179], 0\n"
"v_mfma_f32_32x32x16_bf16 v[80:95], v[176:179], v[176:179], 0\n"
"v_mfma_f32_32x32x16_bf16 v[96:111], v[176:179], v[176:179], 0\n"
"v_mfma_f32_32x32x16_bf16 v[112:127], v[176:179], v[176:179], 0\n"
"v_mov_b32 v200, 0\n"
"v_mov_b32 v201, 0\n"
"s_mov_b32 s42, 0xbf800000\n"
"s_mov_b32 s43, 0xbf800000\n"
"s_waitcnt vmcnt(12)\n"
"s_barrier\n"
"ds_read_b128 a[64:67], v216 offset:0\n"
"ds_read_b128 a[68:71], v216 offset:1024\n"
"ds_read_b128 a[72:75], v216 offset:2048\n"
"ds_read_b128 a[76:79], v216 offset:3072\n"
"ds_read_b128 a[80:83], v216 offset:4096\n"
"ds_read_b128 a[84:87], v216 offset:5120\n"
"ds_read_b128 a[88:91], v216 offset:6144\n"
"ds_read_b128 a[92:95], v216 offset:7168\n"
"s_waitcnt lgkmcnt(4)\n"
"ds_read_b128 a[96:99], v216 offset:8192\n"
"ds_read_b128 a[100:103], v216 offset:9216\n"
"ds_read_b128 a[104:107], v216 offset:10240\n"
"ds_read_b128 a[108:111], v216 offset:11264\n"
"ds_read_b128 a[112:115], v216 offset:12288\n"
"ds_read_b128 a[116:119], v216 offset:13312\n"
"ds_read_b128 a[120:123], v216 offset:14336\n"
"ds_read_b128 a[124:127], v216 offset:15360\n"
"s_waitcnt lgkmcnt(4)\n"
"ds_read_b128 a[128:131], v217 offset:0\n"
"ds_read_b128 a[132:135], v217 offset:1024\n"
"ds_read_b128 a[136:139], v217 offset:2048\n"
"ds_read_b128 a[140:143], v217 offset:3072\n"
"ds_read_b128 a[144:147], v217 offset:4096\n"
"ds_read_b128 a[148:151], v217 offset:5120\n"
"ds_read_b128 a[152:155], v217 offset:6144\n"
"s_add_u32 m0, s40, 0\n"
"s_nop 0\n"
"buffer_load_dwordx4 v219, s[20:23], s38 offen offset:0 sc1 lds\n"
"buffer_load_dwordx4 v219, s[20:23], s38 offen offset:1024 sc1 lds\n"
"buffer_load_dwordx4 v219, s[20:23], s38 offen offset:2048 sc1 lds\n"
"buffer_load_dwordx4 v219, s[20:23], s38 offen offset:3072 sc1 lds\n"
"s_add_u32 m0, s40, 4096\n"
"s_nop 0\n"
"buffer_load_dwordx4 v220, s[20:23], s38 offen offset:0 sc1 lds\n"
"buffer_load_dwordx4 v220, s[20:23], s38 offen offset:1024 sc1 lds\n"
"buffer_load_dwordx4 v220, s[20:23], s38 offen offset:2048 sc1 lds\n"
"buffer_load_dwordx4 v220, s[20:23], s38 offen offset:3072 sc1 lds\n"
"s_add_u32 s38, s38, 32768\n"
"s_cmp_eq_u32 s38, s49\n"
"s_cselect_b32 s38, 0, s38\n"
"s_cmp_eq_u32 s38, s50\n"
"s_cselect_b32 s38, 2097152, s38\n"
"s_add_u32 m0, s40, 32768\n"
"s_nop 0\n"
"buffer_load_dwordx4 v219, s[20:23], s38 offen offset:0 sc1 lds\n"
"buffer_load_dwordx4 v219, s[20:23], s38 offen offset:1024 sc1 lds\n"
"buffer_load_dwordx4 v219, s[20:23], s38 offen offset:2048 sc1 lds\n"
"buffer_load_dwordx4 v219, s[20:23], s38 offen offset:3072 sc1 lds\n"
"s_waitcnt vmcnt(12)\n"
"v_readlane_b32 s44, v255, 35\n"
"v_mov_b32 v246, 0x7ffffff0\n"
"v_writelane_b32 v246, 0, 0\n"
"v_mov_b32 v247, s57\n"
"s_nop 1\n"
"buffer_store_dword v247, v246, s[28:31], s44 offen offset:0 sc1\n"
"s_getpc_b64 s[60:61]\n"
"L_ct_2:\n"
"s_sub_u32 s60, s60, L_ct_2-L_code_begin\n"
"s_subb_u32 s61, s61, 0\n"
"s_and_b32 s61, s61, 0xffff\n"
"s_mov_b32 s62, L_code_end-L_code_begin\n"
"s_mov_b32 s63, 0x20000\n"
"v_readlane_b32 s44, v255, 44\n"
"v_mbcnt_lo_u32_b32 v253, -1, 0\n"
"v_and_b32 v253, 3, v253\n"
"v_lshlrev_b32 v253, 7, v253\n"
"s_nop 1\n"
"buffer_load_dword v252, v253, s[60:63], s44 offen offset:0\n"
"v_readlane_b32 s58, v255, 45\n"
"s_mov_b32 s59, 0x7fffff00\n"
"s_nop 1\n"
"L_at_seg:\n"
"s_sub_u32 s41, s51, 1\n"
"s_cbranch_scc1 L_at_mask0\n"
"L_at_norm:\n"
"s_waitcnt lgkmcnt(0)\n"
"v_mfma_f32_16x16x32_bf16 v[128:131], a[64:67], a[0:3], v[176:179]\n"
"ds_read_b128 a[156:159], v217 offset:7168\n"
"s_add_u32 m0, m0, 4096\n"
"v_mfma_f32_16x16x32_bf16 v[128:131], a[68:71], a[4:7], v[128:131]\n"
"ds_read_b128 a[160:163], v217 offset:8192\n"
"buffer_load_dwordx4 v220, s[20:23], s38 offen offset:0 sc1 lds\n"
"v_mfma_f32_16x16x32_bf16 v[128:131], a[72:75], a[8:11], v[128:131]\n"
"ds_read_b128 a[164:167], v217 offset:9216\n"
"v_add_u32 v224, s36, v218\n"
"v_mfma_f32_16x16x32_bf16 v[128:131], a[76:79], a[12:15], v[128:131]\n"
"ds_read_b128 a[168:171], v217 offset:10240\n"
"s_add_u32 s68, s37, 32768\n"
"v_mfma_f32_16x16x32_bf16 v[128:131], a[80:83], a[16:19], v[128:131]\n"
"ds_read_b128 a[172:175], v217 offset:11264\n"
"s_cmp_ge_u32 s68, s39\n"
"v_mfma_f32_16x16x32_bf16 v[128:131], a[84:87], a[20:23], v[128:131]\n"
"ds_read_b128 a[176:179], v217 offset:12288\n"
"buffer_load_dwordx4 v220, s[20:23], s38 offen offset:1024 sc1 lds\n"
"v_mfma_f32_16x16x32_bf16 v[128:131], a[88:91], a[24:27], v[128:131]\n"
"ds_read_b128 a[180:183], v217 offset:13312\n"
"s_cselect_b32 s68, s40, s68\n"
"v_mfma_f32_16x16x32_bf16 v[128:131], a[92:95], a[28:31], v[128:131]\n"
"ds_read_b128 a[184:187], v217 offset:14336\n"
"v_add_u32 v216, s68, v218\n"
"v_mfma_f32_16x16x32_bf16 v[132:135], a[64:67], a[32:35], v[180:183]\n"
"ds_read_b128 a[188:191], v217 offset:15360\n"
"s_add_u32 s69, s38, 32768\n"
"v_mfma_f32_16x16x32_bf16 v[132:135], a[68:71], a[36:39], v[132:135]\n"
"ds_read_b128 a[192:195], v217 offset:16384\n"
"buffer_load_dwordx4 v220, s[20:23], s38 offen offset:2048 sc1 lds\n"
"v_mfma_f32_16x16x32_bf16 v[132:135], a[72:75], a[40:43], v[132:135]\n"
"ds_read_b128 a[196:199], v217 offset:17408\n"
"ds_read_b128 a[64:67], v224 offset:16384\n"
"v_mfma_f32_16x16x32_bf16 v[132:135], a[76:79], a[44:47], v[132:135]\n"
"v_exp_f32 v184, v128\n"
"v_mfma_f32_16x16x32_bf16 v[132:135], a[80:83], a[48:51], v[132:135]\n"
"ds_read_b128 a[68:71], v224 offset:17408\n"
"v_mfma_f32_16x16x32_bf16 v[132:135], a[84:87], a[52:55], v[132:135]\n"
"buffer_load_dwordx4 v220, s[20:23], s38 offen offset:3072 sc1 lds\n"
"v_mfma_f32_16x16x32_bf16 v[132:135], a[88:91], a[56:59], v[132:135]\n"
"v_exp_f32 v185, v129\n"
"v_mfma_f32_16x16x32_bf16 v[132:135], a[92:95], a[60:63], v[132:135]\n"
"ds_read_b128 a[72:75], v224 offset:18432\n"
"v_mfma_f32_16x16x32_bf16 v[136:139], a[96:99], a[0:3], v[176:179]\n"
"ds_read_b128 a[76:79], v224 offset:19456\n"
"v_add_f32 v202, v184, v185\n"
"v_mfma_f32_16x16x32_bf16 v[136:139], a[100:103], a[4:7], v[136:139]\n"
"v_exp_f32 v186, v130\n"
"v_mfma_f32_16x16x32_bf16 v[136:139], a[104:107], a[8:11], v[136:139]\n"
"ds_read_b128 a[80:83], v224 offset:20480\n"
"v_cvt_pk_bf16_f32 v160, v184, v185\n"
"v_mfma_f32_16x16x32_bf16 v[136:139], a[108:111], a[12:15], v[136:139]\n"
"s_add_u32 s38, s38, 65536\n"
"v_mfma_f32_16x16x32_bf16 v[136:139], a[112:115], a[16:19], v[136:139]\n"
"v_exp_f32 v187, v131\n"
"v_mfma_f32_16x16x32_bf16 v[136:139], a[116:119], a[20:23], v[136:139]\n"
"ds_read_b128 a[84:87], v224 offset:21504\n"
"v_add_f32 v202, v202, v186\n"
"v_mfma_f32_16x16x32_bf16 v[136:139], a[120:123], a[24:27], v[136:139]\n"
"v_exp_f32 v192, v132\n"
"v_mfma_f32_16x16x32_bf16 v[136:139], a[124:127], a[28:31], v[136:139]\n"
"ds_read_b128 a[88:91], v224 offset:22528\n"
"v_add_f32 v202, v202, v187\n"
"v_mfma_f32_16x16x32_bf16 v[140:143], a[96:99], a[32:35], v[180:183]\n"
"ds_read_b128 a[92:95], v224 offset:23552\n"
"v_cvt_pk_bf16_f32 v161, v186, v187\n"
"v_mfma_f32_16x16x32_bf16 v[140:143], a[100:103], a[36:39], v[140:143]\n"
"v_exp_f32 v193, v133\n"
"v_mfma_f32_16x16x32_bf16 v[140:143], a[104:107], a[40:43], v[140:143]\n"
"ds_read_b128 a[96:99], v224 offset:24576\n"
"v_add_f32 v203, v192, v193\n"
"v_mfma_f32_16x16x32_bf16 v[140:143], a[108:111], a[44:47], v[140:143]\n"
"v_cvt_pk_bf16_f32 v168, v192, v193\n"
"s_cmp_eq_u32 s38, s49\n"
"v_mfma_f32_16x16x32_bf16 v[140:143], a[112:115], a[48:51], v[140:143]\n"
"v_exp_f32 v194, v134\n"
"v_mfma_f32_16x16x32_bf16 v[140:143], a[116:119], a[52:55], v[140:143]\n"
"ds_read_b128 a[100:103], v224 offset:25600\n"
"s_cselect_b32 s38, 0, s38\n"
"v_mfma_f32_16x16x32_bf16 v[140:143], a[120:123], a[56:59], v[140:143]\n"
"ds_read_b128 a[104:107], v224 offset:26624\n"
"s_cmp_eq_u32 s38, s50\n"
"v_mfma_f32_16x16x32_bf16 v[140:143], a[124:127], a[60:63], v[140:143]\n"
"s_cselect_b32 s38, 2097152, s38\n"
"s_waitcnt lgkmcnt(3)\n"
"v_mfma_f32_16x16x32_bf16 v[144:147], a[64:67], a[0:3], v[176:179]\n"
"v_exp_f32 v195, v135\n"
"v_mfma_f32_16x16x32_bf16 v[144:147], a[68:71], a[4:7], v[144:147]\n"
"ds_read_b128 a[108:111], v224 offset:27648\n"
"v_add_f32 v203, v203, v194\n"
"v_mfma_f32_16x16x32_bf16 v[144:147], a[72:75], a[8:11], v[144:147]\n"
"v_exp_f32 v188, v136\n"
"v_mfma_f32_16x16x32_bf16 v[144:147], a[76:79], a[12:15], v[144:147]\n"
"ds_read_b128 a[112:115], v224 offset:28672\n"
"v_add_f32 v203, v203, v195\n"
"v_mfma_f32_16x16x32_bf16 v[144:147], a[80:83], a[16:19], v[144:147]\n"
"ds_read_b128 a[116:119], v224 offset:29696\n"
"v_cvt_pk_bf16_f32 v169, v194, v195\n"
"v_mfma_f32_16x16x32_bf16 v[144:147], a[84:87], a[20:23], v[144:147]\n"
"v_exp_f32 v189, v137\n"
"v_mfma_f32_16x16x32_bf16 v[144:147], a[88:91], a[24:27], v[144:147]\n"
"ds_read_b128 a[120:123], v224 offset:30720\n"
"v_add_f32 v202, v202, v188\n"
"v_mfma_f32_16x16x32_bf16 v[144:147], a[92:95], a[28:31], v[144:147]\n"
"v_add_f32 v202, v202, v189\n"
"v_cvt_pk_bf16_f32 v162, v188, v189\n"
"v_mfma_f32_16x16x32_bf16 v[148:151], a[64:67], a[32:35], v[180:183]\n"
"v_exp_f32 v190, v138\n"
"v_mfma_f32_16x16x32_bf16 v[148:151], a[68:71], a[36:39], v[148:151]\n"
"ds_read_b128 a[124:127], v224 offset:31744\n"
"ds_read_b128 a[200:203], v217 offset:18432\n"
"v_mfma_f32_16x16x32_bf16 v[148:151], a[72:75], a[40:43], v[148:151]\n"
"v_mfma_f32_16x16x32_bf16 v[148:151], a[76:79], a[44:47], v[148:151]\n"
"v_exp_f32 v191, v139\n"
"v_mfma_f32_16x16x32_bf16 v[148:151], a[80:83], a[48:51], v[148:151]\n"
"ds_read_b128 a[204:207], v217 offset:19456\n"
"v_add_f32 v202, v202, v190\n"
"v_mfma_f32_16x16x32_bf16 v[148:151], a[84:87], a[52:55], v[148:151]\n"
"v_exp_f32 v196, v140\n"
"v_mfma_f32_16x16x32_bf16 v[148:151], a[88:91], a[56:59], v[148:151]\n"
"v_add_f32 v202, v202, v191\n"
"v_cvt_pk_bf16_f32 v163, v190, v191\n"
"v_mfma_f32_16x16x32_bf16 v[148:151], a[92:95], a[60:63], v[148:151]\n"
"s_waitcnt lgkmcnt(3)\n"
"v_mfma_f32_16x16x32_bf16 v[152:155], a[96:99], a[0:3], v[176:179]\n"
"s_waitcnt vmcnt(16) lgkmcnt(2)\n"
"s_barrier\n"
"v_mfma_f32_16x16x32_bf16 v[152:155], a[100:103], a[4:7], v[152:155]\n"
"v_exp_f32 v197, v141\n"
"v_mfma_f32_16x16x32_bf16 v[152:155], a[104:107], a[8:11], v[152:155]\n"
"s_mov_b32 m0, s36\n"
"ds_read_b128 a[208:211], v217 offset:20480\n"
"v_mfma_f32_16x16x32_bf16 v[152:155], a[108:111], a[12:15], v[152:155]\n"
"buffer_load_dwordx4 v219, s[20:23], s69 offen offset:0 sc1 lds\n"
"v_cmp_lt_f32 vcc, s42, v202\n"
"v_mfma_f32_16x16x32_bf16 v[152:155], a[112:115], a[16:19], v[152:155]\n"
"v_exp_f32 v198, v142\n"
"v_mfma_f32_16x16x32_bf16 v[152:155], a[116:119], a[20:23], v[152:155]\n"
"ds_read_b128 a[212:215], v217 offset:21504\n"
"v_add_f32 v203, v203, v196\n"
"v_mfma_f32_16x16x32_bf16 v[152:155], a[120:123], a[24:27], v[152:155]\n"
"v_add_f32 v203, v203, v197\n"
"v_cvt_pk_bf16_f32 v170, v196, v197\n"
"v_mfma_f32_16x16x32_bf16 v[152:155], a[124:127], a[28:31], v[152:155]\n"
"v_exp_f32 v199, v143\n"
"v_mfma_f32_16x16x32_bf16 v[156:159], a[96:99], a[32:35], v[180:183]\n"
"buffer_load_dwordx4 v219, s[20:23], s69 offen offset:1024 sc1 lds\n"
"ds_read_b128 a[216:219], v217 offset:22528\n"
"v_mfma_f32_16x16x32_bf16 v[156:159], a[100:103], a[36:39], v[156:159]\n"
"v_exp_f32 v184, v144\n"
"v_mfma_f32_16x16x32_bf16 v[156:159], a[104:107], a[40:43], v[156:159]\n"
"v_add_f32 v203, v203, v198\n"
"v_add_f32 v203, v203, v199\n"
"v_mfma_f32_16x16x32_bf16 v[156:159], a[108:111], a[44:47], v[156:159]\n"
"v_cvt_pk_bf16_f32 v171, v198, v199\n"
"v_mfma_f32_16x16x32_bf16 v[156:159], a[112:115], a[48:51], v[156:159]\n"
"v_exp_f32 v185, v145\n"
"v_mfma_f32_16x16x32_bf16 v[156:159], a[116:119], a[52:55], v[156:159]\n"
"buffer_load_dwordx4 v219, s[20:23], s69 offen offset:2048 sc1 lds\n"
"ds_read_b128 a[220:223], v217 offset:23552\n"
"v_mfma_f32_16x16x32_bf16 v[156:159], a[120:123], a[56:59], v[156:159]\n"
"v_add_f32 v204, v184, v185\n"
"v_cvt_pk_bf16_f32 v164, v184, v185\n"
"v_mfma_f32_16x16x32_bf16 v[156:159], a[124:127], a[60:63], v[156:159]\n"
"s_cbranch_vccnz L_slow_n_00\n"
"L_ret_n_00:\n"
"v_mfma_f32_16x16x32_bf16 v[0:3], a[128:131], v[160:163], v[0:3]\n"
"v_exp_f32 v186, v146\n"
"v_mfma_f32_16x16x32_bf16 v[8:11], a[132:135], v[160:163], v[8:11]\n"
"ds_read_b128 a[224:227], v217 offset:24576\n"
"v_cmp_lt_f32 vcc, s43, v203\n"
"v_mfma_f32_16x16x32_bf16 v[16:19], a[136:139], v[160:163], v[16:19]\n"
"buffer_load_dwordx4 v219, s[20:23], s69 offen offset:3072 sc1 lds\n"
"s_add_u32 m0, m0, 4096\n"
"v_mfma_f32_16x16x32_bf16 v[24:27], a[140:143], v[160:163], v[24:27]\n"
"v_exp_f32 v187, v147\n"
"v_mfma_f32_16x16x32_bf16 v[32:35], a[144:147], v[160:163], v[32:35]\n"
"v_exp_f32 v188, v152\n"
"v_mfma_f32_16x16x32_bf16 v[40:43], a[148:151], v[160:163], v[40:43]\n"
"ds_read_b128 a[228:231], v217 offset:25600\n"
"v_add_f32 v204, v204, v186\n"
"v_mfma_f32_16x16x32_bf16 v[48:51], a[152:155], v[160:163], v[48:51]\n"
"v_add_f32 v204, v204, v187\n"
"v_cvt_pk_bf16_f32 v165, v186, v187\n"
"v_mfma_f32_16x16x32_bf16 v[56:59], a[156:159], v[160:163], v[56:59]\n"
"v_exp_f32 v189, v153\n"
"v_mfma_f32_16x16x32_bf16 v[64:67], a[160:163], v[160:163], v[64:67]\n"
"buffer_load_dwordx4 v220, s[20:23], s69 offen offset:0 sc1 lds\n"
"ds_read_b128 a[232:235], v217 offset:26624\n"
"v_mfma_f32_16x16x32_bf16 v[72:75], a[164:167], v[160:163], v[72:75]\n"
"v_add_f32 v204, v204, v188\n"
"v_add_f32 v204, v204, v189\n"
"v_mfma_f32_16x16x32_bf16 v[80:83], a[168:171], v[160:163], v[80:83]\n"
"v_exp_f32 v190, v154\n"
"v_mfma_f32_16x16x32_bf16 v[88:91], a[172:175], v[160:163], v[88:91]\n"
"v_cvt_pk_bf16_f32 v166, v188, v189\n"
"v_add_f32 v200, v200, v202\n"
"v_mfma_f32_16x16x32_bf16 v[96:99], a[176:179], v[160:163], v[96:99]\n"
"ds_read_b128 a[236:239], v217 offset:27648\n"
"ds_read_b128 a[64:67], v216 offset:0\n"
"v_mfma_f32_16x16x32_bf16 v[104:107], a[180:183], v[160:163], v[104:107]\n"
"v_exp_f32 v191, v155\n"
"v_mfma_f32_16x16x32_bf16 v[112:115], a[184:187], v[160:163], v[112:115]\n"
"buffer_load_dwordx4 v220, s[20:23], s69 offen offset:1024 sc1 lds\n"
"v_add_f32 v204, v204, v190\n"
"v_mfma_f32_16x16x32_bf16 v[120:123], a[188:191], v[160:163], v[120:123]\n"
"s_cbranch_vccnz L_slow_n_01\n"
"L_ret_n_01:\n"
"v_add_f32 v204, v204, v191\n"
"v_mfma_f32_16x16x32_bf16 v[4:7], a[128:131], v[168:171], v[4:7]\n"
"v_exp_f32 v192, v148\n"
"v_mfma_f32_16x16x32_bf16 v[12:15], a[132:135], v[168:171], v[12:15]\n"
"ds_read_b128 a[240:243], v217 offset:28672\n"
"v_cvt_pk_bf16_f32 v167, v190, v191\n"
"v_mfma_f32_16x16x32_bf16 v[20:23], a[136:139], v[168:171], v[20:23]\n"
"v_cmp_lt_f32 vcc, s42, v204\n"
"v_add_f32 v201, v201, v203\n"
"v_mfma_f32_16x16x32_bf16 v[28:31], a[140:143], v[168:171], v[28:31]\n"
"v_exp_f32 v193, v149\n"
"v_mfma_f32_16x16x32_bf16 v[36:39], a[144:147], v[168:171], v[36:39]\n"
"buffer_load_dwordx4 v220, s[20:23], s69 offen offset:2048 sc1 lds\n"
"ds_read_b128 a[244:247], v217 offset:29696\n"
"v_mfma_f32_16x16x32_bf16 v[44:47], a[148:151], v[168:171], v[44:47]\n"
"v_add_f32 v205, v192, v193\n"
"v_cvt_pk_bf16_f32 v172, v192, v193\n"
"v_mfma_f32_16x16x32_bf16 v[52:55], a[152:155], v[168:171], v[52:55]\n"
"v_exp_f32 v194, v150\n"
"v_mfma_f32_16x16x32_bf16 v[60:63], a[156:159], v[168:171], v[60:63]\n"
"ds_read_b128 a[68:71], v216 offset:1024\n"
"v_mfma_f32_16x16x32_bf16 v[68:71], a[160:163], v[168:171], v[68:71]\n"
"ds_read_b128 a[248:251], v217 offset:30720\n"
"ds_read_b128 a[72:75], v216 offset:2048\n"
"v_mfma_f32_16x16x32_bf16 v[76:79], a[164:167], v[168:171], v[76:79]\n"
"v_exp_f32 v195, v151\n"
"v_mfma_f32_16x16x32_bf16 v[84:87], a[168:171], v[168:171], v[84:87]\n"
"buffer_load_dwordx4 v220, s[20:23], s69 offen offset:3072 sc1 lds\n"
"v_add_f32 v205, v205, v194\n"
"v_mfma_f32_16x16x32_bf16 v[92:95], a[172:175], v[168:171], v[92:95]\n"
"v_exp_f32 v196, v156\n"
"v_mfma_f32_16x16x32_bf16 v[100:103], a[176:179], v[168:171], v[100:103]\n"
"ds_read_b128 a[252:255], v217 offset:31744\n"
"v_add_f32 v205, v205, v195\n"
"v_mfma_f32_16x16x32_bf16 v[108:111], a[180:183], v[168:171], v[108:111]\n"
"v_cvt_pk_bf16_f32 v173, v194, v195\n"
"ds_read_b128 a[76:79], v216 offset:3072\n"
"v_mfma_f32_16x16x32_bf16 v[116:119], a[184:187], v[168:171], v[116:119]\n"
"ds_read_b128 a[80:83], v216 offset:4096\n"
"v_mfma_f32_16x16x32_bf16 v[124:127], a[188:191], v[168:171], v[124:127]\n"
"s_cbranch_vccnz L_slow_n_10\n"
"L_ret_n_10:\n"
"ds_read_b128 a[84:87], v216 offset:5120\n"
"v_mfma_f32_16x16x32_bf16 v[0:3], a[192:195], v[164:167], v[0:3]\n"
"v_exp_f32 v197, v157\n"
"v_mfma_f32_16x16x32_bf16 v[8:11], a[196:199], v[164:167], v[8:11]\n"
"v_add_f32 v205, v205, v196\n"
"v_add_f32 v205, v205, v197\n"
"s_waitcnt lgkmcnt(4)\n"
"v_mfma_f32_16x16x32_bf16 v[16:19], a[200:203], v[164:167], v[16:19]\n"
"s_waitcnt vmcnt(16) lgkmcnt(3)\n"
"s_barrier\n"
"v_mfma_f32_16x16x32_bf16 v[24:27], a[204:207], v[164:167], v[24:27]\n"
"v_exp_f32 v198, v158\n"
"v_mfma_f32_16x16x32_bf16 v[32:35], a[208:211], v[164:167], v[32:35]\n"
"v_cvt_pk_bf16_f32 v174, v196, v197\n"
"s_mov_b32 m0, s37\n"
"v_mfma_f32_16x16x32_bf16 v[40:43], a[212:215], v[164:167], v[40:43]\n"
"buffer_load_dwordx4 v219, s[20:23], s38 offen offset:0 sc1 lds\n"
"s_add_u32 s37, s68, 32768\n"
"v_mfma_f32_16x16x32_bf16 v[48:51], a[216:219], v[164:167], v[48:51]\n"
"v_exp_f32 v199, v159\n"
"v_mfma_f32_16x16x32_bf16 v[56:59], a[220:223], v[164:167], v[56:59]\n"
"v_add_f32 v205, v205, v198\n"
"v_add_f32 v205, v205, v199\n"
"v_mfma_f32_16x16x32_bf16 v[64:67], a[224:227], v[164:167], v[64:67]\n"
"v_cvt_pk_bf16_f32 v175, v198, v199\n"
"v_add_f32 v200, v200, v204\n"
"v_mfma_f32_16x16x32_bf16 v[72:75], a[228:231], v[164:167], v[72:75]\n"
"v_cmp_lt_f32 vcc, s43, v205\n"
"ds_read_b128 a[88:91], v216 offset:6144\n"
"v_mfma_f32_16x16x32_bf16 v[80:83], a[232:235], v[164:167], v[80:83]\n"
"s_cmp_ge_u32 s37, s39\n"
"s_cselect_b32 s37, s40, s37\n"
"v_mfma_f32_16x16x32_bf16 v[88:91], a[236:239], v[164:167], v[88:91]\n"
"buffer_load_dwordx4 v219, s[20:23], s38 offen offset:1024 sc1 lds\n"
"v_add_u32 v217, s37, v218\n"
"v_mfma_f32_16x16x32_bf16 v[96:99], a[240:243], v[164:167], v[96:99]\n"
"s_mov_b32 s36, s68\n"
"ds_read_b128 a[92:95], v216 offset:7168\n"
"v_mfma_f32_16x16x32_bf16 v[104:107], a[244:247], v[164:167], v[104:107]\n"
"ds_read_b128 a[96:99], v216 offset:8192\n"
"v_mfma_f32_16x16x32_bf16 v[112:115], a[248:251], v[164:167], v[112:115]\n"
"ds_read_b128 a[100:103], v216 offset:9216\n"
"v_mfma_f32_16x16x32_bf16 v[120:123], a[252:255], v[164:167], v[120:123]\n"
"s_cbranch_vccnz L_slow_n_11\n"
"L_ret_n_11:\n"
"ds_read_b128 a[104:107], v216 offset:10240\n"
"v_mfma_f32_16x16x32_bf16 v[4:7], a[192:195], v[172:175], v[4:7]\n"
"v_add_f32 v201, v201, v205\n"
"ds_read_b128 a[108:111], v216 offset:11264\n"
"v_mfma_f32_16x16x32_bf16 v[12:15], a[196:199], v[172:175], v[12:15]\n"
"buffer_load_dwordx4 v219, s[20:23], s38 offen offset:2048 sc1 lds\n"
"ds_read_b128 a[112:115], v216 offset:12288\n"
"v_mfma_f32_16x16x32_bf16 v[20:23], a[200:203], v[172:175], v[20:23]\n"
"ds_read_b128 a[116:119], v216 offset:13312\n"
"v_mfma_f32_16x16x32_bf16 v[28:31], a[204:207], v[172:175], v[28:31]\n"
"ds_read_b128 a[120:123], v216 offset:14336\n"
"v_mfma_f32_16x16x32_bf16 v[36:39], a[208:211], v[172:175], v[36:39]\n"
"ds_read_b128 a[124:127], v216 offset:15360\n"
"v_mfma_f32_16x16x32_bf16 v[44:47], a[212:215], v[172:175], v[44:47]\n"
"ds_read_b128 a[128:131], v217 offset:0\n"
"v_mfma_f32_16x16x32_bf16 v[52:55], a[216:219], v[172:175], v[52:55]\n"
"ds_read_b128 a[132:135], v217 offset:1024\n"
"v_mfma_f32_16x16x32_bf16 v[60:63], a[220:223], v[172:175], v[60:63]\n"
"buffer_load_dwordx4 v219, s[20:23], s38 offen offset:3072 sc1 lds\n"
"ds_read_b128 a[136:139], v217 offset:2048\n"
"v_mfma_f32_16x16x32_bf16 v[68:71], a[224:227], v[172:175], v[68:71]\n"
"ds_read_b128 a[140:143], v217 offset:3072\n"
"v_mfma_f32_16x16x32_bf16 v[76:79], a[228:231], v[172:175], v[76:79]\n"
"ds_read_b128 a[144:147], v217 offset:4096\n"
"v_mfma_f32_16x16x32_bf16 v[84:87], a[232:235], v[172:175], v[84:87]\n"
"ds_read_b128 a[148:151], v217 offset:5120\n"
"v_mfma_f32_16x16x32_bf16 v[92:95], a[236:239], v[172:175], v[92:95]\n"
"ds_read_b128 a[152:155], v217 offset:6144\n"
"v_mfma_f32_16x16x32_bf16 v[100:103], a[240:243], v[172:175], v[100:103]\n"
"v_mfma_f32_16x16x32_bf16 v[108:111], a[244:247], v[172:175], v[108:111]\n"
"v_mfma_f32_16x16x32_bf16 v[116:119], a[248:251], v[172:175], v[116:119]\n"
"v_mfma_f32_16x16x32_bf16 v[124:127], a[252:255], v[172:175], v[124:127]\n"
"s_sub_u32 s41, s41, 1\n"
"s_cbranch_scc0 L_at_norm\n"
"s_cmp_eq_u32 s58, 0\n"
"s_cbranch_scc1 L_at_mask0\n"
"s_cmp_eq_u32 s58, -1\n"
"s_cbranch_scc1 L_at_epi\n"
"s_cmp_eq_u32 s58, -2\n"
"s_cbranch_scc1 L_at_run3\n"
"v_mov_b32 v246, 0x7ffffff0\n"
"v_writelane_b32 v246, 0, 0\n"
"v_mov_b32 v247, s57\n"
"s_waitcnt vmcnt(16)\n"
"buffer_store_dword v247, v246, s[28:31], s59 offen offset:0 sc1\n"
"s_mov_b32 s59, 0x7fffff00\n"
"s_sub_u32 s41, s58, 1\n"
"s_mov_b32 s58, 0\n"
"s_branch L_at_norm\n"
"L_at_run3:\n"
"s_getpc_b64 s[60:61]\n"
"L_ct_3:\n"
"s_sub_u32 s60, s60, L_ct_3-L_code_begin\n"
"s_subb_u32 s61, s61, 0\n"
"s_and_b32 s61, s61, 0xffff\n"
"s_mov_b32 s62, L_code_end-L_code_begin\n"
"s_mov_b32 s63, 0x20000\n"
"v_readlane_b32 s44, v255, 44\n"
"v_mbcnt_lo_u32_b32 v253, -1, 0\n"
"v_and_b32 v253, 3, v253\n"
"v_lshlrev_b32 v253, 7, v253\n"
"s_nop 1\n"
"buffer_load_dword v254, v253, s[60:63], s44 offen offset:0\n"
"v_readlane_b32 s74, v255, 25\n"
"s_mov_b32 s41, 0\n"
"s_mov_b32 s58, -1\n"
"s_nop 2\n"
"buffer_load_dword v246, v248, s[28:31], s74 offen offset:0 sc1\n"
"s_branch L_at_norm\n"
"L_at_mask0:\n"
"s_getpc_b64 s[60:61]\n"
"L_ct_4:\n"
"s_sub_u32 s60, s60, L_ct_4-L_code_begin\n"
"s_subb_u32 s61, s61, 0\n"
"s_and_b32 s61, s61, 0xffff\n"
"s_mov_b32 s62, L_code_end-L_code_begin\n"
"s_mov_b32 s63, 0x20000\n"
"v_readlane_b32 s44, v255, 44\n"
"v_mbcnt_lo_u32_b32 v253, -1, 0\n"
"v_and_b32 v253, 3, v253\n"
"v_lshlrev_b32 v253, 7, v253\n"
"s_nop 1\n"
"buffer_load_dword v254, v253, s[60:63], s44 offen offset:0\n"
"s_sub_u32 s41, s47, 1\n"
"s_cbranch_scc1 L_at_epi\n"
"v_cmp_le_i32_e64 s[60:61], 0, v221\n"
"v_cmp_le_i32_e64 s[62:63], 1, v221\n"
"v_cmp_le_i32_e64 s[64:65], 2, v221\n"
"v_cmp_le_i32_e64 s[66:67], 3, v221\n"
"v_cndmask_b32_e64 v128, v222, v176, s[60:61]\n"
"v_cndmask_b32_e64 v129, v222, v176, s[62:63]\n"
"v_cndmask_b32_e64 v130, v222, v176, s[64:65]\n"
"v_cndmask_b32_e64 v131, v222, v176, s[66:67]\n"
"v_cmp_le_i32_e64 s[60:61], 16, v221\n"
"v_cmp_le_i32_e64 s[62:63], 17, v221\n"
"v_cmp_le_i32_e64 s[64:65], 18, v221\n"
"v_cmp_le_i32_e64 s[66:67], 19, v221\n"
"v_cndmask_b32_e64 v136, v222, v176, s[60:61]\n"
"v_cndmask_b32_e64 v137, v222, v176, s[62:63]\n"
"v_cndmask_b32_e64 v138, v222, v176, s[64:65]\n"
"v_cndmask_b32_e64 v139, v222, v176, s[66:67]\n"
"v_cmp_le_i32_e64 s[60:61], 32, v221\n"
"v_cmp_le_i32_e64 s[62:63], 33, v221\n"
"v_cmp_le_i32_e64 s[64:65], 34, v221\n"
"v_cmp_le_i32_e64 s[66:67], 35, v221\n"
"v_cndmask_b32_e64 v144, v222, v176, s[60:61]\n"
"v_cndmask_b32_e64 v145, v222, v176, s[62:63]\n"
"v_cndmask_b32_e64 v146, v222, v176, s[64:65]\n"
"v_cndmask_b32_e64 v147, v222, v176, s[66:67]\n"
"v_cmp_le_i32_e64 s[60:61], 48, v221\n"
"v_cmp_le_i32_e64 s[62:63], 49, v221\n"
"v_cmp_le_i32_e64 s[64:65], 50, v221\n"
"v_cmp_le_i32_e64 s[66:67], 51, v221\n"
"v_cndmask_b32_e64 v152, v222, v176, s[60:61]\n"
"v_cndmask_b32_e64 v153, v222, v176, s[62:63]\n"
"v_cndmask_b32_e64 v154, v222, v176, s[64:65]\n"
"v_cndmask_b32_e64 v155, v222, v176, s[66:67]\n"
"s_nop 1\n"
"s_waitcnt lgkmcnt(0)\n"
"v_mfma_f32_16x16x32_bf16 v[128:131], a[64:67], a[0:3], v[128:131]\n"
"ds_read_b128 a[156:159], v217 offset:7168\n"
"s_add_u32 m0, m0, 4096\n"
"v_mfma_f32_16x16x32_bf16 v[128:131], a[68:71], a[4:7], v[128:131]\n"
"ds_read_b128 a[160:163], v217 offset:8192\n"
"buffer_load_dwordx4 v220, s[20:23], s38 offen offset:0 sc1 lds\n"
"v_mfma_f32_16x16x32_bf16 v[128:131], a[72:75], a[8:11], v[128:131]\n"
"ds_read_b128 a[164:167], v217 offset:9216\n"
"v_add_u32 v224, s36, v218\n"
"v_mfma_f32_16x16x32_bf16 v[128:131], a[76:79], a[12:15], v[128:131]\n"
"ds_read_b128 a[168:171], v217 offset:10240\n"
"s_add_u32 s68, s37, 32768\n"
"v_mfma_f32_16x16x32_bf16 v[128:131], a[80:83], a[16:19], v[128:131]\n"
"ds_read_b128 a[172:175], v217 offset:11264\n"
"s_cmp_ge_u32 s68, s39\n"
"v_mfma_f32_16x16x32_bf16 v[128:131], a[84:87], a[20:23], v[128:131]\n"
"ds_read_b128 a[176:179], v217 offset:12288\n"
"buffer_load_dwordx4 v220, s[20:23], s38 offen offset:1024 sc1 lds\n"
"v_mfma_f32_16x16x32_bf16 v[128:131], a[88:91], a[24:27], v[128:131]\n"
"ds_read_b128 a[180:183], v217 offset:13312\n"
"s_cselect_b32 s68, s40, s68\n"
"v_mfma_f32_16x16x32_bf16 v[128:131], a[92:95], a[28:31], v[128:131]\n"
"ds_read_b128 a[184:187], v217 offset:14336\n"
"v_add_u32 v216, s68, v218\n"
"v_mfma_f32_16x16x32_bf16 v[132:135], a[64:67], a[32:35], v[180:183]\n"
"ds_read_b128 a[188:191], v217 offset:15360\n"
"s_add_u32 s69, s38, 32768\n"
"v_mfma_f32_16x16x32_bf16 v[132:135], a[68:71], a[36:39], v[132:135]\n"
"ds_read_b128 a[192:195], v217 offset:16384\n"
"buffer_load_dwordx4 v220, s[20:23], s38 offen offset:2048 sc1 lds\n"
"v_mfma_f32_16x16x32_bf16 v[132:135], a[72:75], a[40:43], v[132:135]\n"
"ds_read_b128 a[196:199], v217 offset:17408\n"
"ds_read_b128 a[64:67], v224 offset:16384\n"
"v_mfma_f32_16x16x32_bf16 v[132:135], a[76:79], a[44:47], v[132:135]\n"
"v_exp_f32 v184, v128\n"
"v_mfma_f32_16x16x32_bf16 v[132:135], a[80:83], a[48:51], v[132:135]\n"
"ds_read_b128 a[68:71], v224 offset:17408\n"
"v_mfma_f32_16x16x32_bf16 v[132:135], a[84:87], a[52:55], v[132:135]\n"
"buffer_load_dwordx4 v220, s[20:23], s38 offen offset:3072 sc1 lds\n"
"v_mfma_f32_16x16x32_bf16 v[132:135], a[88:91], a[56:59], v[132:135]\n"
"v_exp_f32 v185, v129\n"
"v_mfma_f32_16x16x32_bf16 v[132:135], a[92:95], a[60:63], v[132:135]\n"
"ds_read_b128 a[72:75], v224 offset:18432\n"
"v_mfma_f32_16x16x32_bf16 v[136:139], a[96:99], a[0:3], v[136:139]\n"
"ds_read_b128 a[76:79], v224 offset:19456\n"
"v_add_f32 v202, v184, v185\n"
"v_mfma_f32_16x16x32_bf16 v[136:139], a[100:103], a[4:7], v[136:139]\n"
"v_exp_f32 v186, v130\n"
"v_mfma_f32_16x16x32_bf16 v[136:139], a[104:107], a[8:11], v[136:139]\n"
"ds_read_b128 a[80:83], v224 offset:20480\n"
"v_cvt_pk_bf16_f32 v160, v184, v185\n"
"v_mfma_f32_16x16x32_bf16 v[136:139], a[108:111], a[12:15], v[136:139]\n"
"s_add_u32 s38, s38, 65536\n"
"v_mfma_f32_16x16x32_bf16 v[136:139], a[112:115], a[16:19], v[136:139]\n"
"v_exp_f32 v187, v131\n"
"v_mfma_f32_16x16x32_bf16 v[136:139], a[116:119], a[20:23], v[136:139]\n"
"ds_read_b128 a[84:87], v224 offset:21504\n"
"v_add_f32 v202, v202, v186\n"
"v_mfma_f32_16x16x32_bf16 v[136:139], a[120:123], a[24:27], v[136:139]\n"
"v_exp_f32 v192, v132\n"
"v_mfma_f32_16x16x32_bf16 v[136:139], a[124:127], a[28:31], v[136:139]\n"
"ds_read_b128 a[88:91], v224 offset:22528\n"
"v_add_f32 v202, v202, v187\n"
"v_mfma_f32_16x16x32_bf16 v[140:143], a[96:99], a[32:35], v[180:183]\n"
"ds_read_b128 a[92:95], v224 offset:23552\n"
"v_cvt_pk_bf16_f32 v161, v186, v187\n"
"v_mfma_f32_16x16x32_bf16 v[140:143], a[100:103], a[36:39], v[140:143]\n"
"v_exp_f32 v193, v133\n"
"v_mfma_f32_16x16x32_bf16 v[140:143], a[104:107], a[40:43], v[140:143]\n"
"ds_read_b128 a[96:99], v224 offset:24576\n"
"v_add_f32 v203, v192, v193\n"
"v_mfma_f32_16x16x32_bf16 v[140:143], a[108:111], a[44:47], v[140:143]\n"
"v_cvt_pk_bf16_f32 v168, v192, v193\n"
"s_cmp_eq_u32 s38, s49\n"
"v_mfma_f32_16x16x32_bf16 v[140:143], a[112:115], a[48:51], v[140:143]\n"
"v_exp_f32 v194, v134\n"
"v_mfma_f32_16x16x32_bf16 v[140:143], a[116:119], a[52:55], v[140:143]\n"
"ds_read_b128 a[100:103], v224 offset:25600\n"
"s_cselect_b32 s38, 0, s38\n"
"v_mfma_f32_16x16x32_bf16 v[140:143], a[120:123], a[56:59], v[140:143]\n"
"ds_read_b128 a[104:107], v224 offset:26624\n"
"s_cmp_eq_u32 s38, s50\n"
"v_mfma_f32_16x16x32_bf16 v[140:143], a[124:127], a[60:63], v[140:143]\n"
"s_cselect_b32 s38, 2097152, s38\n"
"s_waitcnt lgkmcnt(3)\n"
"v_mfma_f32_16x16x32_bf16 v[144:147], a[64:67], a[0:3], v[144:147]\n"
"v_exp_f32 v195, v135\n"
"v_mfma_f32_16x16x32_bf16 v[144:147], a[68:71], a[4:7], v[144:147]\n"
"ds_read_b128 a[108:111], v224 offset:27648\n"
"v_add_f32 v203, v203, v194\n"
"v_mfma_f32_16x16x32_bf16 v[144:147], a[72:75], a[8:11], v[144:147]\n"
"v_exp_f32 v188, v136\n"
"v_mfma_f32_16x16x32_bf16 v[144:147], a[76:79], a[12:15], v[144:147]\n"
"ds_read_b128 a[112:115], v224 offset:28672\n"
"v_add_f32 v203, v203, v195\n"
"v_mfma_f32_16x16x32_bf16 v[144:147], a[80:83], a[16:19], v[144:147]\n"
"ds_read_b128 a[116:119], v224 offset:29696\n"
"v_cvt_pk_bf16_f32 v169, v194, v195\n"
"v_mfma_f32_16x16x32_bf16 v[144:147], a[84:87], a[20:23], v[144:147]\n"
"v_exp_f32 v189, v137\n"
"v_mfma_f32_16x16x32_bf16 v[144:147], a[88:91], a[24:27], v[144:147]\n"
"ds_read_b128 a[120:123], v224 offset:30720\n"
"v_add_f32 v202, v202, v188\n"
"v_mfma_f32_16x16x32_bf16 v[144:147], a[92:95], a[28:31], v[144:147]\n"
"v_add_f32 v202, v202, v189\n"
"v_cvt_pk_bf16_f32 v162, v188, v189\n"
"v_mfma_f32_16x16x32_bf16 v[148:151], a[64:67], a[32:35], v[180:183]\n"
"v_exp_f32 v190, v138\n"
"v_mfma_f32_16x16x32_bf16 v[148:151], a[68:71], a[36:39], v[148:151]\n"
"ds_read_b128 a[124:127], v224 offset:31744\n"
"ds_read_b128 a[200:203], v217 offset:18432\n"
"v_mfma_f32_16x16x32_bf16 v[148:151], a[72:75], a[40:43], v[148:151]\n"
"v_mfma_f32_16x16x32_bf16 v[148:151], a[76:79], a[44:47], v[148:151]\n"
"v_exp_f32 v191, v139\n"
"v_mfma_f32_16x16x32_bf16 v[148:151], a[80:83], a[48:51], v[148:151]\n"
"ds_read_b128 a[204:207], v217 offset:19456\n"
"v_add_f32 v202, v202, v190\n"
"v_mfma_f32_16x16x32_bf16 v[148:151], a[84:87], a[52:55], v[148:151]\n"
"v_exp_f32 v196, v140\n"
"v_mfma_f32_16x16x32_bf16 v[148:151], a[88:91], a[56:59], v[148:151]\n"
"v_add_f32 v202, v202, v191\n"
"v_cvt_pk_bf16_f32 v163, v190, v191\n"
"v_mfma_f32_16x16x32_bf16 v[148:151], a[92:95], a[60:63], v[148:151]\n"
"s_waitcnt lgkmcnt(3)\n"
"v_mfma_f32_16x16x32_bf16 v[152:155], a[96:99], a[0:3], v[152:155]\n"
"s_waitcnt vmcnt(16) lgkmcnt(2)\n"
"s_barrier\n"
"v_mfma_f32_16x16x32_bf16 v[152:155], a[100:103], a[4:7], v[152:155]\n"
"v_exp_f32 v197, v141\n"
"v_mfma_f32_16x16x32_bf16 v[152:155], a[104:107], a[8:11], v[152:155]\n"
"s_mov_b32 m0, s36\n"
"ds_read_b128 a[208:211], v217 offset:20480\n"
"v_mfma_f32_16x16x32_bf16 v[152:155], a[108:111], a[12:15], v[152:155]\n"
"buffer_load_dwordx4 v219, s[20:23], s69 offen offset:0 sc1 lds\n"
"v_cmp_lt_f32 vcc, s42, v202\n"
"v_mfma_f32_16x16x32_bf16 v[152:155], a[112:115], a[16:19], v[152:155]\n"
"v_exp_f32 v198, v142\n"
"v_mfma_f32_16x16x32_bf16 v[152:155], a[116:119], a[20:23], v[152:155]\n"
"ds_read_b128 a[212:215], v217 offset:21504\n"
"v_add_f32 v203, v203, v196\n"
"v_mfma_f32_16x16x32_bf16 v[152:155], a[120:123], a[24:27], v[152:155]\n"
"v_add_f32 v203, v203, v197\n"
"v_cvt_pk_bf16_f32 v170, v196, v197\n"
"v_mfma_f32_16x16x32_bf16 v[152:155], a[124:127], a[28:31], v[152:155]\n"
"v_exp_f32 v199, v143\n"
"v_mfma_f32_16x16x32_bf16 v[156:159], a[96:99], a[32:35], v[180:183]\n"
"buffer_load_dwordx4 v219, s[20:23], s69 offen offset:1024 sc1 lds\n"
"ds_read_b128 a[216:219], v217 offset:22528\n"
"v_mfma_f32_16x16x32_bf16 v[156:159], a[100:103], a[36:39], v[156:159]\n"
"v_exp_f32 v184, v144\n"
"v_mfma_f32_16x16x32_bf16 v[156:159], a[104:107], a[40:43], v[156:159]\n"
"v_add_f32 v203, v203, v198\n"
"v_add_f32 v203, v203, v199\n"
"v_mfma_f32_16x16x32_bf16 v[156:159], a[108:111], a[44:47], v[156:159]\n"
"v_cvt_pk_bf16_f32 v171, v198, v199\n"
"v_mfma_f32_16x16x32_bf16 v[156:159], a[112:115], a[48:51], v[156:159]\n"
"v_exp_f32 v185, v145\n"
"v_mfma_f32_16x16x32_bf16 v[156:159], a[116:119], a[52:55], v[156:159]\n"
"buffer_load_dwordx4 v219, s[20:23], s69 offen offset:2048 sc1 lds\n"
"ds_read_b128 a[220:223], v217 offset:23552\n"
"v_mfma_f32_16x16x32_bf16 v[156:159], a[120:123], a[56:59], v[156:159]\n"
"v_add_f32 v204, v184, v185\n"
"v_cvt_pk_bf16_f32 v164, v184, v185\n"
"v_mfma_f32_16x16x32_bf16 v[156:159], a[124:127], a[60:63], v[156:159]\n"
"s_cbranch_vccnz L_slow_m_00\n"
"L_ret_m_00:\n"
"v_mfma_f32_16x16x32_bf16 v[0:3], a[128:131], v[160:163], v[0:3]\n"
"v_exp_f32 v186, v146\n"
"v_mfma_f32_16x16x32_bf16 v[8:11], a[132:135], v[160:163], v[8:11]\n"
"ds_read_b128 a[224:227], v217 offset:24576\n"
"v_cmp_lt_f32 vcc, s43, v203\n"
"v_mfma_f32_16x16x32_bf16 v[16:19], a[136:139], v[160:163], v[16:19]\n"
"buffer_load_dwordx4 v219, s[20:23], s69 offen offset:3072 sc1 lds\n"
"s_add_u32 m0, m0, 4096\n"
"v_mfma_f32_16x16x32_bf16 v[24:27], a[140:143], v[160:163], v[24:27]\n"
"v_exp_f32 v187, v147\n"
"v_mfma_f32_16x16x32_bf16 v[32:35], a[144:147], v[160:163], v[32:35]\n"
"v_exp_f32 v188, v152\n"
"v_mfma_f32_16x16x32_bf16 v[40:43], a[148:151], v[160:163], v[40:43]\n"
"ds_read_b128 a[228:231], v217 offset:25600\n"
"v_add_f32 v204, v204, v186\n"
"v_mfma_f32_16x16x32_bf16 v[48:51], a[152:155], v[160:163], v[48:51]\n"
"v_add_f32 v204, v204, v187\n"
"v_cvt_pk_bf16_f32 v165, v186, v187\n"
"v_mfma_f32_16x16x32_bf16 v[56:59], a[156:159], v[160:163], v[56:59]\n"
"v_exp_f32 v189, v153\n"
"v_mfma_f32_16x16x32_bf16 v[64:67], a[160:163], v[160:163], v[64:67]\n"
"buffer_load_dwordx4 v220, s[20:23], s69 offen offset:0 sc1 lds\n"
"ds_read_b128 a[232:235], v217 offset:26624\n"
"v_mfma_f32_16x16x32_bf16 v[72:75], a[164:167], v[160:163], v[72:75]\n"
"v_add_f32 v204, v204, v188\n"
"v_add_f32 v204, v204, v189\n"
"v_mfma_f32_16x16x32_bf16 v[80:83], a[168:171], v[160:163], v[80:83]\n"
"v_exp_f32 v190, v154\n"
"v_mfma_f32_16x16x32_bf16 v[88:91], a[172:175], v[160:163], v[88:91]\n"
"v_cvt_pk_bf16_f32 v166, v188, v189\n"
"v_add_f32 v200, v200, v202\n"
"v_mfma_f32_16x16x32_bf16 v[96:99], a[176:179], v[160:163], v[96:99]\n"
"ds_read_b128 a[236:239], v217 offset:27648\n"
"ds_read_b128 a[64:67], v216 offset:0\n"
"v_mfma_f32_16x16x32_bf16 v[104:107], a[180:183], v[160:163], v[104:107]\n"
"v_exp_f32 v191, v155\n"
"v_mfma_f32_16x16x32_bf16 v[112:115], a[184:187], v[160:163], v[112:115]\n"
"buffer_load_dwordx4 v220, s[20:23], s69 offen offset:1024 sc1 lds\n"
"v_add_f32 v204, v204, v190\n"
"v_mfma_f32_16x16x32_bf16 v[120:123], a[188:191], v[160:163], v[120:123]\n"
"s_cbranch_vccnz L_slow_m_01\n"
"L_ret_m_01:\n"
"v_add_f32 v204, v204, v191\n"
"v_mfma_f32_16x16x32_bf16 v[4:7], a[128:131], v[168:171], v[4:7]\n"
"v_exp_f32 v192, v148\n"
"v_mfma_f32_16x16x32_bf16 v[12:15], a[132:135], v[168:171], v[12:15]\n"
"ds_read_b128 a[240:243], v217 offset:28672\n"
"v_cvt_pk_bf16_f32 v167, v190, v191\n"
"v_mfma_f32_16x16x32_bf16 v[20:23], a[136:139], v[168:171], v[20:23]\n"
"v_cmp_lt_f32 vcc, s42, v204\n"
"v_add_f32 v201, v201, v203\n"
"v_mfma_f32_16x16x32_bf16 v[28:31], a[140:143], v[168:171], v[28:31]\n"
"v_exp_f32 v193, v149\n"
"v_mfma_f32_16x16x32_bf16 v[36:39], a[144:147], v[168:171], v[36:39]\n"
"buffer_load_dwordx4 v220, s[20:23], s69 offen offset:2048 sc1 lds\n"
"ds_read_b128 a[244:247], v217 offset:29696\n"
"v_mfma_f32_16x16x32_bf16 v[44:47], a[148:151], v[168:171], v[44:47]\n"
"v_add_f32 v205, v192, v193\n"
"v_cvt_pk_bf16_f32 v172, v192, v193\n"
"v_mfma_f32_16x16x32_bf16 v[52:55], a[152:155], v[168:171], v[52:55]\n"
"v_exp_f32 v194, v150\n"
"v_mfma_f32_16x16x32_bf16 v[60:63], a[156:159], v[168:171], v[60:63]\n"
"ds_read_b128 a[68:71], v216 offset:1024\n"
"v_mfma_f32_16x16x32_bf16 v[68:71], a[160:163], v[168:171], v[68:71]\n"
"ds_read_b128 a[248:251], v217 offset:30720\n"
"ds_read_b128 a[72:75], v216 offset:2048\n"
"v_mfma_f32_16x16x32_bf16 v[76:79], a[164:167], v[168:171], v[76:79]\n"
"v_exp_f32 v195, v151\n"
"v_mfma_f32_16x16x32_bf16 v[84:87], a[168:171], v[168:171], v[84:87]\n"
"buffer_load_dwordx4 v220, s[20:23], s69 offen offset:3072 sc1 lds\n"
"v_add_f32 v205, v205, v194\n"
"v_mfma_f32_16x16x32_bf16 v[92:95], a[172:175], v[168:171], v[92:95]\n"
"v_exp_f32 v196, v156\n"
"v_mfma_f32_16x16x32_bf16 v[100:103], a[176:179], v[168:171], v[100:103]\n"
"ds_read_b128 a[252:255], v217 offset:31744\n"
"v_add_f32 v205, v205, v195\n"
"v_mfma_f32_16x16x32_bf16 v[108:111], a[180:183], v[168:171], v[108:111]\n"
"v_cvt_pk_bf16_f32 v173, v194, v195\n"
"ds_read_b128 a[76:79], v216 offset:3072\n"
"v_mfma_f32_16x16x32_bf16 v[116:119], a[184:187], v[168:171], v[116:119]\n"
"ds_read_b128 a[80:83], v216 offset:4096\n"
"v_mfma_f32_16x16x32_bf16 v[124:127], a[188:191], v[168:171], v[124:127]\n"
"s_cbranch_vccnz L_slow_m_10\n"
"L_ret_m_10:\n"
"ds_read_b128 a[84:87], v216 offset:5120\n"
"v_mfma_f32_16x16x32_bf16 v[0:3], a[192:195], v[164:167], v[0:3]\n"
"v_exp_f32 v197, v157\n"
"v_mfma_f32_16x16x32_bf16 v[8:11], a[196:199], v[164:167], v[8:11]\n"
"v_add_f32 v205, v205, v196\n"
"v_add_f32 v205, v205, v197\n"
"s_waitcnt lgkmcnt(4)\n"
"v_mfma_f32_16x16x32_bf16 v[16:19], a[200:203], v[164:167], v[16:19]\n"
"s_waitcnt vmcnt(16) lgkmcnt(3)\n"
"s_barrier\n"
"v_mfma_f32_16x16x32_bf16 v[24:27], a[204:207], v[164:167], v[24:27]\n"
"v_exp_f32 v198, v158\n"
"v_mfma_f32_16x16x32_bf16 v[32:35], a[208:211], v[164:167], v[32:35]\n"
"v_cvt_pk_bf16_f32 v174, v196, v197\n"
"s_mov_b32 m0, s37\n"
"v_mfma_f32_16x16x32_bf16 v[40:43], a[212:215], v[164:167], v[40:43]\n"
"buffer_load_dwordx4 v219, s[20:23], s38 offen offset:0 sc1 lds\n"
"s_add_u32 s37, s68, 32768\n"
"v_mfma_f32_16x16x32_bf16 v[48:51], a[216:219], v[164:167], v[48:51]\n"
"v_exp_f32 v199, v159\n"
"v_mfma_f32_16x16x32_bf16 v[56:59], a[220:223], v[164:167], v[56:59]\n"
"v_add_f32 v205, v205, v198\n"
"v_add_f32 v205, v205, v199\n"
"v_mfma_f32_16x16x32_bf16 v[64:67], a[224:227], v[164:167], v[64:67]\n"
"v_cvt_pk_bf16_f32 v175, v198, v199\n"
"v_add_f32 v200, v200, v204\n"
"v_mfma_f32_16x16x32_bf16 v[72:75], a[228:231], v[164:167], v[72:75]\n"
"v_cmp_lt_f32 vcc, s43, v205\n"
"ds_read_b128 a[88:91], v216 offset:6144\n"
"v_mfma_f32_16x16x32_bf16 v[80:83], a[232:235], v[164:167], v[80:83]\n"
"s_cmp_ge_u32 s37, s39\n"
"s_cselect_b32 s37, s40, s37\n"
"v_mfma_f32_16x16x32_bf16 v[88:91], a[236:239], v[164:167], v[88:91]\n"
"buffer_load_dwordx4 v219, s[20:23], s38 offen offset:1024 sc1 lds\n"
"v_add_u32 v217, s37, v218\n"
"v_mfma_f32_16x16x32_bf16 v[96:99], a[240:243], v[164:167], v[96:99]\n"
"s_mov_b32 s36, s68\n"
"ds_read_b128 a[92:95], v216 offset:7168\n"
"v_mfma_f32_16x16x32_bf16 v[104:107], a[244:247], v[164:167], v[104:107]\n"
"ds_read_b128 a[96:99], v216 offset:8192\n"
"v_mfma_f32_16x16x32_bf16 v[112:115], a[248:251], v[164:167], v[112:115]\n"
"ds_read_b128 a[100:103], v216 offset:9216\n"
"v_mfma_f32_16x16x32_bf16 v[120:123], a[252:255], v[164:167], v[120:123]\n"
"s_cbranch_vccnz L_slow_m_11\n"
"L_ret_m_11:\n"
"ds_read_b128 a[104:107], v216 offset:10240\n"
"v_mfma_f32_16x16x32_bf16 v[4:7], a[192:195], v[172:175], v[4:7]\n"
"v_add_f32 v201, v201, v205\n"
"ds_read_b128 a[108:111], v216 offset:11264\n"
"v_mfma_f32_16x16x32_bf16 v[12:15], a[196:199], v[172:175], v[12:15]\n"
"buffer_load_dwordx4 v219, s[20:23], s38 offen offset:2048 sc1 lds\n"
"ds_read_b128 a[112:115], v216 offset:12288\n"
"v_mfma_f32_16x16x32_bf16 v[20:23], a[200:203], v[172:175], v[20:23]\n"
"ds_read_b128 a[116:119], v216 offset:13312\n"
"v_mfma_f32_16x16x32_bf16 v[28:31], a[204:207], v[172:175], v[28:31]\n"
"ds_read_b128 a[120:123], v216 offset:14336\n"
"v_mfma_f32_16x16x32_bf16 v[36:39], a[208:211], v[172:175], v[36:39]\n"
"ds_read_b128 a[124:127], v216 offset:15360\n"
"v_mfma_f32_16x16x32_bf16 v[44:47], a[212:215], v[172:175], v[44:47]\n"
"ds_read_b128 a[128:131], v217 offset:0\n"
"v_mfma_f32_16x16x32_bf16 v[52:55], a[216:219], v[172:175], v[52:55]\n"
"ds_read_b128 a[132:135], v217 offset:1024\n"
"v_mfma_f32_16x16x32_bf16 v[60:63], a[220:223], v[172:175], v[60:63]\n"
"buffer_load_dwordx4 v219, s[20:23], s38 offen offset:3072 sc1 lds\n"
"ds_read_b128 a[136:139], v217 offset:2048\n"
"v_mfma_f32_16x16x32_bf16 v[68:71], a[224:227], v[172:175], v[68:71]\n"
"ds_read_b128 a[140:143], v217 offset:3072\n"
"v_mfma_f32_16x16x32_bf16 v[76:79], a[228:231], v[172:175], v[76:79]\n"
"ds_read_b128 a[144:147], v217 offset:4096\n"
"v_mfma_f32_16x16x32_bf16 v[84:87], a[232:235], v[172:175], v[84:87]\n"
"ds_read_b128 a[148:151], v217 offset:5120\n"
"v_mfma_f32_16x16x32_bf16 v[92:95], a[236:239], v[172:175], v[92:95]\n"
"ds_read_b128 a[152:155], v217 offset:6144\n"
"v_mfma_f32_16x16x32_bf16 v[100:103], a[240:243], v[172:175], v[100:103]\n"
"v_mfma_f32_16x16x32_bf16 v[108:111], a[244:247], v[172:175], v[108:111]\n"
"v_mfma_f32_16x16x32_bf16 v[116:119], a[248:251], v[172:175], v[116:119]\n"
"v_mfma_f32_16x16x32_bf16 v[124:127], a[252:255], v[172:175], v[124:127]\n"
"s_sub_u32 s41, s41, 1\n"
"v_mov_b32 v246, 0x7ffffff0\n"
"v_writelane_b32 v246, 0, 0\n"
"v_mov_b32 v247, s57\n"
"s_waitcnt vmcnt(16)\n"
"buffer_store_dword v247, v246, s[28:31], s59 offen offset:0 sc1\n"
"s_mov_b32 s59, 0x7fffff00\n"
"v_cmp_le_i32_e64 s[60:61], 0, v223\n"
"v_cmp_le_i32_e64 s[62:63], 1, v223\n"
"v_cmp_le_i32_e64 s[64:65], 2, v223\n"
"v_cmp_le_i32_e64 s[66:67], 3, v223\n"
"v_cndmask_b32_e64 v132, v222, v180, s[60:61]\n"
"v_cndmask_b32_e64 v133, v222, v180, s[62:63]\n"
"v_cndmask_b32_e64 v134, v222, v180, s[64:65]\n"
"v_cndmask_b32_e64 v135, v222, v180, s[66:67]\n"
"v_cmp_le_i32_e64 s[60:61], 16, v223\n"
"v_cmp_le_i32_e64 s[62:63], 17, v223\n"
"v_cmp_le_i32_e64 s[64:65], 18, v223\n"
"v_cmp_le_i32_e64 s[66:67], 19, v223\n"
"v_cndmask_b32_e64 v140, v222, v180, s[60:61]\n"
"v_cndmask_b32_e64 v141, v222, v180, s[62:63]\n"
"v_cndmask_b32_e64 v142, v222, v180, s[64:65]\n"
"v_cndmask_b32_e64 v143, v222, v180, s[66:67]\n"
"v_cmp_le_i32_e64 s[60:61], 32, v223\n"
"v_cmp_le_i32_e64 s[62:63], 33, v223\n"
"v_cmp_le_i32_e64 s[64:65], 34, v223\n"
"v_cmp_le_i32_e64 s[66:67], 35, v223\n"
"v_cndmask_b32_e64 v148, v222, v180, s[60:61]\n"
"v_cndmask_b32_e64 v149, v222, v180, s[62:63]\n"
"v_cndmask_b32_e64 v150, v222, v180, s[64:65]\n"
"v_cndmask_b32_e64 v151, v222, v180, s[66:67]\n"
"v_cmp_le_i32_e64 s[60:61], 48, v223\n"
"v_cmp_le_i32_e64 s[62:63], 49, v223\n"
"v_cmp_le_i32_e64 s[64:65], 50, v223\n"
"v_cmp_le_i32_e64 s[66:67], 51, v223\n"
"v_cndmask_b32_e64 v156, v222, v180, s[60:61]\n"
"v_cndmask_b32_e64 v157, v222, v180, s[62:63]\n"
"v_cndmask_b32_e64 v158, v222, v180, s[64:65]\n"
"v_cndmask_b32_e64 v159, v222, v180, s[66:67]\n"
"s_nop 1\n"
"ds_read_b128 a[156:159], v217 offset:7168\n"
"s_add_u32 m0, m0, 4096\n"
"ds_read_b128 a[160:163], v217 offset:8192\n"
"buffer_load_dwordx4 v220, s[20:23], s38 offen offset:0 sc1 lds\n"
"ds_read_b128 a[164:167], v217 offset:9216\n"
"v_add_u32 v224, s36, v218\n"
"ds_read_b128 a[168:171], v217 offset:10240\n"
"s_add_u32 s68, s37, 32768\n"
"ds_read_b128 a[172:175], v217 offset:11264\n"
"s_cmp_ge_u32 s68, s39\n"
"ds_read_b128 a[176:179], v217 offset:12288\n"
"buffer_load_dwordx4 v220, s[20:23], s38 offen offset:1024 sc1 lds\n"
"ds_read_b128 a[180:183], v217 offset:13312\n"
"s_cselect_b32 s68, s40, s68\n"
"ds_read_b128 a[184:187], v217 offset:14336\n"
"v_add_u32 v216, s68, v218\n"
"s_waitcnt lgkmcnt(8)\n"
"v_mfma_f32_16x16x32_bf16 v[132:135], a[64:67], a[32:35], v[132:135]\n"
"ds_read_b128 a[188:191], v217 offset:15360\n"
"s_add_u32 s69, s38, 32768\n"
"v_mfma_f32_16x16x32_bf16 v[132:135], a[68:71], a[36:39], v[132:135]\n"
"ds_read_b128 a[192:195], v217 offset:16384\n"
"buffer_load_dwordx4 v220, s[20:23], s38 offen offset:2048 sc1 lds\n"
"v_mfma_f32_16x16x32_bf16 v[132:135], a[72:75], a[40:43], v[132:135]\n"
"ds_read_b128 a[196:199], v217 offset:17408\n"
"ds_read_b128 a[64:67], v224 offset:16384\n"
"v_mfma_f32_16x16x32_bf16 v[132:135], a[76:79], a[44:47], v[132:135]\n"
"v_mfma_f32_16x16x32_bf16 v[132:135], a[80:83], a[48:51], v[132:135]\n"
"ds_read_b128 a[68:71], v224 offset:17408\n"
"v_mfma_f32_16x16x32_bf16 v[132:135], a[84:87], a[52:55], v[132:135]\n"
"buffer_load_dwordx4 v220, s[20:23], s38 offen offset:3072 sc1 lds\n"
"v_mfma_f32_16x16x32_bf16 v[132:135], a[88:91], a[56:59], v[132:135]\n"
"v_mfma_f32_16x16x32_bf16 v[132:135], a[92:95], a[60:63], v[132:135]\n"
"ds_read_b128 a[72:75], v224 offset:18432\n"
"ds_read_b128 a[76:79], v224 offset:19456\n"
"ds_read_b128 a[80:83], v224 offset:20480\n"
"s_add_u32 s38, s38, 65536\n"
"ds_read_b128 a[84:87], v224 offset:21504\n"
"s_nop 2\n"
"v_exp_f32 v192, v132\n"
"ds_read_b128 a[88:91], v224 offset:22528\n"
"v_mfma_f32_16x16x32_bf16 v[140:143], a[96:99], a[32:35], v[140:143]\n"
"ds_read_b128 a[92:95], v224 offset:23552\n"
"v_mfma_f32_16x16x32_bf16 v[140:143], a[100:103], a[36:39], v[140:143]\n"
"v_exp_f32 v193, v133\n"
"v_mfma_f32_16x16x32_bf16 v[140:143], a[104:107], a[40:43], v[140:143]\n"
"ds_read_b128 a[96:99], v224 offset:24576\n"
"v_add_f32 v203, v192, v193\n"
"v_mfma_f32_16x16x32_bf16 v[140:143], a[108:111], a[44:47], v[140:143]\n"
"v_cvt_pk_bf16_f32 v168, v192, v193\n"
"s_cmp_eq_u32 s38, s49\n"
"v_mfma_f32_16x16x32_bf16 v[140:143], a[112:115], a[48:51], v[140:143]\n"
"v_exp_f32 v194, v134\n"
"v_mfma_f32_16x16x32_bf16 v[140:143], a[116:119], a[52:55], v[140:143]\n"
"ds_read_b128 a[100:103], v224 offset:25600\n"
"s_cselect_b32 s38, 0, s38\n"
"v_mfma_f32_16x16x32_bf16 v[140:143], a[120:123], a[56:59], v[140:143]\n"
"ds_read_b128 a[104:107], v224 offset:26624\n"
"s_cmp_eq_u32 s38, s50\n"
"v_mfma_f32_16x16x32_bf16 v[140:143], a[124:127], a[60:63], v[140:143]\n"
"s_cselect_b32 s38, 2097152, s38\n"
"v_exp_f32 v195, v135\n"
"ds_read_b128 a[108:111], v224 offset:27648\n"
"v_add_f32 v203, v203, v194\n"
"ds_read_b128 a[112:115], v224 offset:28672\n"
"v_add_f32 v203, v203, v195\n"
"ds_read_b128 a[116:119], v224 offset:29696\n"
"v_cvt_pk_bf16_f32 v169, v194, v195\n"
"ds_read_b128 a[120:123], v224 offset:30720\n"
"s_waitcnt lgkmcnt(7)\n"
"v_mfma_f32_16x16x32_bf16 v[148:151], a[64:67], a[32:35], v[148:151]\n"
"v_mfma_f32_16x16x32_bf16 v[148:151], a[68:71], a[36:39], v[148:151]\n"
"ds_read_b128 a[124:127], v224 offset:31744\n"
"ds_read_b128 a[200:203], v217 offset:18432\n"
"v_mfma_f32_16x16x32_bf16 v[148:151], a[72:75], a[40:43], v[148:151]\n"
"v_mfma_f32_16x16x32_bf16 v[148:151], a[76:79], a[44:47], v[148:151]\n"
"v_mfma_f32_16x16x32_bf16 v[148:151], a[80:83], a[48:51], v[148:151]\n"
"ds_read_b128 a[204:207], v217 offset:19456\n"
"v_mfma_f32_16x16x32_bf16 v[148:151], a[84:87], a[52:55], v[148:151]\n"
"v_exp_f32 v196, v140\n"
"v_mfma_f32_16x16x32_bf16 v[148:151], a[88:91], a[56:59], v[148:151]\n"
"v_mfma_f32_16x16x32_bf16 v[148:151], a[92:95], a[60:63], v[148:151]\n"
"s_waitcnt vmcnt(16) lgkmcnt(2)\n"
"s_barrier\n"
"v_exp_f32 v197, v141\n"
"s_mov_b32 m0, s36\n"
"ds_read_b128 a[208:211], v217 offset:20480\n"
"buffer_load_dwordx4 v219, s[20:23], s69 offen offset:0 sc1 lds\n"
"v_exp_f32 v198, v142\n"
"ds_read_b128 a[212:215], v217 offset:21504\n"
"v_add_f32 v203, v203, v196\n"
"v_add_f32 v203, v203, v197\n"
"v_cvt_pk_bf16_f32 v170, v196, v197\n"
"v_exp_f32 v199, v143\n"
"v_mfma_f32_16x16x32_bf16 v[156:159], a[96:99], a[32:35], v[156:159]\n"
"buffer_load_dwordx4 v219, s[20:23], s69 offen offset:1024 sc1 lds\n"
"ds_read_b128 a[216:219], v217 offset:22528\n"
"v_mfma_f32_16x16x32_bf16 v[156:159], a[100:103], a[36:39], v[156:159]\n"
"v_mfma_f32_16x16x32_bf16 v[156:159], a[104:107], a[40:43], v[156:159]\n"
"v_add_f32 v203, v203, v198\n"
"v_add_f32 v203, v203, v199\n"
"v_mfma_f32_16x16x32_bf16 v[156:159], a[108:111], a[44:47], v[156:159]\n"
"v_cvt_pk_bf16_f32 v171, v198, v199\n"
"v_mfma_f32_16x16x32_bf16 v[156:159], a[112:115], a[48:51], v[156:159]\n"
"v_mfma_f32_16x16x32_bf16 v[156:159], a[116:119], a[52:55], v[156:159]\n"
"buffer_load_dwordx4 v219, s[20:23], s69 offen offset:2048 sc1 lds\n"
"ds_read_b128 a[220:223], v217 offset:23552\n"
"v_mfma_f32_16x16x32_bf16 v[156:159], a[120:123], a[56:59], v[156:159]\n"
"v_mfma_f32_16x16x32_bf16 v[156:159], a[124:127], a[60:63], v[156:159]\n"
"ds_read_b128 a[224:227], v217 offset:24576\n"
"v_cmp_lt_f32 vcc, s43, v203\n"
"buffer_load_dwordx4 v219, s[20:23], s69 offen offset:3072 sc1 lds\n"
"s_add_u32 m0, m0, 4096\n"
"ds_read_b128 a[228:231], v217 offset:25600\n"
"buffer_load_dwordx4 v220, s[20:23], s69 offen offset:0 sc1 lds\n"
"ds_read_b128 a[232:235], v217 offset:26624\n"
"ds_read_b128 a[236:239], v217 offset:27648\n"
"ds_read_b128 a[64:67], v216 offset:0\n"
"buffer_load_dwordx4 v220, s[20:23], s69 offen offset:1024 sc1 lds\n"
"s_cbranch_vccnz L_slow_h_01\n"
"L_ret_h_01:\n"
"v_mfma_f32_16x16x32_bf16 v[4:7], a[128:131], v[168:171], v[4:7]\n"
"v_exp_f32 v192, v148\n"
"v_mfma_f32_16x16x32_bf16 v[12:15], a[132:135], v[168:171], v[12:15]\n"
"ds_read_b128 a[240:243], v217 offset:28672\n"
"v_mfma_f32_16x16x32_bf16 v[20:23], a[136:139], v[168:171], v[20:23]\n"
"v_add_f32 v201, v201, v203\n"
"v_mfma_f32_16x16x32_bf16 v[28:31], a[140:143], v[168:171], v[28:31]\n"
"v_exp_f32 v193, v149\n"
"v_mfma_f32_16x16x32_bf16 v[36:39], a[144:147], v[168:171], v[36:39]\n"
"buffer_load_dwordx4 v220, s[20:23], s69 offen offset:2048 sc1 lds\n"
"ds_read_b128 a[244:247], v217 offset:29696\n"
"v_mfma_f32_16x16x32_bf16 v[44:47], a[148:151], v[168:171], v[44:47]\n"
"v_add_f32 v205, v192, v193\n"
"v_cvt_pk_bf16_f32 v172, v192, v193\n"
"v_mfma_f32_16x16x32_bf16 v[52:55], a[152:155], v[168:171], v[52:55]\n"
"v_exp_f32 v194, v150\n"
"v_mfma_f32_16x16x32_bf16 v[60:63], a[156:159], v[168:171], v[60:63]\n"
"ds_read_b128 a[68:71], v216 offset:1024\n"
"v_mfma_f32_16x16x32_bf16 v[68:71], a[160:163], v[168:171], v[68:71]\n"
"ds_read_b128 a[248:251], v217 offset:30720\n"
"ds_read_b128 a[72:75], v216 offset:2048\n"
"v_mfma_f32_16x16x32_bf16 v[76:79], a[164:167], v[168:171], v[76:79]\n"
"v_exp_f32 v195, v151\n"
"v_mfma_f32_16x16x32_bf16 v[84:87], a[168:171], v[168:171], v[84:87]\n"
"buffer_load_dwordx4 v220, s[20:23], s69 offen offset:3072 sc1 lds\n"
"v_add_f32 v205, v205, v194\n"
"v_mfma_f32_16x16x32_bf16 v[92:95], a[172:175], v[168:171], v[92:95]\n"
"v_exp_f32 v196, v156\n"
"v_mfma_f32_16x16x32_bf16 v[100:103], a[176:179], v[168:171], v[100:103]\n"
"ds_read_b128 a[252:255], v217 offset:31744\n"
"v_add_f32 v205, v205, v195\n"
"v_mfma_f32_16x16x32_bf16 v[108:111], a[180:183], v[168:171], v[108:111]\n"
"v_cvt_pk_bf16_f32 v173, v194, v195\n"
"ds_read_b128 a[76:79], v216 offset:3072\n"
"v_mfma_f32_16x16x32_bf16 v[116:119], a[184:187], v[168:171], v[116:119]\n"
"ds_read_b128 a[80:83], v216 offset:4096\n"
"v_mfma_f32_16x16x32_bf16 v[124:127], a[188:191], v[168:171], v[124:127]\n"
"ds_read_b128 a[84:87], v216 offset:5120\n"
"v_exp_f32 v197, v157\n"
"v_add_f32 v205, v205, v196\n"
"v_add_f32 v205, v205, v197\n"
"s_waitcnt vmcnt(16) lgkmcnt(3)\n"
"s_barrier\n"
"v_exp_f32 v198, v158\n"
"v_cvt_pk_bf16_f32 v174, v196, v197\n"
"s_mov_b32 m0, s37\n"
"s_nop 0\n"
"buffer_load_dwordx4 v219, s[20:23], s38 offen offset:0 sc1 lds\n"
"s_add_u32 s37, s68, 32768\n"
"v_exp_f32 v199, v159\n"
"v_add_f32 v205, v205, v198\n"
"v_add_f32 v205, v205, v199\n"
"v_cvt_pk_bf16_f32 v175, v198, v199\n"
"v_cmp_lt_f32 vcc, s43, v205\n"
"ds_read_b128 a[88:91], v216 offset:6144\n"
"s_cmp_ge_u32 s37, s39\n"
"s_cselect_b32 s37, s40, s37\n"
"buffer_load_dwordx4 v219, s[20:23], s38 offen offset:1024 sc1 lds\n"
"v_add_u32 v217, s37, v218\n"
"s_mov_b32 s36, s68\n"
"ds_read_b128 a[92:95], v216 offset:7168\n"
"ds_read_b128 a[96:99], v216 offset:8192\n"
"ds_read_b128 a[100:103], v216 offset:9216\n"
"s_cbranch_vccnz L_slow_h_11\n"
"L_ret_h_11:\n"
"ds_read_b128 a[104:107], v216 offset:10240\n"
"v_mfma_f32_16x16x32_bf16 v[4:7], a[192:195], v[172:175], v[4:7]\n"
"v_add_f32 v201, v201, v205\n"
"ds_read_b128 a[108:111], v216 offset:11264\n"
"v_mfma_f32_16x16x32_bf16 v[12:15], a[196:199], v[172:175], v[12:15]\n"
"buffer_load_dwordx4 v219, s[20:23], s38 offen offset:2048 sc1 lds\n"
"ds_read_b128 a[112:115], v216 offset:12288\n"
"v_mfma_f32_16x16x32_bf16 v[20:23], a[200:203], v[172:175], v[20:23]\n"
"ds_read_b128 a[116:119], v216 offset:13312\n"
"v_mfma_f32_16x16x32_bf16 v[28:31], a[204:207], v[172:175], v[28:31]\n"
"ds_read_b128 a[120:123], v216 offset:14336\n"
"v_mfma_f32_16x16x32_bf16 v[36:39], a[208:211], v[172:175], v[36:39]\n"
"ds_read_b128 a[124:127], v216 offset:15360\n"
"v_mfma_f32_16x16x32_bf16 v[44:47], a[212:215], v[172:175], v[44:47]\n"
"ds_read_b128 a[128:131], v217 offset:0\n"
"v_mfma_f32_16x16x32_bf16 v[52:55], a[216:219], v[172:175], v[52:55]\n"
"ds_read_b128 a[132:135], v217 offset:1024\n"
"v_mfma_f32_16x16x32_bf16 v[60:63], a[220:223], v[172:175], v[60:63]\n"
"buffer_load_dwordx4 v219, s[20:23], s38 offen offset:3072 sc1 lds\n"
"ds_read_b128 a[136:139], v217 offset:2048\n"
"v_mfma_f32_16x16x32_bf16 v[68:71], a[224:227], v[172:175], v[68:71]\n"
"ds_read_b128 a[140:143], v217 offset:3072\n"
"v_mfma_f32_16x16x32_bf16 v[76:79], a[228:231], v[172:175], v[76:79]\n"
"ds_read_b128 a[144:147], v217 offset:4096\n"
"v_mfma_f32_16x16x32_bf16 v[84:87], a[232:235], v[172:175], v[84:87]\n"
"ds_read_b128 a[148:151], v217 offset:5120\n"
"v_mfma_f32_16x16x32_bf16 v[92:95], a[236:239], v[172:175], v[92:95]\n"
"ds_read_b128 a[152:155], v217 offset:6144\n"
"v_mfma_f32_16x16x32_bf16 v[100:103], a[240:243], v[172:175], v[100:103]\n"
"v_mfma_f32_16x16x32_bf16 v[108:111], a[244:247], v[172:175], v[108:111]\n"
"v_mfma_f32_16x16x32_bf16 v[116:119], a[248:251], v[172:175], v[116:119]\n"
"v_mfma_f32_16x16x32_bf16 v[124:127], a[252:255], v[172:175], v[124:127]\n"
"s_sub_u32 s41, s41, 1\n"
"s_nop 7\n"
"L_at_epi:\n"
"v_mov_b32 v207, v200\n"
"s_nop 1\n"
"v_permlane32_swap_b32 v207, v200\n"
"v_add_f32 v200, v200, v207\n"
"v_mov_b32 v207, v200\n"
"s_nop 1\n"
"v_permlane16_swap_b32 v207, v200\n"
"v_add_f32 v200, v200, v207\n"
"v_mov_b32 v207, v201\n"
"s_nop 1\n"
"v_permlane32_swap_b32 v207, v201\n"
"v_add_f32 v201, v201, v207\n"
"v_mov_b32 v207, v201\n"
"s_nop 1\n"
"v_permlane16_swap_b32 v207, v201\n"
"v_add_f32 v201, v201, v207\n"
"s_cmp_eq_u32 s70, 1\n"
"s_cbranch_scc1 L_at_part\n"
"s_cmp_eq_u32 s70, 2\n"
"s_cbranch_scc1 L_at_comb\n"
"s_branch L_at_ef\n"
"L_slow_n_00:\n"
"s_nop 7\n"
"v_max3_f32 v206, v128, v129, v130\n"
"v_max3_f32 v206, v206, v131, v136\n"
"v_max3_f32 v206, v206, v137, v138\n"
"v_max3_f32 v206, v206, v139, v144\n"
"v_max3_f32 v206, v206, v145, v146\n"
"v_max3_f32 v206, v206, v147, v152\n"
"v_max3_f32 v206, v206, v153, v154\n"
"v_max_f32 v206, v206, v155\n"
"v_mov_b32 v207, v206\n"
"s_nop 1\n"
"v_permlane32_swap_b32 v207, v206\n"
"v_max_f32 v206, v206, v207\n"
"v_mov_b32 v207, v206\n"
"s_nop 1\n"
"v_permlane16_swap_b32 v207, v206\n"
"v_max_f32 v206, v206, v207\n"
"s_cmp_lt_i32 s42, 0\n"
"s_cbranch_scc1 L_first_n_0\n"
"v_max_f32 v206, 0, v206\n"
"v_exp_f32_e64 v208, -v206\n"
"s_nop 0\n"
"v_mul_f32 v200, v200, v208\n"
"v_mul_f32 v0, v0, v208\n"
"v_mul_f32 v1, v1, v208\n"
"v_mul_f32 v2, v2, v208\n"
"v_mul_f32 v3, v3, v208\n"
"v_mul_f32 v8, v8, v208\n"
"v_mul_f32 v9, v9, v208\n"
"v_mul_f32 v10, v10, v208\n"
"v_mul_f32 v11, v11, v208\n"
"v_mul_f32 v16, v16, v208\n"
"v_mul_f32 v17, v17, v208\n"
"v_mul_f32 v18, v18, v208\n"
"v_mul_f32 v19, v19, v208\n"
"v_mul_f32 v24, v24, v208\n"
"v_mul_f32 v25, v25, v208\n"
"v_mul_f32 v26, v26, v208\n"
"v_mul_f32 v27, v27, v208\n"
"v_mul_f32 v32, v32, v208\n"
"v_mul_f32 v33, v33, v208\n"
"v_mul_f32 v34, v34, v208\n"
"v_mul_f32 v35, v35, v208\n"
"v_mul_f32 v40, v40, v208\n"
"v_mul_f32 v41, v41, v208\n"
"v_mul_f32 v42, v42, v208\n"
"v_mul_f32 v43, v43, v208\n"
"v_mul_f32 v48, v48, v208\n"
"v_mul_f32 v49, v49, v208\n"
"v_mul_f32 v50, v50, v208\n"
"v_mul_f32 v51, v51, v208\n"
"v_mul_f32 v56, v56, v208\n"
"v_mul_f32 v57, v57, v208\n"
"v_mul_f32 v58, v58, v208\n"
"v_mul_f32 v59, v59, v208\n"
"v_mul_f32 v64, v64, v208\n"
"v_mul_f32 v65, v65, v208\n"
"v_mul_f32 v66, v66, v208\n"
"v_mul_f32 v67, v67, v208\n"
"v_mul_f32 v72, v72, v208\n"
"v_mul_f32 v73, v73, v208\n"
"v_mul_f32 v74, v74, v208\n"
"v_mul_f32 v75, v75, v208\n"
"v_mul_f32 v80, v80, v208\n"
"v_mul_f32 v81, v81, v208\n"
"v_mul_f32 v82, v82, v208\n"
"v_mul_f32 v83, v83, v208\n"
"v_mul_f32 v88, v88, v208\n"
"v_mul_f32 v89, v89, v208\n"
"v_mul_f32 v90, v90, v208\n"
"v_mul_f32 v91, v91, v208\n"
"v_mul_f32 v96, v96, v208\n"
"v_mul_f32 v97, v97, v208\n"
"v_mul_f32 v98, v98, v208\n"
"v_mul_f32 v99, v99, v208\n"
"v_mul_f32 v104, v104, v208\n"
"v_mul_f32 v105, v105, v208\n"
"v_mul_f32 v106, v106, v208\n"
"v_mul_f32 v107, v107, v208\n"
"v_mul_f32 v112, v112, v208\n"
"v_mul_f32 v113, v113, v208\n"
"v_mul_f32 v114, v114, v208\n"
"v_mul_f32 v115, v115, v208\n"
"v_mul_f32 v120, v120, v208\n"
"v_mul_f32 v121, v121, v208\n"
"v_mul_f32 v122, v122, v208\n"
"v_mul_f32 v123, v123, v208\n"
"L_first_n_0:\n"
"s_mov_b32 s42, 0x44800000\n"
"v_sub_f32 v176, v176, v206\n"
"v_sub_f32 v177, v177, v206\n"
"v_sub_f32 v178, v178, v206\n"
"v_sub_f32 v179, v179, v206\n"
"v_sub_f32 v128, v128, v206\n"
"v_sub_f32 v129, v129, v206\n"
"v_sub_f32 v130, v130, v206\n"
"v_sub_f32 v131, v131, v206\n"
"v_sub_f32 v136, v136, v206\n"
"v_sub_f32 v137, v137, v206\n"
"v_sub_f32 v138, v138, v206\n"
"v_sub_f32 v139, v139, v206\n"
"v_sub_f32 v144, v144, v206\n"
"v_sub_f32 v145, v145, v206\n"
"v_sub_f32 v146, v146, v206\n"
"v_sub_f32 v147, v147, v206\n"
"v_sub_f32 v152, v152, v206\n"
"v_sub_f32 v153, v153, v206\n"
"v_sub_f32 v154, v154, v206\n"
"v_sub_f32 v155, v155, v206\n"
"v_exp_f32 v184, v128\n"
"v_exp_f32 v185, v129\n"
"s_nop 0\n"
"v_add_f32 v202, v184, v185\n"
"v_exp_f32 v186, v130\n"
"v_cvt_pk_bf16_f32 v160, v184, v185\n"
"v_exp_f32 v187, v131\n"
"v_add_f32 v202, v202, v186\n"
"v_add_f32 v202, v202, v187\n"
"v_cvt_pk_bf16_f32 v161, v186, v187\n"
"v_exp_f32 v188, v136\n"
"v_exp_f32 v189, v137\n"
"v_add_f32 v202, v202, v188\n"
"v_add_f32 v202, v202, v189\n"
"v_cvt_pk_bf16_f32 v162, v188, v189\n"
"v_exp_f32 v190, v138\n"
"v_exp_f32 v191, v139\n"
"v_add_f32 v202, v202, v190\n"
"v_add_f32 v202, v202, v191\n"
"v_cvt_pk_bf16_f32 v163, v190, v191\n"
"v_exp_f32 v184, v144\n"
"v_exp_f32 v185, v145\n"
"s_nop 0\n"
"v_add_f32 v204, v184, v185\n"
"v_cvt_pk_bf16_f32 v164, v184, v185\n"
"s_nop 1\n"
"s_branch L_ret_n_00\n"
"L_slow_n_01:\n"
"s_nop 7\n"
"v_max3_f32 v206, v132, v133, v134\n"
"v_max3_f32 v206, v206, v135, v140\n"
"v_max3_f32 v206, v206, v141, v142\n"
"v_max3_f32 v206, v206, v143, v148\n"
"v_max3_f32 v206, v206, v149, v150\n"
"v_max3_f32 v206, v206, v151, v156\n"
"v_max3_f32 v206, v206, v157, v158\n"
"v_max_f32 v206, v206, v159\n"
"v_mov_b32 v207, v206\n"
"s_nop 1\n"
"v_permlane32_swap_b32 v207, v206\n"
"v_max_f32 v206, v206, v207\n"
"v_mov_b32 v207, v206\n"
"s_nop 1\n"
"v_permlane16_swap_b32 v207, v206\n"
"v_max_f32 v206, v206, v207\n"
"s_cmp_lt_i32 s43, 0\n"
"s_cbranch_scc1 L_first_n_1\n"
"v_max_f32 v206, 0, v206\n"
"v_exp_f32_e64 v208, -v206\n"
"s_nop 0\n"
"v_mul_f32 v201, v201, v208\n"
"v_mul_f32 v4, v4, v208\n"
"v_mul_f32 v5, v5, v208\n"
"v_mul_f32 v6, v6, v208\n"
"v_mul_f32 v7, v7, v208\n"
"v_mul_f32 v12, v12, v208\n"
"v_mul_f32 v13, v13, v208\n"
"v_mul_f32 v14, v14, v208\n"
"v_mul_f32 v15, v15, v208\n"
"v_mul_f32 v20, v20, v208\n"
"v_mul_f32 v21, v21, v208\n"
"v_mul_f32 v22, v22, v208\n"
"v_mul_f32 v23, v23, v208\n"
"v_mul_f32 v28, v28, v208\n"
"v_mul_f32 v29, v29, v208\n"
"v_mul_f32 v30, v30, v208\n"
"v_mul_f32 v31, v31, v208\n"
"v_mul_f32 v36, v36, v208\n"
"v_mul_f32 v37, v37, v208\n"
"v_mul_f32 v38, v38, v208\n"
"v_mul_f32 v39, v39, v208\n"
"v_mul_f32 v44, v44, v208\n"
"v_mul_f32 v45, v45, v208\n"
"v_mul_f32 v46, v46, v208\n"
"v_mul_f32 v47, v47, v208\n"
"v_mul_f32 v52, v52, v208\n"
"v_mul_f32 v53, v53, v208\n"
"v_mul_f32 v54, v54, v208\n"
"v_mul_f32 v55, v55, v208\n"
"v_mul_f32 v60, v60, v208\n"
"v_mul_f32 v61, v61, v208\n"
"v_mul_f32 v62, v62, v208\n"
"v_mul_f32 v63, v63, v208\n"
"v_mul_f32 v68, v68, v208\n"
"v_mul_f32 v69, v69, v208\n"
"v_mul_f32 v70, v70, v208\n"
"v_mul_f32 v71, v71, v208\n"
"v_mul_f32 v76, v76, v208\n"
"v_mul_f32 v77, v77, v208\n"
"v_mul_f32 v78, v78, v208\n"
"v_mul_f32 v79, v79, v208\n"
"v_mul_f32 v84, v84, v208\n"
"v_mul_f32 v85, v85, v208\n"
"v_mul_f32 v86, v86, v208\n"
"v_mul_f32 v87, v87, v208\n"
"v_mul_f32 v92, v92, v208\n"
"v_mul_f32 v93, v93, v208\n"
"v_mul_f32 v94, v94, v208\n"
"v_mul_f32 v95, v95, v208\n"
"v_mul_f32 v100, v100, v208\n"
"v_mul_f32 v101, v101, v208\n"
"v_mul_f32 v102, v102, v208\n"
"v_mul_f32 v103, v103, v208\n"
"v_mul_f32 v108, v108, v208\n"
"v_mul_f32 v109, v109, v208\n"
"v_mul_f32 v110, v110, v208\n"
"v_mul_f32 v111, v111, v208\n"
"v_mul_f32 v116, v116, v208\n"
"v_mul_f32 v117, v117, v208\n"
"v_mul_f32 v118, v118, v208\n"
"v_mul_f32 v119, v119, v208\n"
"v_mul_f32 v124, v124, v208\n"
"v_mul_f32 v125, v125, v208\n"
"v_mul_f32 v126, v126, v208\n"
"v_mul_f32 v127, v127, v208\n"
"L_first_n_1:\n"
"s_mov_b32 s43, 0x44800000\n"
"v_sub_f32 v180, v180, v206\n"
"v_sub_f32 v181, v181, v206\n"
"v_sub_f32 v182, v182, v206\n"
"v_sub_f32 v183, v183, v206\n"
"v_sub_f32 v132, v132, v206\n"
"v_sub_f32 v133, v133, v206\n"
"v_sub_f32 v134, v134, v206\n"
"v_sub_f32 v135, v135, v206\n"
"v_sub_f32 v140, v140, v206\n"
"v_sub_f32 v141, v141, v206\n"
"v_sub_f32 v142, v142, v206\n"
"v_sub_f32 v143, v143, v206\n"
"v_sub_f32 v148, v148, v206\n"
"v_sub_f32 v149, v149, v206\n"
"v_sub_f32 v150, v150, v206\n"
"v_sub_f32 v151, v151, v206\n"
"v_sub_f32 v156, v156, v206\n"
"v_sub_f32 v157, v157, v206\n"
"v_sub_f32 v158, v158, v206\n"
"v_sub_f32 v159, v159, v206\n"
"v_exp_f32 v192, v132\n"
"v_exp_f32 v193, v133\n"
"s_nop 0\n"
"v_add_f32 v203, v192, v193\n"
"v_cvt_pk_bf16_f32 v168, v192, v193\n"
"v_exp_f32 v194, v134\n"
"v_exp_f32 v195, v135\n"
"v_add_f32 v203, v203, v194\n"
"v_add_f32 v203, v203, v195\n"
"v_cvt_pk_bf16_f32 v169, v194, v195\n"
"v_exp_f32 v196, v140\n"
"v_exp_f32 v197, v141\n"
"v_exp_f32 v198, v142\n"
"v_add_f32 v203, v203, v196\n"
"v_add_f32 v203, v203, v197\n"
"v_cvt_pk_bf16_f32 v170, v196, v197\n"
"v_exp_f32 v199, v143\n"
"v_add_f32 v203, v203, v198\n"
"v_add_f32 v203, v203, v199\n"
"v_cvt_pk_bf16_f32 v171, v198, v199\n"
"s_nop 1\n"
"s_branch L_ret_n_01\n"
"L_slow_n_10:\n"
"s_nop 7\n"
"v_max3_f32 v206, v144, v145, v146\n"
"v_max3_f32 v206, v206, v147, v152\n"
"v_max3_f32 v206, v206, v153, v154\n"
"v_max_f32 v206, v206, v155\n"
"v_mov_b32 v207, v206\n"
"s_nop 1\n"
"v_permlane32_swap_b32 v207, v206\n"
"v_max_f32 v206, v206, v207\n"
"v_mov_b32 v207, v206\n"
"s_nop 1\n"
"v_permlane16_swap_b32 v207, v206\n"
"v_max_f32 v206, v206, v207\n"
"v_max_f32 v206, 0, v206\n"
"v_exp_f32_e64 v208, -v206\n"
"s_nop 0\n"
"v_mul_f32 v200, v200, v208\n"
"v_mul_f32 v0, v0, v208\n"
"v_mul_f32 v1, v1, v208\n"
"v_mul_f32 v2, v2, v208\n"
"v_mul_f32 v3, v3, v208\n"
"v_mul_f32 v8, v8, v208\n"
"v_mul_f32 v9, v9, v208\n"
"v_mul_f32 v10, v10, v208\n"
"v_mul_f32 v11, v11, v208\n"
"v_mul_f32 v16, v16, v208\n"
"v_mul_f32 v17, v17, v208\n"
"v_mul_f32 v18, v18, v208\n"
"v_mul_f32 v19, v19, v208\n"
"v_mul_f32 v24, v24, v208\n"
"v_mul_f32 v25, v25, v208\n"
"v_mul_f32 v26, v26, v208\n"
"v_mul_f32 v27, v27, v208\n"
"v_mul_f32 v32, v32, v208\n"
"v_mul_f32 v33, v33, v208\n"
"v_mul_f32 v34, v34, v208\n"
"v_mul_f32 v35, v35, v208\n"
"v_mul_f32 v40, v40, v208\n"
"v_mul_f32 v41, v41, v208\n"
"v_mul_f32 v42, v42, v208\n"
"v_mul_f32 v43, v43, v208\n"
"v_mul_f32 v48, v48, v208\n"
"v_mul_f32 v49, v49, v208\n"
"v_mul_f32 v50, v50, v208\n"
"v_mul_f32 v51, v51, v208\n"
"v_mul_f32 v56, v56, v208\n"
"v_mul_f32 v57, v57, v208\n"
"v_mul_f32 v58, v58, v208\n"
"v_mul_f32 v59, v59, v208\n"
"v_mul_f32 v64, v64, v208\n"
"v_mul_f32 v65, v65, v208\n"
"v_mul_f32 v66, v66, v208\n"
"v_mul_f32 v67, v67, v208\n"
"v_mul_f32 v72, v72, v208\n"
"v_mul_f32 v73, v73, v208\n"
"v_mul_f32 v74, v74, v208\n"
"v_mul_f32 v75, v75, v208\n"
"v_mul_f32 v80, v80, v208\n"
"v_mul_f32 v81, v81, v208\n"
"v_mul_f32 v82, v82, v208\n"
"v_mul_f32 v83, v83, v208\n"
"v_mul_f32 v88, v88, v208\n"
"v_mul_f32 v89, v89, v208\n"
"v_mul_f32 v90, v90, v208\n"
"v_mul_f32 v91, v91, v208\n"
"v_mul_f32 v96, v96, v208\n"
"v_mul_f32 v97, v97, v208\n"
"v_mul_f32 v98, v98, v208\n"
"v_mul_f32 v99, v99, v208\n"
"v_mul_f32 v104, v104, v208\n"
"v_mul_f32 v105, v105, v208\n"
"v_mul_f32 v106, v106, v208\n"
"v_mul_f32 v107, v107, v208\n"
"v_mul_f32 v112, v112, v208\n"
"v_mul_f32 v113, v113, v208\n"
"v_mul_f32 v114, v114, v208\n"
"v_mul_f32 v115, v115, v208\n"
"v_mul_f32 v120, v120, v208\n"
"v_mul_f32 v121, v121, v208\n"
"v_mul_f32 v122, v122, v208\n"
"v_mul_f32 v123, v123, v208\n"
"v_sub_f32 v176, v176, v206\n"
"v_sub_f32 v177, v177, v206\n"
"v_sub_f32 v178, v178, v206\n"
"v_sub_f32 v179, v179, v206\n"
"v_sub_f32 v144, v144, v206\n"
"v_sub_f32 v145, v145, v206\n"
"v_sub_f32 v146, v146, v206\n"
"v_sub_f32 v147, v147, v206\n"
"v_sub_f32 v152, v152, v206\n"
"v_sub_f32 v153, v153, v206\n"
"v_sub_f32 v154, v154, v206\n"
"v_sub_f32 v155, v155, v206\n"
"v_exp_f32 v184, v144\n"
"v_exp_f32 v185, v145\n"
"s_nop 0\n"
"v_add_f32 v204, v184, v185\n"
"v_cvt_pk_bf16_f32 v164, v184, v185\n"
"v_exp_f32 v186, v146\n"
"v_exp_f32 v187, v147\n"
"v_exp_f32 v188, v152\n"
"v_add_f32 v204, v204, v186\n"
"v_add_f32 v204, v204, v187\n"
"v_cvt_pk_bf16_f32 v165, v186, v187\n"
"v_exp_f32 v189, v153\n"
"v_add_f32 v204, v204, v188\n"
"v_add_f32 v204, v204, v189\n"
"v_exp_f32 v190, v154\n"
"v_cvt_pk_bf16_f32 v166, v188, v189\n"
"v_exp_f32 v191, v155\n"
"v_add_f32 v204, v204, v190\n"
"v_add_f32 v204, v204, v191\n"
"v_cvt_pk_bf16_f32 v167, v190, v191\n"
"s_nop 1\n"
"s_branch L_ret_n_10\n"
"L_slow_n_11:\n"
"s_nop 7\n"
"v_max3_f32 v206, v148, v149, v150\n"
"v_max3_f32 v206, v206, v151, v156\n"
"v_max3_f32 v206, v206, v157, v158\n"
"v_max_f32 v206, v206, v159\n"
"v_mov_b32 v207, v206\n"
"s_nop 1\n"
"v_permlane32_swap_b32 v207, v206\n"
"v_max_f32 v206, v206, v207\n"
"v_mov_b32 v207, v206\n"
"s_nop 1\n"
"v_permlane16_swap_b32 v207, v206\n"
"v_max_f32 v206, v206, v207\n"
"v_max_f32 v206, 0, v206\n"
"v_exp_f32_e64 v208, -v206\n"
"s_nop 0\n"
"v_mul_f32 v201, v201, v208\n"
"v_mul_f32 v4, v4, v208\n"
"v_mul_f32 v5, v5, v208\n"
"v_mul_f32 v6, v6, v208\n"
"v_mul_f32 v7, v7, v208\n"
"v_mul_f32 v12, v12, v208\n"
"v_mul_f32 v13, v13, v208\n"
"v_mul_f32 v14, v14, v208\n"
"v_mul_f32 v15, v15, v208\n"
"v_mul_f32 v20, v20, v208\n"
"v_mul_f32 v21, v21, v208\n"
"v_mul_f32 v22, v22, v208\n"
"v_mul_f32 v23, v23, v208\n"
"v_mul_f32 v28, v28, v208\n"
"v_mul_f32 v29, v29, v208\n"
"v_mul_f32 v30, v30, v208\n"
"v_mul_f32 v31, v31, v208\n"
"v_mul_f32 v36, v36, v208\n"
"v_mul_f32 v37, v37, v208\n"
"v_mul_f32 v38, v38, v208\n"
"v_mul_f32 v39, v39, v208\n"
"v_mul_f32 v44, v44, v208\n"
"v_mul_f32 v45, v45, v208\n"
"v_mul_f32 v46, v46, v208\n"
"v_mul_f32 v47, v47, v208\n"
"v_mul_f32 v52, v52, v208\n"
"v_mul_f32 v53, v53, v208\n"
"v_mul_f32 v54, v54, v208\n"
"v_mul_f32 v55, v55, v208\n"
"v_mul_f32 v60, v60, v208\n"
"v_mul_f32 v61, v61, v208\n"
"v_mul_f32 v62, v62, v208\n"
"v_mul_f32 v63, v63, v208\n"
"v_mul_f32 v68, v68, v208\n"
"v_mul_f32 v69, v69, v208\n"
"v_mul_f32 v70, v70, v208\n"
"v_mul_f32 v71, v71, v208\n"
"v_mul_f32 v76, v76, v208\n"
"v_mul_f32 v77, v77, v208\n"
"v_mul_f32 v78, v78, v208\n"
"v_mul_f32 v79, v79, v208\n"
"v_mul_f32 v84, v84, v208\n"
"v_mul_f32 v85, v85, v208\n"
"v_mul_f32 v86, v86, v208\n"
"v_mul_f32 v87, v87, v208\n"
"v_mul_f32 v92, v92, v208\n"
"v_mul_f32 v93, v93, v208\n"
"v_mul_f32 v94, v94, v208\n"
"v_mul_f32 v95, v95, v208\n"
"v_mul_f32 v100, v100, v208\n"
"v_mul_f32 v101, v101, v208\n"
"v_mul_f32 v102, v102, v208\n"
"v_mul_f32 v103, v103, v208\n"
"v_mul_f32 v108, v108, v208\n"
"v_mul_f32 v109, v109, v208\n"
"v_mul_f32 v110, v110, v208\n"
"v_mul_f32 v111, v111, v208\n"
"v_mul_f32 v116, v116, v208\n"
"v_mul_f32 v117, v117, v208\n"
"v_mul_f32 v118, v118, v208\n"
"v_mul_f32 v119, v119, v208\n"
"v_mul_f32 v124, v124, v208\n"
"v_mul_f32 v125, v125, v208\n"
"v_mul_f32 v126, v126, v208\n"
"v_mul_f32 v127, v127, v208\n"
"v_sub_f32 v180, v180, v206\n"
"v_sub_f32 v181, v181, v206\n"
"v_sub_f32 v182, v182, v206\n"
"v_sub_f32 v183, v183, v206\n"
"v_sub_f32 v148, v148, v206\n"
"v_sub_f32 v149, v149, v206\n"
"v_sub_f32 v150, v150, v206\n"
"v_sub_f32 v151, v151, v206\n"
"v_sub_f32 v156, v156, v206\n"
"v_sub_f32 v157, v157, v206\n"
"v_sub_f32 v158, v158, v206\n"
"v_sub_f32 v159, v159, v206\n"
"v_exp_f32 v192, v148\n"
"v_exp_f32 v193, v149\n"
"s_nop 0\n"
"v_add_f32 v205, v192, v193\n"
"v_cvt_pk_bf16_f32 v172, v192, v193\n"
"v_exp_f32 v194, v150\n"
"v_exp_f32 v195, v151\n"
"v_add_f32 v205, v205, v194\n"
"v_exp_f32 v196, v156\n"
"v_add_f32 v205, v205, v195\n"
"v_cvt_pk_bf16_f32 v173, v194, v195\n"
"v_exp_f32 v197, v157\n"
"v_add_f32 v205, v205, v196\n"
"v_add_f32 v205, v205, v197\n"
"v_exp_f32 v198, v158\n"
"v_cvt_pk_bf16_f32 v174, v196, v197\n"
"v_exp_f32 v199, v159\n"
"v_add_f32 v205, v205, v198\n"
"v_add_f32 v205, v205, v199\n"
"v_cvt_pk_bf16_f32 v175, v198, v199\n"
"s_nop 1\n"
"s_branch L_ret_n_11\n"
"L_slow_m_00:\n"
"s_nop 7\n"
"v_max3_f32 v206, v128, v129, v130\n"
"v_max3_f32 v206, v206, v131, v136\n"
"v_max3_f32 v206, v206, v137, v138\n"
"v_max3_f32 v206, v206, v139, v144\n"
"v_max3_f32 v206, v206, v145, v146\n"
"v_max3_f32 v206, v206, v147, v152\n"
"v_max3_f32 v206, v206, v153, v154\n"
"v_max_f32 v206, v206, v155\n"
"v_mov_b32 v207, v206\n"
"s_nop 1\n"
"v_permlane32_swap_b32 v207, v206\n"
"v_max_f32 v206, v206, v207\n"
"v_mov_b32 v207, v206\n"
"s_nop 1\n"
"v_permlane16_swap_b32 v207, v206\n"
"v_max_f32 v206, v206, v207\n"
"s_cmp_lt_i32 s42, 0\n"
"s_cbranch_scc1 L_first_m_0\n"
"v_max_f32 v206, 0, v206\n"
"v_exp_f32_e64 v208, -v206\n"
"s_nop 0\n"
"v_mul_f32 v200, v200, v208\n"
"v_mul_f32 v0, v0, v208\n"
"v_mul_f32 v1, v1, v208\n"
"v_mul_f32 v2, v2, v208\n"
"v_mul_f32 v3, v3, v208\n"
"v_mul_f32 v8, v8, v208\n"
"v_mul_f32 v9, v9, v208\n"
"v_mul_f32 v10, v10, v208\n"
"v_mul_f32 v11, v11, v208\n"
"v_mul_f32 v16, v16, v208\n"
"v_mul_f32 v17, v17, v208\n"
"v_mul_f32 v18, v18, v208\n"
"v_mul_f32 v19, v19, v208\n"
"v_mul_f32 v24, v24, v208\n"
"v_mul_f32 v25, v25, v208\n"
"v_mul_f32 v26, v26, v208\n"
"v_mul_f32 v27, v27, v208\n"
"v_mul_f32 v32, v32, v208\n"
"v_mul_f32 v33, v33, v208\n"
"v_mul_f32 v34, v34, v208\n"
"v_mul_f32 v35, v35, v208\n"
"v_mul_f32 v40, v40, v208\n"
"v_mul_f32 v41, v41, v208\n"
"v_mul_f32 v42, v42, v208\n"
"v_mul_f32 v43, v43, v208\n"
"v_mul_f32 v48, v48, v208\n"
"v_mul_f32 v49, v49, v208\n"
"v_mul_f32 v50, v50, v208\n"
"v_mul_f32 v51, v51, v208\n"
"v_mul_f32 v56, v56, v208\n"
"v_mul_f32 v57, v57, v208\n"
"v_mul_f32 v58, v58, v208\n"
"v_mul_f32 v59, v59, v208\n"
"v_mul_f32 v64, v64, v208\n"
"v_mul_f32 v65, v65, v208\n"
"v_mul_f32 v66, v66, v208\n"
"v_mul_f32 v67, v67, v208\n"
"v_mul_f32 v72, v72, v208\n"
"v_mul_f32 v73, v73, v208\n"
"v_mul_f32 v74, v74, v208\n"
"v_mul_f32 v75, v75, v208\n"
"v_mul_f32 v80, v80, v208\n"
"v_mul_f32 v81, v81, v208\n"
"v_mul_f32 v82, v82, v208\n"
"v_mul_f32 v83, v83, v208\n"
"v_mul_f32 v88, v88, v208\n"
"v_mul_f32 v89, v89, v208\n"
"v_mul_f32 v90, v90, v208\n"
"v_mul_f32 v91, v91, v208\n"
"v_mul_f32 v96, v96, v208\n"
"v_mul_f32 v97, v97, v208\n"
"v_mul_f32 v98, v98, v208\n"
"v_mul_f32 v99, v99, v208\n"
"v_mul_f32 v104, v104, v208\n"
"v_mul_f32 v105, v105, v208\n"
"v_mul_f32 v106, v106, v208\n"
"v_mul_f32 v107, v107, v208\n"
"v_mul_f32 v112, v112, v208\n"
"v_mul_f32 v113, v113, v208\n"
"v_mul_f32 v114, v114, v208\n"
"v_mul_f32 v115, v115, v208\n"
"v_mul_f32 v120, v120, v208\n"
"v_mul_f32 v121, v121, v208\n"
"v_mul_f32 v122, v122, v208\n"
"v_mul_f32 v123, v123, v208\n"
"L_first_m_0:\n"
"s_mov_b32 s42, 0x44800000\n"
"v_sub_f32 v176, v176, v206\n"
"v_sub_f32 v177, v177, v206\n"
"v_sub_f32 v178, v178, v206\n"
"v_sub_f32 v179, v179, v206\n"
"v_sub_f32 v128, v128, v206\n"
"v_sub_f32 v129, v129, v206\n"
"v_sub_f32 v130, v130, v206\n"
"v_sub_f32 v131, v131, v206\n"
"v_sub_f32 v136, v136, v206\n"
"v_sub_f32 v137, v137, v206\n"
"v_sub_f32 v138, v138, v206\n"
"v_sub_f32 v139, v139, v206\n"
"v_sub_f32 v144, v144, v206\n"
"v_sub_f32 v145, v145, v206\n"
"v_sub_f32 v146, v146, v206\n"
"v_sub_f32 v147, v147, v206\n"
"v_sub_f32 v152, v152, v206\n"
"v_sub_f32 v153, v153, v206\n"
"v_sub_f32 v154, v154, v206\n"
"v_sub_f32 v155, v155, v206\n"
"v_exp_f32 v184, v128\n"
"v_exp_f32 v185, v129\n"
"s_nop 0\n"
"v_add_f32 v202, v184, v185\n"
"v_exp_f32 v186, v130\n"
"v_cvt_pk_bf16_f32 v160, v184, v185\n"
"v_exp_f32 v187, v131\n"
"v_add_f32 v202, v202, v186\n"
"v_add_f32 v202, v202, v187\n"
"v_cvt_pk_bf16_f32 v161, v186, v187\n"
"v_exp_f32 v188, v136\n"
"v_exp_f32 v189, v137\n"
"v_add_f32 v202, v202, v188\n"
"v_add_f32 v202, v202, v189\n"
"v_cvt_pk_bf16_f32 v162, v188, v189\n"
"v_exp_f32 v190, v138\n"
"v_exp_f32 v191, v139\n"
"v_add_f32 v202, v202, v190\n"
"v_add_f32 v202, v202, v191\n"
"v_cvt_pk_bf16_f32 v163, v190, v191\n"
"v_exp_f32 v184, v144\n"
"v_exp_f32 v185, v145\n"
"s_nop 0\n"
"v_add_f32 v204, v184, v185\n"
"v_cvt_pk_bf16_f32 v164, v184, v185\n"
"s_nop 1\n"
"s_branch L_ret_m_00\n"
"L_slow_m_01:\n"
"s_nop 7\n"
"v_max3_f32 v206, v132, v133, v134\n"
"v_max3_f32 v206, v206, v135, v140\n"
"v_max3_f32 v206, v206, v141, v142\n"
"v_max3_f32 v206, v206, v143, v148\n"
"v_max3_f32 v206, v206, v149, v150\n"
"v_max3_f32 v206, v206, v151, v156\n"
"v_max3_f32 v206, v206, v157, v158\n"
"v_max_f32 v206, v206, v159\n"
"v_mov_b32 v207, v206\n"
"s_nop 1\n"
"v_permlane32_swap_b32 v207, v206\n"
"v_max_f32 v206, v206, v207\n"
"v_mov_b32 v207, v206\n"
"s_nop 1\n"
"v_permlane16_swap_b32 v207, v206\n"
"v_max_f32 v206, v206, v207\n"
"s_cmp_lt_i32 s43, 0\n"
"s_cbranch_scc1 L_first_m_1\n"
"v_max_f32 v206, 0, v206\n"
"v_exp_f32_e64 v208, -v206\n"
"s_nop 0\n"
"v_mul_f32 v201, v201, v208\n"
"v_mul_f32 v4, v4, v208\n"
"v_mul_f32 v5, v5, v208\n"
"v_mul_f32 v6, v6, v208\n"
"v_mul_f32 v7, v7, v208\n"
"v_mul_f32 v12, v12, v208\n"
"v_mul_f32 v13, v13, v208\n"
"v_mul_f32 v14, v14, v208\n"
"v_mul_f32 v15, v15, v208\n"
"v_mul_f32 v20, v20, v208\n"
"v_mul_f32 v21, v21, v208\n"
"v_mul_f32 v22, v22, v208\n"
"v_mul_f32 v23, v23, v208\n"
"v_mul_f32 v28, v28, v208\n"
"v_mul_f32 v29, v29, v208\n"
"v_mul_f32 v30, v30, v208\n"
"v_mul_f32 v31, v31, v208\n"
"v_mul_f32 v36, v36, v208\n"
"v_mul_f32 v37, v37, v208\n"
"v_mul_f32 v38, v38, v208\n"
"v_mul_f32 v39, v39, v208\n"
"v_mul_f32 v44, v44, v208\n"
"v_mul_f32 v45, v45, v208\n"
"v_mul_f32 v46, v46, v208\n"
"v_mul_f32 v47, v47, v208\n"
"v_mul_f32 v52, v52, v208\n"
"v_mul_f32 v53, v53, v208\n"
"v_mul_f32 v54, v54, v208\n"
"v_mul_f32 v55, v55, v208\n"
"v_mul_f32 v60, v60, v208\n"
"v_mul_f32 v61, v61, v208\n"
"v_mul_f32 v62, v62, v208\n"
"v_mul_f32 v63, v63, v208\n"
"v_mul_f32 v68, v68, v208\n"
"v_mul_f32 v69, v69, v208\n"
"v_mul_f32 v70, v70, v208\n"
"v_mul_f32 v71, v71, v208\n"
"v_mul_f32 v76, v76, v208\n"
"v_mul_f32 v77, v77, v208\n"
"v_mul_f32 v78, v78, v208\n"
"v_mul_f32 v79, v79, v208\n"
"v_mul_f32 v84, v84, v208\n"
"v_mul_f32 v85, v85, v208\n"
"v_mul_f32 v86, v86, v208\n"
"v_mul_f32 v87, v87, v208\n"
"v_mul_f32 v92, v92, v208\n"
"v_mul_f32 v93, v93, v208\n"
"v_mul_f32 v94, v94, v208\n"
"v_mul_f32 v95, v95, v208\n"
"v_mul_f32 v100, v100, v208\n"
"v_mul_f32 v101, v101, v208\n"
"v_mul_f32 v102, v102, v208\n"
"v_mul_f32 v103, v103, v208\n"
"v_mul_f32 v108, v108, v208\n"
"v_mul_f32 v109, v109, v208\n"
"v_mul_f32 v110, v110, v208\n"
"v_mul_f32 v111, v111, v208\n"
"v_mul_f32 v116, v116, v208\n"
"v_mul_f32 v117, v117, v208\n"
"v_mul_f32 v118, v118, v208\n"
"v_mul_f32 v119, v119, v208\n"
"v_mul_f32 v124, v124, v208\n"
"v_mul_f32 v125, v125, v208\n"
"v_mul_f32 v126, v126, v208\n"
"v_mul_f32 v127, v127, v208\n"
"L_first_m_1:\n"
"s_mov_b32 s43, 0x44800000\n"
"v_sub_f32 v180, v180, v206\n"
"v_sub_f32 v181, v181, v206\n"
"v_sub_f32 v182, v182, v206\n"
"v_sub_f32 v183, v183, v206\n"
"v_sub_f32 v132, v132, v206\n"
"v_sub_f32 v133, v133, v206\n"
"v_sub_f32 v134, v134, v206\n"
"v_sub_f32 v135, v135, v206\n"
"v_sub_f32 v140, v140, v206\n"
"v_sub_f32 v141, v141, v206\n"
"v_sub_f32 v142, v142, v206\n"
"v_sub_f32 v143, v143, v206\n"
"v_sub_f32 v148, v148, v206\n"
"v_sub_f32 v149, v149, v206\n"
"v_sub_f32 v150, v150, v206\n"
"v_sub_f32 v151, v151, v206\n"
"v_sub_f32 v156, v156, v206\n"
"v_sub_f32 v157, v157, v206\n"
"v_sub_f32 v158, v158, v206\n"
"v_sub_f32 v159, v159, v206\n"
"v_exp_f32 v192, v132\n"
"v_exp_f32 v193, v133\n"
"s_nop 0\n"
"v_add_f32 v203, v192, v193\n"
"v_cvt_pk_bf16_f32 v168, v192, v193\n"
"v_exp_f32 v194, v134\n"
"v_exp_f32 v195, v135\n"
"v_add_f32 v203, v203, v194\n"
"v_add_f32 v203, v203, v195\n"
"v_cvt_pk_bf16_f32 v169, v194, v195\n"
"v_exp_f32 v196, v140\n"
"v_exp_f32 v197, v141\n"
"v_exp_f32 v198, v142\n"
"v_add_f32 v203, v203, v196\n"
"v_add_f32 v203, v203, v197\n"
"v_cvt_pk_bf16_f32 v170, v196, v197\n"
"v_exp_f32 v199, v143\n"
"v_add_f32 v203, v203, v198\n"
"v_add_f32 v203, v203, v199\n"
"v_cvt_pk_bf16_f32 v171, v198, v199\n"
"s_nop 1\n"
"s_branch L_ret_m_01\n"
"L_slow_m_10:\n"
"s_nop 7\n"
"v_max3_f32 v206, v144, v145, v146\n"
"v_max3_f32 v206, v206, v147, v152\n"
"v_max3_f32 v206, v206, v153, v154\n"
"v_max_f32 v206, v206, v155\n"
"v_mov_b32 v207, v206\n"
"s_nop 1\n"
"v_permlane32_swap_b32 v207, v206\n"
"v_max_f32 v206, v206, v207\n"
"v_mov_b32 v207, v206\n"
"s_nop 1\n"
"v_permlane16_swap_b32 v207, v206\n"
"v_max_f32 v206, v206, v207\n"
"v_max_f32 v206, 0, v206\n"
"v_exp_f32_e64 v208, -v206\n"
"s_nop 0\n"
"v_mul_f32 v200, v200, v208\n"
"v_mul_f32 v0, v0, v208\n"
"v_mul_f32 v1, v1, v208\n"
"v_mul_f32 v2, v2, v208\n"
"v_mul_f32 v3, v3, v208\n"
"v_mul_f32 v8, v8, v208\n"
"v_mul_f32 v9, v9, v208\n"
"v_mul_f32 v10, v10, v208\n"
"v_mul_f32 v11, v11, v208\n"
"v_mul_f32 v16, v16, v208\n"
"v_mul_f32 v17, v17, v208\n"
"v_mul_f32 v18, v18, v208\n"
"v_mul_f32 v19, v19, v208\n"
"v_mul_f32 v24, v24, v208\n"
"v_mul_f32 v25, v25, v208\n"
"v_mul_f32 v26, v26, v208\n"
"v_mul_f32 v27, v27, v208\n"
"v_mul_f32 v32, v32, v208\n"
"v_mul_f32 v33, v33, v208\n"
"v_mul_f32 v34, v34, v208\n"
"v_mul_f32 v35, v35, v208\n"
"v_mul_f32 v40, v40, v208\n"
"v_mul_f32 v41, v41, v208\n"
"v_mul_f32 v42, v42, v208\n"
"v_mul_f32 v43, v43, v208\n"
"v_mul_f32 v48, v48, v208\n"
"v_mul_f32 v49, v49, v208\n"
"v_mul_f32 v50, v50, v208\n"
"v_mul_f32 v51, v51, v208\n"
"v_mul_f32 v56, v56, v208\n"
"v_mul_f32 v57, v57, v208\n"
"v_mul_f32 v58, v58, v208\n"
"v_mul_f32 v59, v59, v208\n"
"v_mul_f32 v64, v64, v208\n"
"v_mul_f32 v65, v65, v208\n"
"v_mul_f32 v66, v66, v208\n"
"v_mul_f32 v67, v67, v208\n"
"v_mul_f32 v72, v72, v208\n"
"v_mul_f32 v73, v73, v208\n"
"v_mul_f32 v74, v74, v208\n"
"v_mul_f32 v75, v75, v208\n"
"v_mul_f32 v80, v80, v208\n"
"v_mul_f32 v81, v81, v208\n"
"v_mul_f32 v82, v82, v208\n"
"v_mul_f32 v83, v83, v208\n"
"v_mul_f32 v88, v88, v208\n"
"v_mul_f32 v89, v89, v208\n"
"v_mul_f32 v90, v90, v208\n"
"v_mul_f32 v91, v91, v208\n"
"v_mul_f32 v96, v96, v208\n"
"v_mul_f32 v97, v97, v208\n"
"v_mul_f32 v98, v98, v208\n"
"v_mul_f32 v99, v99, v208\n"
"v_mul_f32 v104, v104, v208\n"
"v_mul_f32 v105, v105, v208\n"
"v_mul_f32 v106, v106, v208\n"
"v_mul_f32 v107, v107, v208\n"
"v_mul_f32 v112, v112, v208\n"
"v_mul_f32 v113, v113, v208\n"
"v_mul_f32 v114, v114, v208\n"
"v_mul_f32 v115, v115, v208\n"
"v_mul_f32 v120, v120, v208\n"
"v_mul_f32 v121, v121, v208\n"
"v_mul_f32 v122, v122, v208\n"
"v_mul_f32 v123, v123, v208\n"
"v_sub_f32 v176, v176, v206\n"
"v_sub_f32 v177, v177, v206\n"
"v_sub_f32 v178, v178, v206\n"
"v_sub_f32 v179, v179, v206\n"
"v_sub_f32 v144, v144, v206\n"
"v_sub_f32 v145, v145, v206\n"
"v_sub_f32 v146, v146, v206\n"
"v_sub_f32 v147, v147, v206\n"
"v_sub_f32 v152, v152, v206\n"
"v_sub_f32 v153, v153, v206\n"
"v_sub_f32 v154, v154, v206\n"
"v_sub_f32 v155, v155, v206\n"
"v_exp_f32 v184, v144\n"
"v_exp_f32 v185, v145\n"
"s_nop 0\n"
"v_add_f32 v204, v184, v185\n"
"v_cvt_pk_bf16_f32 v164, v184, v185\n"
"v_exp_f32 v186, v146\n"
"v_exp_f32 v187, v147\n"
"v_exp_f32 v188, v152\n"
"v_add_f32 v204, v204, v186\n"
"v_add_f32 v204, v204, v187\n"
"v_cvt_pk_bf16_f32 v165, v186, v187\n"
"v_exp_f32 v189, v153\n"
"v_add_f32 v204, v204, v188\n"
"v_add_f32 v204, v204, v189\n"
"v_exp_f32 v190, v154\n"
"v_cvt_pk_bf16_f32 v166, v188, v189\n"
"v_exp_f32 v191, v155\n"
"v_add_f32 v204, v204, v190\n"
"v_add_f32 v204, v204, v191\n"
"v_cvt_pk_bf16_f32 v167, v190, v191\n"
"s_nop 1\n"
"s_branch L_ret_m_10\n"
"L_slow_m_11:\n"
"s_nop 7\n"
"v_max3_f32 v206, v148, v149, v150\n"
"v_max3_f32 v206, v206, v151, v156\n"
"v_max3_f32 v206, v206, v157, v158\n"
"v_max_f32 v206, v206, v159\n"
"v_mov_b32 v207, v206\n"
"s_nop 1\n"
"v_permlane32_swap_b32 v207, v206\n"
"v_max_f32 v206, v206, v207\n"
"v_mov_b32 v207, v206\n"
"s_nop 1\n"
"v_permlane16_swap_b32 v207, v206\n"
"v_max_f32 v206, v206, v207\n"
"v_max_f32 v206, 0, v206\n"
"v_exp_f32_e64 v208, -v206\n"
"s_nop 0\n"
"v_mul_f32 v201, v201, v208\n"
"v_mul_f32 v4, v4, v208\n"
"v_mul_f32 v5, v5, v208\n"
"v_mul_f32 v6, v6, v208\n"
"v_mul_f32 v7, v7, v208\n"
"v_mul_f32 v12, v12, v208\n"
"v_mul_f32 v13, v13, v208\n"
"v_mul_f32 v14, v14, v208\n"
"v_mul_f32 v15, v15, v208\n"
"v_mul_f32 v20, v20, v208\n"
"v_mul_f32 v21, v21, v208\n"
"v_mul_f32 v22, v22, v208\n"
"v_mul_f32 v23, v23, v208\n"
"v_mul_f32 v28, v28, v208\n"
"v_mul_f32 v29, v29, v208\n"
"v_mul_f32 v30, v30, v208\n"
"v_mul_f32 v31, v31, v208\n"
"v_mul_f32 v36, v36, v208\n"
"v_mul_f32 v37, v37, v208\n"
"v_mul_f32 v38, v38, v208\n"
"v_mul_f32 v39, v39, v208\n"
"v_mul_f32 v44, v44, v208\n"
"v_mul_f32 v45, v45, v208\n"
"v_mul_f32 v46, v46, v208\n"
"v_mul_f32 v47, v47, v208\n"
"v_mul_f32 v52, v52, v208\n"
"v_mul_f32 v53, v53, v208\n"
"v_mul_f32 v54, v54, v208\n"
"v_mul_f32 v55, v55, v208\n"
"v_mul_f32 v60, v60, v208\n"
"v_mul_f32 v61, v61, v208\n"
"v_mul_f32 v62, v62, v208\n"
"v_mul_f32 v63, v63, v208\n"
"v_mul_f32 v68, v68, v208\n"
"v_mul_f32 v69, v69, v208\n"
"v_mul_f32 v70, v70, v208\n"
"v_mul_f32 v71, v71, v208\n"
"v_mul_f32 v76, v76, v208\n"
"v_mul_f32 v77, v77, v208\n"
"v_mul_f32 v78, v78, v208\n"
"v_mul_f32 v79, v79, v208\n"
"v_mul_f32 v84, v84, v208\n"
"v_mul_f32 v85, v85, v208\n"
"v_mul_f32 v86, v86, v208\n"
"v_mul_f32 v87, v87, v208\n"
"v_mul_f32 v92, v92, v208\n"
"v_mul_f32 v93, v93, v208\n"
"v_mul_f32 v94, v94, v208\n"
"v_mul_f32 v95, v95, v208\n"
"v_mul_f32 v100, v100, v208\n"
"v_mul_f32 v101, v101, v208\n"
"v_mul_f32 v102, v102, v208\n"
"v_mul_f32 v103, v103, v208\n"
"v_mul_f32 v108, v108, v208\n"
"v_mul_f32 v109, v109, v208\n"
"v_mul_f32 v110, v110, v208\n"
"v_mul_f32 v111, v111, v208\n"
"v_mul_f32 v116, v116, v208\n"
"v_mul_f32 v117, v117, v208\n"
"v_mul_f32 v118, v118, v208\n"
"v_mul_f32 v119, v119, v208\n"
"v_mul_f32 v124, v124, v208\n"
"v_mul_f32 v125, v125, v208\n"
"v_mul_f32 v126, v126, v208\n"
"v_mul_f32 v127, v127, v208\n"
"v_sub_f32 v180, v180, v206\n"
"v_sub_f32 v181, v181, v206\n"
"v_sub_f32 v182, v182, v206\n"
"v_sub_f32 v183, v183, v206\n"
"v_sub_f32 v148, v148, v206\n"
"v_sub_f32 v149, v149, v206\n"
"v_sub_f32 v150, v150, v206\n"
"v_sub_f32 v151, v151, v206\n"
"v_sub_f32 v156, v156, v206\n"
"v_sub_f32 v157, v157, v206\n"
"v_sub_f32 v158, v158, v206\n"
"v_sub_f32 v159, v159, v206\n"
"v_exp_f32 v192, v148\n"
"v_exp_f32 v193, v149\n"
"s_nop 0\n"
"v_add_f32 v205, v192, v193\n"
"v_cvt_pk_bf16_f32 v172, v192, v193\n"
"v_exp_f32 v194, v150\n"
"v_exp_f32 v195, v151\n"
"v_add_f32 v205, v205, v194\n"
"v_exp_f32 v196, v156\n"
"v_add_f32 v205, v205, v195\n"
"v_cvt_pk_bf16_f32 v173, v194, v195\n"
"v_exp_f32 v197, v157\n"
"v_add_f32 v205, v205, v196\n"
"v_add_f32 v205, v205, v197\n"
"v_exp_f32 v198, v158\n"
"v_cvt_pk_bf16_f32 v174, v196, v197\n"
"v_exp_f32 v199, v159\n"
"v_add_f32 v205, v205, v198\n"
"v_add_f32 v205, v205, v199\n"
"v_cvt_pk_bf16_f32 v175, v198, v199\n"
"s_nop 1\n"
"s_branch L_ret_m_11\n"
"L_slow_h_01:\n"
"s_nop 7\n"
"v_max3_f32 v206, v132, v133, v134\n"
"v_max3_f32 v206, v206, v135, v140\n"
"v_max3_f32 v206, v206, v141, v142\n"
"v_max3_f32 v206, v206, v143, v148\n"
"v_max3_f32 v206, v206, v149, v150\n"
"v_max3_f32 v206, v206, v151, v156\n"
"v_max3_f32 v206, v206, v157, v158\n"
"v_max_f32 v206, v206, v159\n"
"v_mov_b32 v207, v206\n"
"s_nop 1\n"
"v_permlane32_swap_b32 v207, v206\n"
"v_max_f32 v206, v206, v207\n"
"v_mov_b32 v207, v206\n"
"s_nop 1\n"
"v_permlane16_swap_b32 v207, v206\n"
"v_max_f32 v206, v206, v207\n"
"s_cmp_lt_i32 s43, 0\n"
"s_cbranch_scc1 L_first_h_1\n"
"v_max_f32 v206, 0, v206\n"
"v_exp_f32_e64 v208, -v206\n"
"s_nop 0\n"
"v_mul_f32 v201, v201, v208\n"
"v_mul_f32 v4, v4, v208\n"
"v_mul_f32 v5, v5, v208\n"
"v_mul_f32 v6, v6, v208\n"
"v_mul_f32 v7, v7, v208\n"
"v_mul_f32 v12, v12, v208\n"
"v_mul_f32 v13, v13, v208\n"
"v_mul_f32 v14, v14, v208\n"
"v_mul_f32 v15, v15, v208\n"
"v_mul_f32 v20, v20, v208\n"
"v_mul_f32 v21, v21, v208\n"
"v_mul_f32 v22, v22, v208\n"
"v_mul_f32 v23, v23, v208\n"
"v_mul_f32 v28, v28, v208\n"
"v_mul_f32 v29, v29, v208\n"
"v_mul_f32 v30, v30, v208\n"
"v_mul_f32 v31, v31, v208\n"
"v_mul_f32 v36, v36, v208\n"
"v_mul_f32 v37, v37, v208\n"
"v_mul_f32 v38, v38, v208\n"
"v_mul_f32 v39, v39, v208\n"
"v_mul_f32 v44, v44, v208\n"
"v_mul_f32 v45, v45, v208\n"
"v_mul_f32 v46, v46, v208\n"
"v_mul_f32 v47, v47, v208\n"
"v_mul_f32 v52, v52, v208\n"
"v_mul_f32 v53, v53, v208\n"
"v_mul_f32 v54, v54, v208\n"
"v_mul_f32 v55, v55, v208\n"
"v_mul_f32 v60, v60, v208\n"
"v_mul_f32 v61, v61, v208\n"
"v_mul_f32 v62, v62, v208\n"
"v_mul_f32 v63, v63, v208\n"
"v_mul_f32 v68, v68, v208\n"
"v_mul_f32 v69, v69, v208\n"
"v_mul_f32 v70, v70, v208\n"
"v_mul_f32 v71, v71, v208\n"
"v_mul_f32 v76, v76, v208\n"
"v_mul_f32 v77, v77, v208\n"
"v_mul_f32 v78, v78, v208\n"
"v_mul_f32 v79, v79, v208\n"
"v_mul_f32 v84, v84, v208\n"
"v_mul_f32 v85, v85, v208\n"
"v_mul_f32 v86, v86, v208\n"
"v_mul_f32 v87, v87, v208\n"
"v_mul_f32 v92, v92, v208\n"
"v_mul_f32 v93, v93, v208\n"
"v_mul_f32 v94, v94, v208\n"
"v_mul_f32 v95, v95, v208\n"
"v_mul_f32 v100, v100, v208\n"
"v_mul_f32 v101, v101, v208\n"
"v_mul_f32 v102, v102, v208\n"
"v_mul_f32 v103, v103, v208\n"
"v_mul_f32 v108, v108, v208\n"
"v_mul_f32 v109, v109, v208\n"
"v_mul_f32 v110, v110, v208\n"
"v_mul_f32 v111, v111, v208\n"
"v_mul_f32 v116, v116, v208\n"
"v_mul_f32 v117, v117, v208\n"
"v_mul_f32 v118, v118, v208\n"
"v_mul_f32 v119, v119, v208\n"
"v_mul_f32 v124, v124, v208\n"
"v_mul_f32 v125, v125, v208\n"
"v_mul_f32 v126, v126, v208\n"
"v_mul_f32 v127, v127, v208\n"
"L_first_h_1:\n"
"s_mov_b32 s43, 0x44800000\n"
"v_sub_f32 v180, v180, v206\n"
"v_sub_f32 v181, v181, v206\n"
"v_sub_f32 v182, v182, v206\n"
"v_sub_f32 v183, v183, v206\n"
"v_sub_f32 v132, v132, v206\n"
"v_sub_f32 v133, v133, v206\n"
"v_sub_f32 v134, v134, v206\n"
"v_sub_f32 v135, v135, v206\n"
"v_sub_f32 v140, v140, v206\n"
"v_sub_f32 v141, v141, v206\n"
"v_sub_f32 v142, v142, v206\n"
"v_sub_f32 v143, v143, v206\n"
"v_sub_f32 v148, v148, v206\n"
"v_sub_f32 v149, v149, v206\n"
"v_sub_f32 v150, v150, v206\n"
"v_sub_f32 v151, v151, v206\n"
"v_sub_f32 v156, v156, v206\n"
"v_sub_f32 v157, v157, v206\n"
"v_sub_f32 v158, v158, v206\n"
"v_sub_f32 v159, v159, v206\n"
"v_exp_f32 v192, v132\n"
"v_exp_f32 v193, v133\n"
"s_nop 0\n"
"v_add_f32 v203, v192, v193\n"
"v_cvt_pk_bf16_f32 v168, v192, v193\n"
"v_exp_f32 v194, v134\n"
"v_exp_f32 v195, v135\n"
"v_add_f32 v203, v203, v194\n"
"v_add_f32 v203, v203, v195\n"
"v_cvt_pk_bf16_f32 v169, v194, v195\n"
"v_exp_f32 v196, v140\n"
"v_exp_f32 v197, v141\n"
"v_exp_f32 v198, v142\n"
"v_add_f32 v203, v203, v196\n"
"v_add_f32 v203, v203, v197\n"
"v_cvt_pk_bf16_f32 v170, v196, v197\n"
"v_exp_f32 v199, v143\n"
"v_add_f32 v203, v203, v198\n"
"v_add_f32 v203, v203, v199\n"
"v_cvt_pk_bf16_f32 v171, v198, v199\n"
"s_nop 1\n"
"s_branch L_ret_h_01\n"
"L_slow_h_11:\n"
"s_nop 7\n"
"v_max3_f32 v206, v148, v149, v150\n"
"v_max3_f32 v206, v206, v151, v156\n"
"v_max3_f32 v206, v206, v157, v158\n"
"v_max_f32 v206, v206, v159\n"
"v_mov_b32 v207, v206\n"
"s_nop 1\n"
"v_permlane32_swap_b32 v207, v206\n"
"v_max_f32 v206, v206, v207\n"
"v_mov_b32 v207, v206\n"
"s_nop 1\n"
"v_permlane16_swap_b32 v207, v206\n"
"v_max_f32 v206, v206, v207\n"
"v_max_f32 v206, 0, v206\n"
"v_exp_f32_e64 v208, -v206\n"
"s_nop 0\n"
"v_mul_f32 v201, v201, v208\n"
"v_mul_f32 v4, v4, v208\n"
"v_mul_f32 v5, v5, v208\n"
"v_mul_f32 v6, v6, v208\n"
"v_mul_f32 v7, v7, v208\n"
"v_mul_f32 v12, v12, v208\n"
"v_mul_f32 v13, v13, v208\n"
"v_mul_f32 v14, v14, v208\n"
"v_mul_f32 v15, v15, v208\n"
"v_mul_f32 v20, v20, v208\n"
"v_mul_f32 v21, v21, v208\n"
"v_mul_f32 v22, v22, v208\n"
"v_mul_f32 v23, v23, v208\n"
"v_mul_f32 v28, v28, v208\n"
"v_mul_f32 v29, v29, v208\n"
"v_mul_f32 v30, v30, v208\n"
"v_mul_f32 v31, v31, v208\n"
"v_mul_f32 v36, v36, v208\n"
"v_mul_f32 v37, v37, v208\n"
"v_mul_f32 v38, v38, v208\n"
"v_mul_f32 v39, v39, v208\n"
"v_mul_f32 v44, v44, v208\n"
"v_mul_f32 v45, v45, v208\n"
"v_mul_f32 v46, v46, v208\n"
"v_mul_f32 v47, v47, v208\n"
"v_mul_f32 v52, v52, v208\n"
"v_mul_f32 v53, v53, v208\n"
"v_mul_f32 v54, v54, v208\n"
"v_mul_f32 v55, v55, v208\n"
"v_mul_f32 v60, v60, v208\n"
"v_mul_f32 v61, v61, v208\n"
"v_mul_f32 v62, v62, v208\n"
"v_mul_f32 v63, v63, v208\n"
"v_mul_f32 v68, v68, v208\n"
"v_mul_f32 v69, v69, v208\n"
"v_mul_f32 v70, v70, v208\n"
"v_mul_f32 v71, v71, v208\n"
"v_mul_f32 v76, v76, v208\n"
"v_mul_f32 v77, v77, v208\n"
"v_mul_f32 v78, v78, v208\n"
"v_mul_f32 v79, v79, v208\n"
"v_mul_f32 v84, v84, v208\n"
"v_mul_f32 v85, v85, v208\n"
"v_mul_f32 v86, v86, v208\n"
"v_mul_f32 v87, v87, v208\n"
"v_mul_f32 v92, v92, v208\n"
"v_mul_f32 v93, v93, v208\n"
"v_mul_f32 v94, v94, v208\n"
"v_mul_f32 v95, v95, v208\n"
"v_mul_f32 v100, v100, v208\n"
"v_mul_f32 v101, v101, v208\n"
"v_mul_f32 v102, v102, v208\n"
"v_mul_f32 v103, v103, v208\n"
"v_mul_f32 v108, v108, v208\n"
"v_mul_f32 v109, v109, v208\n"
"v_mul_f32 v110, v110, v208\n"
"v_mul_f32 v111, v111, v208\n"
"v_mul_f32 v116, v116, v208\n"
"v_mul_f32 v117, v117, v208\n"
"v_mul_f32 v118, v118, v208\n"
"v_mul_f32 v119, v119, v208\n"
"v_mul_f32 v124, v124, v208\n"
"v_mul_f32 v125, v125, v208\n"
"v_mul_f32 v126, v126, v208\n"
"v_mul_f32 v127, v127, v208\n"
"v_sub_f32 v180, v180, v206\n"
"v_sub_f32 v181, v181, v206\n"
"v_sub_f32 v182, v182, v206\n"
"v_sub_f32 v183, v183, v206\n"
"v_sub_f32 v148, v148, v206\n"
"v_sub_f32 v149, v149, v206\n"
"v_sub_f32 v150, v150, v206\n"
"v_sub_f32 v151, v151, v206\n"
"v_sub_f32 v156, v156, v206\n"
"v_sub_f32 v157, v157, v206\n"
"v_sub_f32 v158, v158, v206\n"
"v_sub_f32 v159, v159, v206\n"
"v_exp_f32 v192, v148\n"
"v_exp_f32 v193, v149\n"
"s_nop 0\n"
"v_add_f32 v205, v192, v193\n"
"v_cvt_pk_bf16_f32 v172, v192, v193\n"
"v_exp_f32 v194, v150\n"
"v_exp_f32 v195, v151\n"
"v_add_f32 v205, v205, v194\n"
"v_exp_f32 v196, v156\n"
"v_add_f32 v205, v205, v195\n"
"v_cvt_pk_bf16_f32 v173, v194, v195\n"
"v_exp_f32 v197, v157\n"
"v_add_f32 v205, v205, v196\n"
"v_add_f32 v205, v205, v197\n"
"v_exp_f32 v198, v158\n"
"v_cvt_pk_bf16_f32 v174, v196, v197\n"
"v_exp_f32 v199, v159\n"
"v_add_f32 v205, v205, v198\n"
"v_add_f32 v205, v205, v199\n"
"v_cvt_pk_bf16_f32 v175, v198, v199\n"
"s_nop 1\n"
"s_branch L_ret_h_11\n"
"L_at_part:\n"
"v_readlane_b32 s45, v255, 9\n"
"s_lshl_b32 s74, s46, 13\n"
"s_add_u32 s44, s45, s74\n"
"buffer_load_dwordx4 a[0:3], v225, s[28:31], s44 offen offset:0 sc1\n"
"buffer_load_dwordx4 a[4:7], v225, s[28:31], s44 offen offset:1024 sc1\n"
"buffer_load_dwordx4 a[8:11], v225, s[28:31], s44 offen offset:2048 sc1\n"
"buffer_load_dwordx4 a[12:15], v225, s[28:31], s44 offen offset:3072 sc1\n"
"s_add_u32 s44, s44, 4096\n"
"buffer_load_dwordx4 a[16:19], v225, s[28:31], s44 offen offset:0 sc1\n"
"buffer_load_dwordx4 a[20:23], v225, s[28:31], s44 offen offset:1024 sc1\n"
"buffer_load_dwordx4 a[24:27], v225, s[28:31], s44 offen offset:2048 sc1\n"
"buffer_load_dwordx4 a[28:31], v225, s[28:31], s44 offen offset:3072 sc1\n"
"s_sub_u32 s44, s45, s74\n"
"s_add_u32 s44, s44, 57344\n"
"buffer_load_dwordx4 a[32:35], v225, s[28:31], s44 offen offset:0 sc1\n"
"buffer_load_dwordx4 a[36:39], v225, s[28:31], s44 offen offset:1024 sc1\n"
"buffer_load_dwordx4 a[40:43], v225, s[28:31], s44 offen offset:2048 sc1\n"
"buffer_load_dwordx4 a[44:47], v225, s[28:31], s44 offen offset:3072 sc1\n"
"s_add_u32 s44, s44, 4096\n"
"buffer_load_dwordx4 a[48:51], v225, s[28:31], s44 offen offset:0 sc1\n"
"buffer_load_dwordx4 a[52:55], v225, s[28:31], s44 offen offset:1024 sc1\n"
"buffer_load_dwordx4 a[56:59], v225, s[28:31], s44 offen offset:2048 sc1\n"
"buffer_load_dwordx4 a[60:63], v225, s[28:31], s44 offen offset:3072 sc1\n"
"v_rcp_f32 v202, v200\n"
"v_rcp_f32 v203, v201\n"
"v_mov_b32 v208, v176\n"
"v_mov_b32 v209, v200\n"
"v_mov_b32 v210, v180\n"
"v_mov_b32 v211, v201\n"
"s_add_u32 s44, s73, 16384\n"
"buffer_store_dwordx4 v[208:211], v225, s[28:31], s44 offen offset:0 sc1\n"
"s_add_u32 s44, s73, 0\n"
"v_mul_f32 v0, v0, v202\n"
"v_mul_f32 v1, v1, v202\n"
"v_mul_f32 v2, v2, v202\n"
"v_mul_f32 v3, v3, v202\n"
"v_mul_f32 v8, v8, v202\n"
"v_mul_f32 v9, v9, v202\n"
"v_mul_f32 v10, v10, v202\n"
"v_mul_f32 v11, v11, v202\n"
"v_cvt_pk_bf16_f32 v128, v0, v1\n"
"v_cvt_pk_bf16_f32 v129, v2, v3\n"
"v_cvt_pk_bf16_f32 v130, v8, v9\n"
"v_cvt_pk_bf16_f32 v131, v10, v11\n"
"buffer_store_dwordx4 v[128:131], v225, s[28:31], s44 offen offset:0 sc1\n"
"s_add_u32 s44, s73, 1024\n"
"v_mul_f32 v16, v16, v202\n"
"v_mul_f32 v17, v17, v202\n"
"v_mul_f32 v18, v18, v202\n"
"v_mul_f32 v19, v19, v202\n"
"v_mul_f32 v24, v24, v202\n"
"v_mul_f32 v25, v25, v202\n"
"v_mul_f32 v26, v26, v202\n"
"v_mul_f32 v27, v27, v202\n"
"v_cvt_pk_bf16_f32 v132, v16, v17\n"
"v_cvt_pk_bf16_f32 v133, v18, v19\n"
"v_cvt_pk_bf16_f32 v134, v24, v25\n"
"v_cvt_pk_bf16_f32 v135, v26, v27\n"
"buffer_store_dwordx4 v[132:135], v225, s[28:31], s44 offen offset:0 sc1\n"
"s_add_u32 s44, s73, 2048\n"
"v_mul_f32 v32, v32, v202\n"
"v_mul_f32 v33, v33, v202\n"
"v_mul_f32 v34, v34, v202\n"
"v_mul_f32 v35, v35, v202\n"
"v_mul_f32 v40, v40, v202\n"
"v_mul_f32 v41, v41, v202\n"
"v_mul_f32 v42, v42, v202\n"
"v_mul_f32 v43, v43, v202\n"
"v_cvt_pk_bf16_f32 v136, v32, v33\n"
"v_cvt_pk_bf16_f32 v137, v34, v35\n"
"v_cvt_pk_bf16_f32 v138, v40, v41\n"
"v_cvt_pk_bf16_f32 v139, v42, v43\n"
"buffer_store_dwordx4 v[136:139], v225, s[28:31], s44 offen offset:0 sc1\n"
"s_add_u32 s44, s73, 3072\n"
"v_mul_f32 v48, v48, v202\n"
"v_mul_f32 v49, v49, v202\n"
"v_mul_f32 v50, v50, v202\n"
"v_mul_f32 v51, v51, v202\n"
"v_mul_f32 v56, v56, v202\n"
"v_mul_f32 v57, v57, v202\n"
"v_mul_f32 v58, v58, v202\n"
"v_mul_f32 v59, v59, v202\n"
"v_cvt_pk_bf16_f32 v140, v48, v49\n"
"v_cvt_pk_bf16_f32 v141, v50, v51\n"
"v_cvt_pk_bf16_f32 v142, v56, v57\n"
"v_cvt_pk_bf16_f32 v143, v58, v59\n"
"buffer_store_dwordx4 v[140:143], v225, s[28:31], s44 offen offset:0 sc1\n"
"s_add_u32 s44, s73, 4096\n"
"v_mul_f32 v64, v64, v202\n"
"v_mul_f32 v65, v65, v202\n"
"v_mul_f32 v66, v66, v202\n"
"v_mul_f32 v67, v67, v202\n"
"v_mul_f32 v72, v72, v202\n"
"v_mul_f32 v73, v73, v202\n"
"v_mul_f32 v74, v74, v202\n"
"v_mul_f32 v75, v75, v202\n"
"v_cvt_pk_bf16_f32 v144, v64, v65\n"
"v_cvt_pk_bf16_f32 v145, v66, v67\n"
"v_cvt_pk_bf16_f32 v146, v72, v73\n"
"v_cvt_pk_bf16_f32 v147, v74, v75\n"
"buffer_store_dwordx4 v[144:147], v225, s[28:31], s44 offen offset:0 sc1\n"
"s_add_u32 s44, s73, 5120\n"
"v_mul_f32 v80, v80, v202\n"
"v_mul_f32 v81, v81, v202\n"
"v_mul_f32 v82, v82, v202\n"
"v_mul_f32 v83, v83, v202\n"
"v_mul_f32 v88, v88, v202\n"
"v_mul_f32 v89, v89, v202\n"
"v_mul_f32 v90, v90, v202\n"
"v_mul_f32 v91, v91, v202\n"
"v_cvt_pk_bf16_f32 v148, v80, v81\n"
"v_cvt_pk_bf16_f32 v149, v82, v83\n"
"v_cvt_pk_bf16_f32 v150, v88, v89\n"
"v_cvt_pk_bf16_f32 v151, v90, v91\n"
"buffer_store_dwordx4 v[148:151], v225, s[28:31], s44 offen offset:0 sc1\n"
"s_add_u32 s44, s73, 6144\n"
"v_mul_f32 v96, v96, v202\n"
"v_mul_f32 v97, v97, v202\n"
"v_mul_f32 v98, v98, v202\n"
"v_mul_f32 v99, v99, v202\n"
"v_mul_f32 v104, v104, v202\n"
"v_mul_f32 v105, v105, v202\n"
"v_mul_f32 v106, v106, v202\n"
"v_mul_f32 v107, v107, v202\n"
"v_cvt_pk_bf16_f32 v152, v96, v97\n"
"v_cvt_pk_bf16_f32 v153, v98, v99\n"
"v_cvt_pk_bf16_f32 v154, v104, v105\n"
"v_cvt_pk_bf16_f32 v155, v106, v107\n"
"buffer_store_dwordx4 v[152:155], v225, s[28:31], s44 offen offset:0 sc1\n"
"s_add_u32 s44, s73, 7168\n"
"v_mul_f32 v112, v112, v202\n"
"v_mul_f32 v113, v113, v202\n"
"v_mul_f32 v114, v114, v202\n"
"v_mul_f32 v115, v115, v202\n"
"v_mul_f32 v120, v120, v202\n"
"v_mul_f32 v121, v121, v202\n"
"v_mul_f32 v122, v122, v202\n"
"v_mul_f32 v123, v123, v202\n"
"v_cvt_pk_bf16_f32 v156, v112, v113\n"
"v_cvt_pk_bf16_f32 v157, v114, v115\n"
"v_cvt_pk_bf16_f32 v158, v120, v121\n"
"v_cvt_pk_bf16_f32 v159, v122, v123\n"
"buffer_store_dwordx4 v[156:159], v225, s[28:31], s44 offen offset:0 sc1\n"
"s_add_u32 s44, s73, 8192\n"
"v_mul_f32 v4, v4, v203\n"
"v_mul_f32 v5, v5, v203\n"
"v_mul_f32 v6, v6, v203\n"
"v_mul_f32 v7, v7, v203\n"
"v_mul_f32 v12, v12, v203\n"
"v_mul_f32 v13, v13, v203\n"
"v_mul_f32 v14, v14, v203\n"
"v_mul_f32 v15, v15, v203\n"
"v_cvt_pk_bf16_f32 v128, v4, v5\n"
"v_cvt_pk_bf16_f32 v129, v6, v7\n"
"v_cvt_pk_bf16_f32 v130, v12, v13\n"
"v_cvt_pk_bf16_f32 v131, v14, v15\n"
"buffer_store_dwordx4 v[128:131], v225, s[28:31], s44 offen offset:0 sc1\n"
"s_add_u32 s44, s73, 9216\n"
"v_mul_f32 v20, v20, v203\n"
"v_mul_f32 v21, v21, v203\n"
"v_mul_f32 v22, v22, v203\n"
"v_mul_f32 v23, v23, v203\n"
"v_mul_f32 v28, v28, v203\n"
"v_mul_f32 v29, v29, v203\n"
"v_mul_f32 v30, v30, v203\n"
"v_mul_f32 v31, v31, v203\n"
"v_cvt_pk_bf16_f32 v132, v20, v21\n"
"v_cvt_pk_bf16_f32 v133, v22, v23\n"
"v_cvt_pk_bf16_f32 v134, v28, v29\n"
"v_cvt_pk_bf16_f32 v135, v30, v31\n"
"buffer_store_dwordx4 v[132:135], v225, s[28:31], s44 offen offset:0 sc1\n"
"s_add_u32 s44, s73, 10240\n"
"v_mul_f32 v36, v36, v203\n"
"v_mul_f32 v37, v37, v203\n"
"v_mul_f32 v38, v38, v203\n"
"v_mul_f32 v39, v39, v203\n"
"v_mul_f32 v44, v44, v203\n"
"v_mul_f32 v45, v45, v203\n"
"v_mul_f32 v46, v46, v203\n"
"v_mul_f32 v47, v47, v203\n"
"v_cvt_pk_bf16_f32 v136, v36, v37\n"
"v_cvt_pk_bf16_f32 v137, v38, v39\n"
"v_cvt_pk_bf16_f32 v138, v44, v45\n"
"v_cvt_pk_bf16_f32 v139, v46, v47\n"
"buffer_store_dwordx4 v[136:139], v225, s[28:31], s44 offen offset:0 sc1\n"
"s_add_u32 s44, s73, 11264\n"
"v_mul_f32 v52, v52, v203\n"
"v_mul_f32 v53, v53, v203\n"
"v_mul_f32 v54, v54, v203\n"
"v_mul_f32 v55, v55, v203\n"
"v_mul_f32 v60, v60, v203\n"
"v_mul_f32 v61, v61, v203\n"
"v_mul_f32 v62, v62, v203\n"
"v_mul_f32 v63, v63, v203\n"
"v_cvt_pk_bf16_f32 v140, v52, v53\n"
"v_cvt_pk_bf16_f32 v141, v54, v55\n"
"v_cvt_pk_bf16_f32 v142, v60, v61\n"
"v_cvt_pk_bf16_f32 v143, v62, v63\n"
"buffer_store_dwordx4 v[140:143], v225, s[28:31], s44 offen offset:0 sc1\n"
"s_add_u32 s44, s73, 12288\n"
"v_mul_f32 v68, v68, v203\n"
"v_mul_f32 v69, v69, v203\n"
"v_mul_f32 v70, v70, v203\n"
"v_mul_f32 v71, v71, v203\n"
"v_mul_f32 v76, v76, v203\n"
"v_mul_f32 v77, v77, v203\n"
"v_mul_f32 v78, v78, v203\n"
"v_mul_f32 v79, v79, v203\n"
"v_cvt_pk_bf16_f32 v144, v68, v69\n"
"v_cvt_pk_bf16_f32 v145, v70, v71\n"
"v_cvt_pk_bf16_f32 v146, v76, v77\n"
"v_cvt_pk_bf16_f32 v147, v78, v79\n"
"buffer_store_dwordx4 v[144:147], v225, s[28:31], s44 offen offset:0 sc1\n"
"s_add_u32 s44, s73, 13312\n"
"v_mul_f32 v84, v84, v203\n"
"v_mul_f32 v85, v85, v203\n"
"v_mul_f32 v86, v86, v203\n"
"v_mul_f32 v87, v87, v203\n"
"v_mul_f32 v92, v92, v203\n"
"v_mul_f32 v93, v93, v203\n"
"v_mul_f32 v94, v94, v203\n"
"v_mul_f32 v95, v95, v203\n"
"v_cvt_pk_bf16_f32 v148, v84, v85\n"
"v_cvt_pk_bf16_f32 v149, v86, v87\n"
"v_cvt_pk_bf16_f32 v150, v92, v93\n"
"v_cvt_pk_bf16_f32 v151, v94, v95\n"
"buffer_store_dwordx4 v[148:151], v225, s[28:31], s44 offen offset:0 sc1\n"
"s_add_u32 s44, s73, 14336\n"
"v_mul_f32 v100, v100, v203\n"
"v_mul_f32 v101, v101, v203\n"
"v_mul_f32 v102, v102, v203\n"
"v_mul_f32 v103, v103, v203\n"
"v_mul_f32 v108, v108, v203\n"
"v_mul_f32 v109, v109, v203\n"
"v_mul_f32 v110, v110, v203\n"
"v_mul_f32 v111, v111, v203\n"
"v_cvt_pk_bf16_f32 v152, v100, v101\n"
"v_cvt_pk_bf16_f32 v153, v102, v103\n"
"v_cvt_pk_bf16_f32 v154, v108, v109\n"
"v_cvt_pk_bf16_f32 v155, v110, v111\n"
"buffer_store_dwordx4 v[152:155], v225, s[28:31], s44 offen offset:0 sc1\n"
"s_add_u32 s44, s73, 15360\n"
"v_mul_f32 v116, v116, v203\n"
"v_mul_f32 v117, v117, v203\n"
"v_mul_f32 v118, v118, v203\n"
"v_mul_f32 v119, v119, v203\n"
"v_mul_f32 v124, v124, v203\n"
"v_mul_f32 v125, v125, v203\n"
"v_mul_f32 v126, v126, v203\n"
"v_mul_f32 v127, v127, v203\n"
"v_cvt_pk_bf16_f32 v156, v116, v117\n"
"v_cvt_pk_bf16_f32 v157, v118, v119\n"
"v_cvt_pk_bf16_f32 v158, v124, v125\n"
"v_cvt_pk_bf16_f32 v159, v126, v127\n"
"buffer_store_dwordx4 v[156:159], v225, s[28:31], s44 offen offset:0 sc1\n"
"v_mov_b32 v176, 0\n"
"v_mov_b32 v177, 0\n"
"v_mov_b32 v178, 0\n"
"v_mov_b32 v179, 0\n"
"v_mov_b32 v180, 0\n"
"v_mov_b32 v181, 0\n"
"v_mov_b32 v182, 0\n"
"v_mov_b32 v183, 0\n"
"s_nop 7\n"
"v_mfma_f32_32x32x16_bf16 v[0:15], v[176:179], v[176:179], 0\n"
"v_mfma_f32_32x32x16_bf16 v[16:31], v[176:179], v[176:179], 0\n"
"v_mfma_f32_32x32x16_bf16 v[32:47], v[176:179], v[176:179], 0\n"
"v_mfma_f32_32x32x16_bf16 v[48:63], v[176:179], v[176:179], 0\n"
"v_mfma_f32_32x32x16_bf16 v[64:79], v[176:179], v[176:179], 0\n"
"v_mfma_f32_32x32x16_bf16 v[80:95], v[176:179], v[176:179], 0\n"
"v_mfma_f32_32x32x16_bf16 v[96:111], v[176:179], v[176:179], 0\n"
"v_mfma_f32_32x32x16_bf16 v[112:127], v[176:179], v[176:179], 0\n"
"v_mov_b32 v200, 0\n"
"v_mov_b32 v201, 0\n"
"s_mov_b32 s42, 0xbf800000\n"
"s_mov_b32 s43, 0xbf800000\n"
"v_readlane_b32 s59, v255, 26\n"
"s_mov_b32 s70, 0\n"
"s_min_u32 s51, s71, 1\n"
"s_sub_u32 s58, s71, s51\n"
"s_waitcnt vmcnt(17)\n"
"s_nop 1\n"
"s_branch L_at_seg\n"
"L_at_comb:\n"
"s_waitcnt vmcnt(16)\n"
"v_cmp_eq_u32 vcc, s57, v246\n"
"s_lshl_b32 s45, 16, s46\n"
"s_and_b32 s44, vcc_lo, s45\n"
"s_cmp_lg_u32 s44, 0\n"
"s_cbranch_scc1 L_got_p\n"
"v_readlane_b32 s74, v255, 26\n"
"s_mov_b32 s66, 100000\n"
"s_mov_b32 s60, 1\n"
"s_mov_b32 s61, 0\n"
"s_nop 2\n"
"L_poll_p:\n"
"buffer_load_dword v246, v248, s[28:31], s74 offen offset:0 sc1\n"
"s_waitcnt vmcnt(0)\n"
"v_cmp_eq_u32 vcc, s57, v246\n"
"s_nop 0\n"
"s_and_b64 s[62:63], vcc, s[60:61]\n"
"s_cmp_eq_u64 s[62:63], s[60:61]\n"
"s_cbranch_scc1 L_got_p\n"
"s_sleep 4\n"
"s_sub_u32 s66, s66, 1\n"
"s_cbranch_scc0 L_poll_p\n"
"L_got_p:\n"
"s_add_u32 s44, s73, 16384\n"
"buffer_load_dwordx4 v[242:245], v225, s[28:31], s44 offen offset:0 sc1\n"
"s_add_u32 s44, s73, 0\n"
"buffer_load_dwordx4 v[128:131], v225, s[28:31], s44 offen offset:0 sc1\n"
"buffer_load_dwordx4 v[132:135], v225, s[28:31], s44 offen offset:1024 sc1\n"
"buffer_load_dwordx4 v[136:139], v225, s[28:31], s44 offen offset:2048 sc1\n"
"buffer_load_dwordx4 v[140:143], v225, s[28:31], s44 offen offset:3072 sc1\n"
"s_add_u32 s44, s73, 4096\n"
"buffer_load_dwordx4 v[144:147], v225, s[28:31], s44 offen offset:0 sc1\n"
"buffer_load_dwordx4 v[148:151], v225, s[28:31], s44 offen offset:1024 sc1\n"
"buffer_load_dwordx4 v[152:155], v225, s[28:31], s44 offen offset:2048 sc1\n"
"buffer_load_dwordx4 v[156:159], v225, s[28:31], s44 offen offset:3072 sc1\n"
"s_add_u32 s44, s73, 8192\n"
"buffer_load_dwordx4 v[160:163], v225, s[28:31], s44 offen offset:0 sc1\n"
"buffer_load_dwordx4 v[164:167], v225, s[28:31], s44 offen offset:1024 sc1\n"
"buffer_load_dwordx4 v[168:171], v225, s[28:31], s44 offen offset:2048 sc1\n"
"buffer_load_dwordx4 v[172:175], v225, s[28:31], s44 offen offset:3072 sc1\n"
"s_add_u32 s44, s73, 12288\n"
"buffer_load_dwordx4 v[226:229], v225, s[28:31], s44 offen offset:0 sc1\n"
"buffer_load_dwordx4 v[230:233], v225, s[28:31], s44 offen offset:1024 sc1\n"
"buffer_load_dwordx4 v[234:237], v225, s[28:31], s44 offen offset:2048 sc1\n"
"buffer_load_dwordx4 v[238:241], v225, s[28:31], s44 offen offset:3072 sc1\n"
"s_waitcnt vmcnt(16)\n"
"v_min_f32 v206, v176, v242\n"
"v_sub_f32 v207, v206, v176\n"
"v_sub_f32 v208, v206, v242\n"
"v_exp_f32 v207, v207\n"
"v_exp_f32 v208, v208\n"
"s_nop 0\n"
"v_mul_f32 v208, v208, v243\n"
"v_fma_f32 v206, v200, v207, v208\n"
"v_rcp_f32 v206, v206\n"
"s_nop 0\n"
"v_mul_f32 v202, v207, v206\n"
"v_mul_f32 v204, v208, v206\n"
"v_min_f32 v206, v180, v244\n"
"v_sub_f32 v207, v206, v180\n"
"v_sub_f32 v208, v206, v244\n"
"v_exp_f32 v207, v207\n"
"v_exp_f32 v208, v208\n"
"s_nop 0\n"
"v_mul_f32 v208, v208, v245\n"
"v_fma_f32 v206, v201, v207, v208\n"
"v_rcp_f32 v206, v206\n"
"s_nop 0\n"
"v_mul_f32 v203, v207, v206\n"
"v_mul_f32 v205, v208, v206\n"
"s_mov_b32 s66, 0xffff0000\n"
"s_waitcnt vmcnt(15)\n"
"v_lshlrev_b32 v184, 16, v128\n"
"v_and_b32 v185, s66, v128\n"
"v_mul_f32 v0, v0, v202\n"
"v_mul_f32 v1, v1, v202\n"
"v_fmac_f32 v0, v184, v204\n"
"v_fmac_f32 v1, v185, v204\n"
"v_lshlrev_b32 v186, 16, v129\n"
"v_and_b32 v187, s66, v129\n"
"v_mul_f32 v2, v2, v202\n"
"v_mul_f32 v3, v3, v202\n"
"v_fmac_f32 v2, v186, v204\n"
"v_fmac_f32 v3, v187, v204\n"
"v_lshlrev_b32 v188, 16, v130\n"
"v_and_b32 v189, s66, v130\n"
"v_mul_f32 v8, v8, v202\n"
"v_mul_f32 v9, v9, v202\n"
"v_fmac_f32 v8, v188, v204\n"
"v_fmac_f32 v9, v189, v204\n"
"v_lshlrev_b32 v190, 16, v131\n"
"v_and_b32 v191, s66, v131\n"
"v_mul_f32 v10, v10, v202\n"
"v_mul_f32 v11, v11, v202\n"
"v_fmac_f32 v10, v190, v204\n"
"v_fmac_f32 v11, v191, v204\n"
"v_cvt_pk_bf16_f32 v0, v0, v1\n"
"v_cvt_pk_bf16_f32 v1, v2, v3\n"
"v_cvt_pk_bf16_f32 v2, v8, v9\n"
"v_cvt_pk_bf16_f32 v3, v10, v11\n"
"s_waitcnt vmcnt(14)\n"
"v_lshlrev_b32 v184, 16, v132\n"
"v_and_b32 v185, s66, v132\n"
"v_mul_f32 v16, v16, v202\n"
"v_mul_f32 v17, v17, v202\n"
"v_fmac_f32 v16, v184, v204\n"
"v_fmac_f32 v17, v185, v204\n"
"v_lshlrev_b32 v186, 16, v133\n"
"v_and_b32 v187, s66, v133\n"
"v_mul_f32 v18, v18, v202\n"
"v_mul_f32 v19, v19, v202\n"
"v_fmac_f32 v18, v186, v204\n"
"v_fmac_f32 v19, v187, v204\n"
"v_lshlrev_b32 v188, 16, v134\n"
"v_and_b32 v189, s66, v134\n"
"v_mul_f32 v24, v24, v202\n"
"v_mul_f32 v25, v25, v202\n"
"v_fmac_f32 v24, v188, v204\n"
"v_fmac_f32 v25, v189, v204\n"
"v_lshlrev_b32 v190, 16, v135\n"
"v_and_b32 v191, s66, v135\n"
"v_mul_f32 v26, v26, v202\n"
"v_mul_f32 v27, v27, v202\n"
"v_fmac_f32 v26, v190, v204\n"
"v_fmac_f32 v27, v191, v204\n"
"v_cvt_pk_bf16_f32 v16, v16, v17\n"
"v_cvt_pk_bf16_f32 v17, v18, v19\n"
"v_cvt_pk_bf16_f32 v18, v24, v25\n"
"v_cvt_pk_bf16_f32 v19, v26, v27\n"
"s_waitcnt vmcnt(13)\n"
"v_lshlrev_b32 v184, 16, v136\n"
"v_and_b32 v185, s66, v136\n"
"v_mul_f32 v32, v32, v202\n"
"v_mul_f32 v33, v33, v202\n"
"v_fmac_f32 v32, v184, v204\n"
"v_fmac_f32 v33, v185, v204\n"
"v_lshlrev_b32 v186, 16, v137\n"
"v_and_b32 v187, s66, v137\n"
"v_mul_f32 v34, v34, v202\n"
"v_mul_f32 v35, v35, v202\n"
"v_fmac_f32 v34, v186, v204\n"
"v_fmac_f32 v35, v187, v204\n"
"v_lshlrev_b32 v188, 16, v138\n"
"v_and_b32 v189, s66, v138\n"
"v_mul_f32 v40, v40, v202\n"
"v_mul_f32 v41, v41, v202\n"
"v_fmac_f32 v40, v188, v204\n"
"v_fmac_f32 v41, v189, v204\n"
"v_lshlrev_b32 v190, 16, v139\n"
"v_and_b32 v191, s66, v139\n"
"v_mul_f32 v42, v42, v202\n"
"v_mul_f32 v43, v43, v202\n"
"v_fmac_f32 v42, v190, v204\n"
"v_fmac_f32 v43, v191, v204\n"
"v_cvt_pk_bf16_f32 v32, v32, v33\n"
"v_cvt_pk_bf16_f32 v33, v34, v35\n"
"v_cvt_pk_bf16_f32 v34, v40, v41\n"
"v_cvt_pk_bf16_f32 v35, v42, v43\n"
"s_waitcnt vmcnt(12)\n"
"v_lshlrev_b32 v184, 16, v140\n"
"v_and_b32 v185, s66, v140\n"
"v_mul_f32 v48, v48, v202\n"
"v_mul_f32 v49, v49, v202\n"
"v_fmac_f32 v48, v184, v204\n"
"v_fmac_f32 v49, v185, v204\n"
"v_lshlrev_b32 v186, 16, v141\n"
"v_and_b32 v187, s66, v141\n"
"v_mul_f32 v50, v50, v202\n"
"v_mul_f32 v51, v51, v202\n"
"v_fmac_f32 v50, v186, v204\n"
"v_fmac_f32 v51, v187, v204\n"
"v_lshlrev_b32 v188, 16, v142\n"
"v_and_b32 v189, s66, v142\n"
"v_mul_f32 v56, v56, v202\n"
"v_mul_f32 v57, v57, v202\n"
"v_fmac_f32 v56, v188, v204\n"
"v_fmac_f32 v57, v189, v204\n"
"v_lshlrev_b32 v190, 16, v143\n"
"v_and_b32 v191, s66, v143\n"
"v_mul_f32 v58, v58, v202\n"
"v_mul_f32 v59, v59, v202\n"
"v_fmac_f32 v58, v190, v204\n"
"v_fmac_f32 v59, v191, v204\n"
"v_cvt_pk_bf16_f32 v48, v48, v49\n"
"v_cvt_pk_bf16_f32 v49, v50, v51\n"
"v_cvt_pk_bf16_f32 v50, v56, v57\n"
"v_cvt_pk_bf16_f32 v51, v58, v59\n"
"s_waitcnt vmcnt(11)\n"
"v_lshlrev_b32 v184, 16, v144\n"
"v_and_b32 v185, s66, v144\n"
"v_mul_f32 v64, v64, v202\n"
"v_mul_f32 v65, v65, v202\n"
"v_fmac_f32 v64, v184, v204\n"
"v_fmac_f32 v65, v185, v204\n"
"v_lshlrev_b32 v186, 16, v145\n"
"v_and_b32 v187, s66, v145\n"
"v_mul_f32 v66, v66, v202\n"
"v_mul_f32 v67, v67, v202\n"
"v_fmac_f32 v66, v186, v204\n"
"v_fmac_f32 v67, v187, v204\n"
"v_lshlrev_b32 v188, 16, v146\n"
"v_and_b32 v189, s66, v146\n"
"v_mul_f32 v72, v72, v202\n"
"v_mul_f32 v73, v73, v202\n"
"v_fmac_f32 v72, v188, v204\n"
"v_fmac_f32 v73, v189, v204\n"
"v_lshlrev_b32 v190, 16, v147\n"
"v_and_b32 v191, s66, v147\n"
"v_mul_f32 v74, v74, v202\n"
"v_mul_f32 v75, v75, v202\n"
"v_fmac_f32 v74, v190, v204\n"
"v_fmac_f32 v75, v191, v204\n"
"v_cvt_pk_bf16_f32 v64, v64, v65\n"
"v_cvt_pk_bf16_f32 v65, v66, v67\n"
"v_cvt_pk_bf16_f32 v66, v72, v73\n"
"v_cvt_pk_bf16_f32 v67, v74, v75\n"
"s_waitcnt vmcnt(10)\n"
"v_lshlrev_b32 v184, 16, v148\n"
"v_and_b32 v185, s66, v148\n"
"v_mul_f32 v80, v80, v202\n"
"v_mul_f32 v81, v81, v202\n"
"v_fmac_f32 v80, v184, v204\n"
"v_fmac_f32 v81, v185, v204\n"
"v_lshlrev_b32 v186, 16, v149\n"
"v_and_b32 v187, s66, v149\n"
"v_mul_f32 v82, v82, v202\n"
"v_mul_f32 v83, v83, v202\n"
"v_fmac_f32 v82, v186, v204\n"
"v_fmac_f32 v83, v187, v204\n"
"v_lshlrev_b32 v188, 16, v150\n"
"v_and_b32 v189, s66, v150\n"
"v_mul_f32 v88, v88, v202\n"
"v_mul_f32 v89, v89, v202\n"
"v_fmac_f32 v88, v188, v204\n"
"v_fmac_f32 v89, v189, v204\n"
"v_lshlrev_b32 v190, 16, v151\n"
"v_and_b32 v191, s66, v151\n"
"v_mul_f32 v90, v90, v202\n"
"v_mul_f32 v91, v91, v202\n"
"v_fmac_f32 v90, v190, v204\n"
"v_fmac_f32 v91, v191, v204\n"
"v_cvt_pk_bf16_f32 v80, v80, v81\n"
"v_cvt_pk_bf16_f32 v81, v82, v83\n"
"v_cvt_pk_bf16_f32 v82, v88, v89\n"
"v_cvt_pk_bf16_f32 v83, v90, v91\n"
"s_waitcnt vmcnt(9)\n"
"v_lshlrev_b32 v184, 16, v152\n"
"v_and_b32 v185, s66, v152\n"
"v_mul_f32 v96, v96, v202\n"
"v_mul_f32 v97, v97, v202\n"
"v_fmac_f32 v96, v184, v204\n"
"v_fmac_f32 v97, v185, v204\n"
"v_lshlrev_b32 v186, 16, v153\n"
"v_and_b32 v187, s66, v153\n"
"v_mul_f32 v98, v98, v202\n"
"v_mul_f32 v99, v99, v202\n"
"v_fmac_f32 v98, v186, v204\n"
"v_fmac_f32 v99, v187, v204\n"
"v_lshlrev_b32 v188, 16, v154\n"
"v_and_b32 v189, s66, v154\n"
"v_mul_f32 v104, v104, v202\n"
"v_mul_f32 v105, v105, v202\n"
"v_fmac_f32 v104, v188, v204\n"
"v_fmac_f32 v105, v189, v204\n"
"v_lshlrev_b32 v190, 16, v155\n"
"v_and_b32 v191, s66, v155\n"
"v_mul_f32 v106, v106, v202\n"
"v_mul_f32 v107, v107, v202\n"
"v_fmac_f32 v106, v190, v204\n"
"v_fmac_f32 v107, v191, v204\n"
"v_cvt_pk_bf16_f32 v96, v96, v97\n"
"v_cvt_pk_bf16_f32 v97, v98, v99\n"
"v_cvt_pk_bf16_f32 v98, v104, v105\n"
"v_cvt_pk_bf16_f32 v99, v106, v107\n"
"s_waitcnt vmcnt(8)\n"
"v_lshlrev_b32 v184, 16, v156\n"
"v_and_b32 v185, s66, v156\n"
"v_mul_f32 v112, v112, v202\n"
"v_mul_f32 v113, v113, v202\n"
"v_fmac_f32 v112, v184, v204\n"
"v_fmac_f32 v113, v185, v204\n"
"v_lshlrev_b32 v186, 16, v157\n"
"v_and_b32 v187, s66, v157\n"
"v_mul_f32 v114, v114, v202\n"
"v_mul_f32 v115, v115, v202\n"
"v_fmac_f32 v114, v186, v204\n"
"v_fmac_f32 v115, v187, v204\n"
"v_lshlrev_b32 v188, 16, v158\n"
"v_and_b32 v189, s66, v158\n"
"v_mul_f32 v120, v120, v202\n"
"v_mul_f32 v121, v121, v202\n"
"v_fmac_f32 v120, v188, v204\n"
"v_fmac_f32 v121, v189, v204\n"
"v_lshlrev_b32 v190, 16, v159\n"
"v_and_b32 v191, s66, v159\n"
"v_mul_f32 v122, v122, v202\n"
"v_mul_f32 v123, v123, v202\n"
"v_fmac_f32 v122, v190, v204\n"
"v_fmac_f32 v123, v191, v204\n"
"v_cvt_pk_bf16_f32 v112, v112, v113\n"
"v_cvt_pk_bf16_f32 v113, v114, v115\n"
"v_cvt_pk_bf16_f32 v114, v120, v121\n"
"v_cvt_pk_bf16_f32 v115, v122, v123\n"
"s_waitcnt vmcnt(7)\n"
"v_lshlrev_b32 v184, 16, v160\n"
"v_and_b32 v185, s66, v160\n"
"v_mul_f32 v4, v4, v203\n"
"v_mul_f32 v5, v5, v203\n"
"v_fmac_f32 v4, v184, v205\n"
"v_fmac_f32 v5, v185, v205\n"
"v_lshlrev_b32 v186, 16, v161\n"
"v_and_b32 v187, s66, v161\n"
"v_mul_f32 v6, v6, v203\n"
"v_mul_f32 v7, v7, v203\n"
"v_fmac_f32 v6, v186, v205\n"
"v_fmac_f32 v7, v187, v205\n"
"v_lshlrev_b32 v188, 16, v162\n"
"v_and_b32 v189, s66, v162\n"
"v_mul_f32 v12, v12, v203\n"
"v_mul_f32 v13, v13, v203\n"
"v_fmac_f32 v12, v188, v205\n"
"v_fmac_f32 v13, v189, v205\n"
"v_lshlrev_b32 v190, 16, v163\n"
"v_and_b32 v191, s66, v163\n"
"v_mul_f32 v14, v14, v203\n"
"v_mul_f32 v15, v15, v203\n"
"v_fmac_f32 v14, v190, v205\n"
"v_fmac_f32 v15, v191, v205\n"
"v_cvt_pk_bf16_f32 v4, v4, v5\n"
"v_cvt_pk_bf16_f32 v5, v6, v7\n"
"v_cvt_pk_bf16_f32 v6, v12, v13\n"
"v_cvt_pk_bf16_f32 v7, v14, v15\n"
"s_waitcnt vmcnt(6)\n"
"v_lshlrev_b32 v184, 16, v164\n"
"v_and_b32 v185, s66, v164\n"
"v_mul_f32 v20, v20, v203\n"
"v_mul_f32 v21, v21, v203\n"
"v_fmac_f32 v20, v184, v205\n"
"v_fmac_f32 v21, v185, v205\n"
"v_lshlrev_b32 v186, 16, v165\n"
"v_and_b32 v187, s66, v165\n"
"v_mul_f32 v22, v22, v203\n"
"v_mul_f32 v23, v23, v203\n"
"v_fmac_f32 v22, v186, v205\n"
"v_fmac_f32 v23, v187, v205\n"
"v_lshlrev_b32 v188, 16, v166\n"
"v_and_b32 v189, s66, v166\n"
"v_mul_f32 v28, v28, v203\n"
"v_mul_f32 v29, v29, v203\n"
"v_fmac_f32 v28, v188, v205\n"
"v_fmac_f32 v29, v189, v205\n"
"v_lshlrev_b32 v190, 16, v167\n"
"v_and_b32 v191, s66, v167\n"
"v_mul_f32 v30, v30, v203\n"
"v_mul_f32 v31, v31, v203\n"
"v_fmac_f32 v30, v190, v205\n"
"v_fmac_f32 v31, v191, v205\n"
"v_cvt_pk_bf16_f32 v20, v20, v21\n"
"v_cvt_pk_bf16_f32 v21, v22, v23\n"
"v_cvt_pk_bf16_f32 v22, v28, v29\n"
"v_cvt_pk_bf16_f32 v23, v30, v31\n"
"s_waitcnt vmcnt(5)\n"
"v_lshlrev_b32 v184, 16, v168\n"
"v_and_b32 v185, s66, v168\n"
"v_mul_f32 v36, v36, v203\n"
"v_mul_f32 v37, v37, v203\n"
"v_fmac_f32 v36, v184, v205\n"
"v_fmac_f32 v37, v185, v205\n"
"v_lshlrev_b32 v186, 16, v169\n"
"v_and_b32 v187, s66, v169\n"
"v_mul_f32 v38, v38, v203\n"
"v_mul_f32 v39, v39, v203\n"
"v_fmac_f32 v38, v186, v205\n"
"v_fmac_f32 v39, v187, v205\n"
"v_lshlrev_b32 v188, 16, v170\n"
"v_and_b32 v189, s66, v170\n"
"v_mul_f32 v44, v44, v203\n"
"v_mul_f32 v45, v45, v203\n"
"v_fmac_f32 v44, v188, v205\n"
"v_fmac_f32 v45, v189, v205\n"
"v_lshlrev_b32 v190, 16, v171\n"
"v_and_b32 v191, s66, v171\n"
"v_mul_f32 v46, v46, v203\n"
"v_mul_f32 v47, v47, v203\n"
"v_fmac_f32 v46, v190, v205\n"
"v_fmac_f32 v47, v191, v205\n"
"v_cvt_pk_bf16_f32 v36, v36, v37\n"
"v_cvt_pk_bf16_f32 v37, v38, v39\n"
"v_cvt_pk_bf16_f32 v38, v44, v45\n"
"v_cvt_pk_bf16_f32 v39, v46, v47\n"
"s_waitcnt vmcnt(4)\n"
"v_lshlrev_b32 v184, 16, v172\n"
"v_and_b32 v185, s66, v172\n"
"v_mul_f32 v52, v52, v203\n"
"v_mul_f32 v53, v53, v203\n"
"v_fmac_f32 v52, v184, v205\n"
"v_fmac_f32 v53, v185, v205\n"
"v_lshlrev_b32 v186, 16, v173\n"
"v_and_b32 v187, s66, v173\n"
"v_mul_f32 v54, v54, v203\n"
"v_mul_f32 v55, v55, v203\n"
"v_fmac_f32 v54, v186, v205\n"
"v_fmac_f32 v55, v187, v205\n"
"v_lshlrev_b32 v188, 16, v174\n"
"v_and_b32 v189, s66, v174\n"
"v_mul_f32 v60, v60, v203\n"
"v_mul_f32 v61, v61, v203\n"
"v_fmac_f32 v60, v188, v205\n"
"v_fmac_f32 v61, v189, v205\n"
"v_lshlrev_b32 v190, 16, v175\n"
"v_and_b32 v191, s66, v175\n"
"v_mul_f32 v62, v62, v203\n"
"v_mul_f32 v63, v63, v203\n"
"v_fmac_f32 v62, v190, v205\n"
"v_fmac_f32 v63, v191, v205\n"
"v_cvt_pk_bf16_f32 v52, v52, v53\n"
"v_cvt_pk_bf16_f32 v53, v54, v55\n"
"v_cvt_pk_bf16_f32 v54, v60, v61\n"
"v_cvt_pk_bf16_f32 v55, v62, v63\n"
"s_waitcnt vmcnt(3)\n"
"v_lshlrev_b32 v184, 16, v226\n"
"v_and_b32 v185, s66, v226\n"
"v_mul_f32 v68, v68, v203\n"
"v_mul_f32 v69, v69, v203\n"
"v_fmac_f32 v68, v184, v205\n"
"v_fmac_f32 v69, v185, v205\n"
"v_lshlrev_b32 v186, 16, v227\n"
"v_and_b32 v187, s66, v227\n"
"v_mul_f32 v70, v70, v203\n"
"v_mul_f32 v71, v71, v203\n"
"v_fmac_f32 v70, v186, v205\n"
"v_fmac_f32 v71, v187, v205\n"
"v_lshlrev_b32 v188, 16, v228\n"
"v_and_b32 v189, s66, v228\n"
"v_mul_f32 v76, v76, v203\n"
"v_mul_f32 v77, v77, v203\n"
"v_fmac_f32 v76, v188, v205\n"
"v_fmac_f32 v77, v189, v205\n"
"v_lshlrev_b32 v190, 16, v229\n"
"v_and_b32 v191, s66, v229\n"
"v_mul_f32 v78, v78, v203\n"
"v_mul_f32 v79, v79, v203\n"
"v_fmac_f32 v78, v190, v205\n"
"v_fmac_f32 v79, v191, v205\n"
"v_cvt_pk_bf16_f32 v68, v68, v69\n"
"v_cvt_pk_bf16_f32 v69, v70, v71\n"
"v_cvt_pk_bf16_f32 v70, v76, v77\n"
"v_cvt_pk_bf16_f32 v71, v78, v79\n"
"s_waitcnt vmcnt(2)\n"
"v_lshlrev_b32 v184, 16, v230\n"
"v_and_b32 v185, s66, v230\n"
"v_mul_f32 v84, v84, v203\n"
"v_mul_f32 v85, v85, v203\n"
"v_fmac_f32 v84, v184, v205\n"
"v_fmac_f32 v85, v185, v205\n"
"v_lshlrev_b32 v186, 16, v231\n"
"v_and_b32 v187, s66, v231\n"
"v_mul_f32 v86, v86, v203\n"
"v_mul_f32 v87, v87, v203\n"
"v_fmac_f32 v86, v186, v205\n"
"v_fmac_f32 v87, v187, v205\n"
"v_lshlrev_b32 v188, 16, v232\n"
"v_and_b32 v189, s66, v232\n"
"v_mul_f32 v92, v92, v203\n"
"v_mul_f32 v93, v93, v203\n"
"v_fmac_f32 v92, v188, v205\n"
"v_fmac_f32 v93, v189, v205\n"
"v_lshlrev_b32 v190, 16, v233\n"
"v_and_b32 v191, s66, v233\n"
"v_mul_f32 v94, v94, v203\n"
"v_mul_f32 v95, v95, v203\n"
"v_fmac_f32 v94, v190, v205\n"
"v_fmac_f32 v95, v191, v205\n"
"v_cvt_pk_bf16_f32 v84, v84, v85\n"
"v_cvt_pk_bf16_f32 v85, v86, v87\n"
"v_cvt_pk_bf16_f32 v86, v92, v93\n"
"v_cvt_pk_bf16_f32 v87, v94, v95\n"
"s_waitcnt vmcnt(1)\n"
"v_lshlrev_b32 v184, 16, v234\n"
"v_and_b32 v185, s66, v234\n"
"v_mul_f32 v100, v100, v203\n"
"v_mul_f32 v101, v101, v203\n"
"v_fmac_f32 v100, v184, v205\n"
"v_fmac_f32 v101, v185, v205\n"
"v_lshlrev_b32 v186, 16, v235\n"
"v_and_b32 v187, s66, v235\n"
"v_mul_f32 v102, v102, v203\n"
"v_mul_f32 v103, v103, v203\n"
"v_fmac_f32 v102, v186, v205\n"
"v_fmac_f32 v103, v187, v205\n"
"v_lshlrev_b32 v188, 16, v236\n"
"v_and_b32 v189, s66, v236\n"
"v_mul_f32 v108, v108, v203\n"
"v_mul_f32 v109, v109, v203\n"
"v_fmac_f32 v108, v188, v205\n"
"v_fmac_f32 v109, v189, v205\n"
"v_lshlrev_b32 v190, 16, v237\n"
"v_and_b32 v191, s66, v237\n"
"v_mul_f32 v110, v110, v203\n"
"v_mul_f32 v111, v111, v203\n"
"v_fmac_f32 v110, v190, v205\n"
"v_fmac_f32 v111, v191, v205\n"
"v_cvt_pk_bf16_f32 v100, v100, v101\n"
"v_cvt_pk_bf16_f32 v101, v102, v103\n"
"v_cvt_pk_bf16_f32 v102, v108, v109\n"
"v_cvt_pk_bf16_f32 v103, v110, v111\n"
"s_waitcnt vmcnt(0)\n"
"v_lshlrev_b32 v184, 16, v238\n"
"v_and_b32 v185, s66, v238\n"
"v_mul_f32 v116, v116, v203\n"
"v_mul_f32 v117, v117, v203\n"
"v_fmac_f32 v116, v184, v205\n"
"v_fmac_f32 v117, v185, v205\n"
"v_lshlrev_b32 v186, 16, v239\n"
"v_and_b32 v187, s66, v239\n"
"v_mul_f32 v118, v118, v203\n"
"v_mul_f32 v119, v119, v203\n"
"v_fmac_f32 v118, v186, v205\n"
"v_fmac_f32 v119, v187, v205\n"
"v_lshlrev_b32 v188, 16, v240\n"
"v_and_b32 v189, s66, v240\n"
"v_mul_f32 v124, v124, v203\n"
"v_mul_f32 v125, v125, v203\n"
"v_fmac_f32 v124, v188, v205\n"
"v_fmac_f32 v125, v189, v205\n"
"v_lshlrev_b32 v190, 16, v241\n"
"v_and_b32 v191, s66, v241\n"
"v_mul_f32 v126, v126, v203\n"
"v_mul_f32 v127, v127, v203\n"
"v_fmac_f32 v126, v190, v205\n"
"v_fmac_f32 v127, v191, v205\n"
"v_cvt_pk_bf16_f32 v116, v116, v117\n"
"v_cvt_pk_bf16_f32 v117, v118, v119\n"
"v_cvt_pk_bf16_f32 v118, v124, v125\n"
"v_cvt_pk_bf16_f32 v119, v126, v127\n"
"s_branch L_op\n"
"L_at_ef:\n"
"v_rcp_f32 v202, v200\n"
"v_rcp_f32 v203, v201\n"
"v_mul_f32 v0, v0, v202\n"
"v_mul_f32 v1, v1, v202\n"
"v_mul_f32 v2, v2, v202\n"
"v_mul_f32 v3, v3, v202\n"
"v_mul_f32 v8, v8, v202\n"
"v_mul_f32 v9, v9, v202\n"
"v_mul_f32 v10, v10, v202\n"
"v_mul_f32 v11, v11, v202\n"
"v_cvt_pk_bf16_f32 v0, v0, v1\n"
"v_cvt_pk_bf16_f32 v1, v2, v3\n"
"v_cvt_pk_bf16_f32 v2, v8, v9\n"
"v_cvt_pk_bf16_f32 v3, v10, v11\n"
"v_mul_f32 v16, v16, v202\n"
"v_mul_f32 v17, v17, v202\n"
"v_mul_f32 v18, v18, v202\n"
"v_mul_f32 v19, v19, v202\n"
"v_mul_f32 v24, v24, v202\n"
"v_mul_f32 v25, v25, v202\n"
"v_mul_f32 v26, v26, v202\n"
"v_mul_f32 v27, v27, v202\n"
"v_cvt_pk_bf16_f32 v16, v16, v17\n"
"v_cvt_pk_bf16_f32 v17, v18, v19\n"
"v_cvt_pk_bf16_f32 v18, v24, v25\n"
"v_cvt_pk_bf16_f32 v19, v26, v27\n"
"v_mul_f32 v32, v32, v202\n"
"v_mul_f32 v33, v33, v202\n"
"v_mul_f32 v34, v34, v202\n"
"v_mul_f32 v35, v35, v202\n"
"v_mul_f32 v40, v40, v202\n"
"v_mul_f32 v41, v41, v202\n"
"v_mul_f32 v42, v42, v202\n"
"v_mul_f32 v43, v43, v202\n"
"v_cvt_pk_bf16_f32 v32, v32, v33\n"
"v_cvt_pk_bf16_f32 v33, v34, v35\n"
"v_cvt_pk_bf16_f32 v34, v40, v41\n"
"v_cvt_pk_bf16_f32 v35, v42, v43\n"
"v_mul_f32 v48, v48, v202\n"
"v_mul_f32 v49, v49, v202\n"
"v_mul_f32 v50, v50, v202\n"
"v_mul_f32 v51, v51, v202\n"
"v_mul_f32 v56, v56, v202\n"
"v_mul_f32 v57, v57, v202\n"
"v_mul_f32 v58, v58, v202\n"
"v_mul_f32 v59, v59, v202\n"
"v_cvt_pk_bf16_f32 v48, v48, v49\n"
"v_cvt_pk_bf16_f32 v49, v50, v51\n"
"v_cvt_pk_bf16_f32 v50, v56, v57\n"
"v_cvt_pk_bf16_f32 v51, v58, v59\n"
"v_mul_f32 v64, v64, v202\n"
"v_mul_f32 v65, v65, v202\n"
"v_mul_f32 v66, v66, v202\n"
"v_mul_f32 v67, v67, v202\n"
"v_mul_f32 v72, v72, v202\n"
"v_mul_f32 v73, v73, v202\n"
"v_mul_f32 v74, v74, v202\n"
"v_mul_f32 v75, v75, v202\n"
"v_cvt_pk_bf16_f32 v64, v64, v65\n"
"v_cvt_pk_bf16_f32 v65, v66, v67\n"
"v_cvt_pk_bf16_f32 v66, v72, v73\n"
"v_cvt_pk_bf16_f32 v67, v74, v75\n"
"v_mul_f32 v80, v80, v202\n"
"v_mul_f32 v81, v81, v202\n"
"v_mul_f32 v82, v82, v202\n"
"v_mul_f32 v83, v83, v202\n"
"v_mul_f32 v88, v88, v202\n"
"v_mul_f32 v89, v89, v202\n"
"v_mul_f32 v90, v90, v202\n"
"v_mul_f32 v91, v91, v202\n"
"v_cvt_pk_bf16_f32 v80, v80, v81\n"
"v_cvt_pk_bf16_f32 v81, v82, v83\n"
"v_cvt_pk_bf16_f32 v82, v88, v89\n"
"v_cvt_pk_bf16_f32 v83, v90, v91\n"
"v_mul_f32 v96, v96, v202\n"
"v_mul_f32 v97, v97, v202\n"
"v_mul_f32 v98, v98, v202\n"
"v_mul_f32 v99, v99, v202\n"
"v_mul_f32 v104, v104, v202\n"
"v_mul_f32 v105, v105, v202\n"
"v_mul_f32 v106, v106, v202\n"
"v_mul_f32 v107, v107, v202\n"
"v_cvt_pk_bf16_f32 v96, v96, v97\n"
"v_cvt_pk_bf16_f32 v97, v98, v99\n"
"v_cvt_pk_bf16_f32 v98, v104, v105\n"
"v_cvt_pk_bf16_f32 v99, v106, v107\n"
"v_mul_f32 v112, v112, v202\n"
"v_mul_f32 v113, v113, v202\n"
"v_mul_f32 v114, v114, v202\n"
"v_mul_f32 v115, v115, v202\n"
"v_mul_f32 v120, v120, v202\n"
"v_mul_f32 v121, v121, v202\n"
"v_mul_f32 v122, v122, v202\n"
"v_mul_f32 v123, v123, v202\n"
"v_cvt_pk_bf16_f32 v112, v112, v113\n"
"v_cvt_pk_bf16_f32 v113, v114, v115\n"
"v_cvt_pk_bf16_f32 v114, v120, v121\n"
"v_cvt_pk_bf16_f32 v115, v122, v123\n"
"v_mul_f32 v4, v4, v203\n"
"v_mul_f32 v5, v5, v203\n"
"v_mul_f32 v6, v6, v203\n"
"v_mul_f32 v7, v7, v203\n"
"v_mul_f32 v12, v12, v203\n"
"v_mul_f32 v13, v13, v203\n"
"v_mul_f32 v14, v14, v203\n"
"v_mul_f32 v15, v15, v203\n"
"v_cvt_pk_bf16_f32 v4, v4, v5\n"
"v_cvt_pk_bf16_f32 v5, v6, v7\n"
"v_cvt_pk_bf16_f32 v6, v12, v13\n"
"v_cvt_pk_bf16_f32 v7, v14, v15\n"
"v_mul_f32 v20, v20, v203\n"
"v_mul_f32 v21, v21, v203\n"
"v_mul_f32 v22, v22, v203\n"
"v_mul_f32 v23, v23, v203\n"
"v_mul_f32 v28, v28, v203\n"
"v_mul_f32 v29, v29, v203\n"
"v_mul_f32 v30, v30, v203\n"
"v_mul_f32 v31, v31, v203\n"
"v_cvt_pk_bf16_f32 v20, v20, v21\n"
"v_cvt_pk_bf16_f32 v21, v22, v23\n"
"v_cvt_pk_bf16_f32 v22, v28, v29\n"
"v_cvt_pk_bf16_f32 v23, v30, v31\n"
"v_mul_f32 v36, v36, v203\n"
"v_mul_f32 v37, v37, v203\n"
"v_mul_f32 v38, v38, v203\n"
"v_mul_f32 v39, v39, v203\n"
"v_mul_f32 v44, v44, v203\n"
"v_mul_f32 v45, v45, v203\n"
"v_mul_f32 v46, v46, v203\n"
"v_mul_f32 v47, v47, v203\n"
"v_cvt_pk_bf16_f32 v36, v36, v37\n"
"v_cvt_pk_bf16_f32 v37, v38, v39\n"
"v_cvt_pk_bf16_f32 v38, v44, v45\n"
"v_cvt_pk_bf16_f32 v39, v46, v47\n"
"v_mul_f32 v52, v52, v203\n"
"v_mul_f32 v53, v53, v203\n"
"v_mul_f32 v54, v54, v203\n"
"v_mul_f32 v55, v55, v203\n"
"v_mul_f32 v60, v60, v203\n"
"v_mul_f32 v61, v61, v203\n"
"v_mul_f32 v62, v62, v203\n"
"v_mul_f32 v63, v63, v203\n"
"v_cvt_pk_bf16_f32 v52, v52, v53\n"
"v_cvt_pk_bf16_f32 v53, v54, v55\n"
"v_cvt_pk_bf16_f32 v54, v60, v61\n"
"v_cvt_pk_bf16_f32 v55, v62, v63\n"
"v_mul_f32 v68, v68, v203\n"
"v_mul_f32 v69, v69, v203\n"
"v_mul_f32 v70, v70, v203\n"
"v_mul_f32 v71, v71, v203\n"
"v_mul_f32 v76, v76, v203\n"
"v_mul_f32 v77, v77, v203\n"
"v_mul_f32 v78, v78, v203\n"
"v_mul_f32 v79, v79, v203\n"
"v_cvt_pk_bf16_f32 v68, v68, v69\n"
"v_cvt_pk_bf16_f32 v69, v70, v71\n"
"v_cvt_pk_bf16_f32 v70, v76, v77\n"
"v_cvt_pk_bf16_f32 v71, v78, v79\n"
"v_mul_f32 v84, v84, v203\n"
"v_mul_f32 v85, v85, v203\n"
"v_mul_f32 v86, v86, v203\n"
"v_mul_f32 v87, v87, v203\n"
"v_mul_f32 v92, v92, v203\n"
"v_mul_f32 v93, v93, v203\n"
"v_mul_f32 v94, v94, v203\n"
"v_mul_f32 v95, v95, v203\n"
"v_cvt_pk_bf16_f32 v84, v84, v85\n"
"v_cvt_pk_bf16_f32 v85, v86, v87\n"
"v_cvt_pk_bf16_f32 v86, v92, v93\n"
"v_cvt_pk_bf16_f32 v87, v94, v95\n"
"v_mul_f32 v100, v100, v203\n"
"v_mul_f32 v101, v101, v203\n"
"v_mul_f32 v102, v102, v203\n"
"v_mul_f32 v103, v103, v203\n"
"v_mul_f32 v108, v108, v203\n"
"v_mul_f32 v109, v109, v203\n"
"v_mul_f32 v110, v110, v203\n"
"v_mul_f32 v111, v111, v203\n"
"v_cvt_pk_bf16_f32 v100, v100, v101\n"
"v_cvt_pk_bf16_f32 v101, v102, v103\n"
"v_cvt_pk_bf16_f32 v102, v108, v109\n"
"v_cvt_pk_bf16_f32 v103, v110, v111\n"
"v_mul_f32 v116, v116, v203\n"
"v_mul_f32 v117, v117, v203\n"
"v_mul_f32 v118, v118, v203\n"
"v_mul_f32 v119, v119, v203\n"
"v_mul_f32 v124, v124, v203\n"
"v_mul_f32 v125, v125, v203\n"
"v_mul_f32 v126, v126, v203\n"
"v_mul_f32 v127, v127, v203\n"
"v_cvt_pk_bf16_f32 v116, v116, v117\n"
"v_cvt_pk_bf16_f32 v117, v118, v119\n"
"v_cvt_pk_bf16_f32 v118, v124, v125\n"
"v_cvt_pk_bf16_f32 v119, v126, v127\n"
"L_op:\n"
"v_readlane_b32 s52, v255, 22\n"
"s_mov_b32 s55, 0x7fffff00\n"
"s_mov_b32 s41, 2\n"
"s_mul_i32 s44, s46, 458752\n"
"s_sub_u32 s44, 1605632, s44\n"
"v_add_u32 v249, s44, v225\n"
"s_add_u32 s53, s52, 16384\n"
"s_add_u32 s54, s52, 32768\n"
"s_nop 1\n"
"L_op_loop:\n"
"s_waitcnt lgkmcnt(0)\n"
"v_mfma_f32_16x16x32_bf16 v[8:11], a[64:67], v[0:3], 0\n"
"ds_read_b128 a[156:159], v217 offset:7168\n"
"s_add_u32 m0, m0, 4096\n"
"v_mfma_f32_16x16x32_bf16 v[8:11], a[68:71], v[16:19], v[8:11]\n"
"ds_read_b128 a[160:163], v217 offset:8192\n"
"buffer_load_dwordx4 v220, s[20:23], s38 offen offset:0 sc1 lds\n"
"v_mfma_f32_16x16x32_bf16 v[8:11], a[72:75], v[32:35], v[8:11]\n"
"ds_read_b128 a[164:167], v217 offset:9216\n"
"v_add_u32 v224, s36, v218\n"
"v_mfma_f32_16x16x32_bf16 v[8:11], a[76:79], v[48:51], v[8:11]\n"
"ds_read_b128 a[168:171], v217 offset:10240\n"
"s_add_u32 s68, s37, 32768\n"
"v_mfma_f32_16x16x32_bf16 v[8:11], a[80:83], v[64:67], v[8:11]\n"
"ds_read_b128 a[172:175], v217 offset:11264\n"
"s_cmp_ge_u32 s68, s39\n"
"v_mfma_f32_16x16x32_bf16 v[8:11], a[84:87], v[80:83], v[8:11]\n"
"ds_read_b128 a[176:179], v217 offset:12288\n"
"buffer_load_dwordx4 v220, s[20:23], s38 offen offset:1024 sc1 lds\n"
"v_mfma_f32_16x16x32_bf16 v[8:11], a[88:91], v[96:99], v[8:11]\n"
"ds_read_b128 a[180:183], v217 offset:13312\n"
"s_cselect_b32 s68, s40, s68\n"
"v_mfma_f32_16x16x32_bf16 v[8:11], a[92:95], v[112:115], v[8:11]\n"
"ds_read_b128 a[184:187], v217 offset:14336\n"
"v_add_u32 v216, s68, v218\n"
"v_mfma_f32_16x16x32_bf16 v[12:15], a[64:67], v[4:7], 0\n"
"ds_read_b128 a[188:191], v217 offset:15360\n"
"s_add_u32 s69, s38, 32768\n"
"v_mfma_f32_16x16x32_bf16 v[12:15], a[68:71], v[20:23], v[12:15]\n"
"ds_read_b128 a[192:195], v217 offset:16384\n"
"buffer_load_dwordx4 v220, s[20:23], s38 offen offset:2048 sc1 lds\n"
"v_mfma_f32_16x16x32_bf16 v[12:15], a[72:75], v[36:39], v[12:15]\n"
"ds_read_b128 a[196:199], v217 offset:17408\n"
"ds_read_b128 a[64:67], v224 offset:16384\n"
"v_mfma_f32_16x16x32_bf16 v[12:15], a[76:79], v[52:55], v[12:15]\n"
"v_cvt_pk_bf16_f32 v128, v104, v105\n"
"v_cvt_pk_bf16_f32 v129, v106, v107\n"
"v_mfma_f32_16x16x32_bf16 v[12:15], a[80:83], v[68:71], v[12:15]\n"
"ds_read_b128 a[68:71], v224 offset:17408\n"
"v_cvt_pk_bf16_f32 v130, v120, v121\n"
"v_mfma_f32_16x16x32_bf16 v[12:15], a[84:87], v[84:87], v[12:15]\n"
"buffer_load_dwordx4 v220, s[20:23], s38 offen offset:3072 sc1 lds\n"
"v_cvt_pk_bf16_f32 v131, v122, v123\n"
"v_mfma_f32_16x16x32_bf16 v[12:15], a[88:91], v[100:103], v[12:15]\n"
"buffer_store_dwordx4 v[128:131], v225, s[28:31], s55 offen offset:0 sc1\n"
"v_cvt_pk_bf16_f32 v132, v108, v109\n"
"v_mfma_f32_16x16x32_bf16 v[12:15], a[92:95], v[116:119], v[12:15]\n"
"ds_read_b128 a[72:75], v224 offset:18432\n"
"v_mfma_f32_16x16x32_bf16 v[24:27], a[96:99], v[0:3], 0\n"
"ds_read_b128 a[76:79], v224 offset:19456\n"
"v_cvt_pk_bf16_f32 v133, v110, v111\n"
"v_mfma_f32_16x16x32_bf16 v[24:27], a[100:103], v[16:19], v[24:27]\n"
"v_cvt_pk_bf16_f32 v134, v124, v125\n"
"v_cvt_pk_bf16_f32 v135, v126, v127\n"
"v_mfma_f32_16x16x32_bf16 v[24:27], a[104:107], v[32:35], v[24:27]\n"
"ds_read_b128 a[80:83], v224 offset:20480\n"
"buffer_store_dwordx4 v[132:135], v249, s[28:31], s55 offen offset:0 sc1\n"
"v_mfma_f32_16x16x32_bf16 v[24:27], a[108:111], v[48:51], v[24:27]\n"
"s_add_u32 s38, s38, 65536\n"
"v_mfma_f32_16x16x32_bf16 v[24:27], a[112:115], v[64:67], v[24:27]\n"
"v_mfma_f32_16x16x32_bf16 v[24:27], a[116:119], v[80:83], v[24:27]\n"
"ds_read_b128 a[84:87], v224 offset:21504\n"
"v_mfma_f32_16x16x32_bf16 v[24:27], a[120:123], v[96:99], v[24:27]\n"
"v_mfma_f32_16x16x32_bf16 v[24:27], a[124:127], v[112:115], v[24:27]\n"
"ds_read_b128 a[88:91], v224 offset:22528\n"
"v_mfma_f32_16x16x32_bf16 v[28:31], a[96:99], v[4:7], 0\n"
"ds_read_b128 a[92:95], v224 offset:23552\n"
"v_mfma_f32_16x16x32_bf16 v[28:31], a[100:103], v[20:23], v[28:31]\n"
"v_mfma_f32_16x16x32_bf16 v[28:31], a[104:107], v[36:39], v[28:31]\n"
"ds_read_b128 a[96:99], v224 offset:24576\n"
"v_mfma_f32_16x16x32_bf16 v[28:31], a[108:111], v[52:55], v[28:31]\n"
"s_cmp_eq_u32 s38, s49\n"
"v_mfma_f32_16x16x32_bf16 v[28:31], a[112:115], v[68:71], v[28:31]\n"
"v_mfma_f32_16x16x32_bf16 v[28:31], a[116:119], v[84:87], v[28:31]\n"
"ds_read_b128 a[100:103], v224 offset:25600\n"
"s_cselect_b32 s38, 0, s38\n"
"v_mfma_f32_16x16x32_bf16 v[28:31], a[120:123], v[100:103], v[28:31]\n"
"ds_read_b128 a[104:107], v224 offset:26624\n"
"s_cmp_eq_u32 s38, s50\n"
"v_mfma_f32_16x16x32_bf16 v[28:31], a[124:127], v[116:119], v[28:31]\n"
"s_cselect_b32 s38, 2097152, s38\n"
"s_waitcnt lgkmcnt(3)\n"
"v_mfma_f32_16x16x32_bf16 v[40:43], a[64:67], v[0:3], 0\n"
"s_add_u32 s55, s52, 49152\n"
"v_cvt_pk_bf16_f32 v136, v8, v9\n"
"v_mfma_f32_16x16x32_bf16 v[40:43], a[68:71], v[16:19], v[40:43]\n"
"ds_read_b128 a[108:111], v224 offset:27648\n"
"v_cvt_pk_bf16_f32 v137, v10, v11\n"
"v_mfma_f32_16x16x32_bf16 v[40:43], a[72:75], v[32:35], v[40:43]\n"
"v_cvt_pk_bf16_f32 v138, v24, v25\n"
"v_cvt_pk_bf16_f32 v139, v26, v27\n"
"v_mfma_f32_16x16x32_bf16 v[40:43], a[76:79], v[48:51], v[40:43]\n"
"ds_read_b128 a[112:115], v224 offset:28672\n"
"buffer_store_dwordx4 v[136:139], v225, s[28:31], s52 offen offset:0 sc1\n"
"v_mfma_f32_16x16x32_bf16 v[40:43], a[80:83], v[64:67], v[40:43]\n"
"ds_read_b128 a[116:119], v224 offset:29696\n"
"v_mfma_f32_16x16x32_bf16 v[40:43], a[84:87], v[80:83], v[40:43]\n"
"v_cvt_pk_bf16_f32 v140, v12, v13\n"
"v_cvt_pk_bf16_f32 v141, v14, v15\n"
"v_mfma_f32_16x16x32_bf16 v[40:43], a[88:91], v[96:99], v[40:43]\n"
"ds_read_b128 a[120:123], v224 offset:30720\n"
"v_cvt_pk_bf16_f32 v142, v28, v29\n"
"v_mfma_f32_16x16x32_bf16 v[40:43], a[92:95], v[112:115], v[40:43]\n"
"v_cvt_pk_bf16_f32 v143, v30, v31\n"
"buffer_store_dwordx4 v[140:143], v249, s[28:31], s52 offen offset:0 sc1\n"
"v_mfma_f32_16x16x32_bf16 v[44:47], a[64:67], v[4:7], 0\n"
"v_mfma_f32_16x16x32_bf16 v[44:47], a[68:71], v[20:23], v[44:47]\n"
"ds_read_b128 a[124:127], v224 offset:31744\n"
"ds_read_b128 a[200:203], v217 offset:18432\n"
"v_mfma_f32_16x16x32_bf16 v[44:47], a[72:75], v[36:39], v[44:47]\n"
"v_mfma_f32_16x16x32_bf16 v[44:47], a[76:79], v[52:55], v[44:47]\n"
"v_mfma_f32_16x16x32_bf16 v[44:47], a[80:83], v[68:71], v[44:47]\n"
"ds_read_b128 a[204:207], v217 offset:19456\n"
"v_mfma_f32_16x16x32_bf16 v[44:47], a[84:87], v[84:87], v[44:47]\n"
"v_mfma_f32_16x16x32_bf16 v[44:47], a[88:91], v[100:103], v[44:47]\n"
"v_mfma_f32_16x16x32_bf16 v[44:47], a[92:95], v[116:119], v[44:47]\n"
"s_waitcnt lgkmcnt(3)\n"
"v_mfma_f32_16x16x32_bf16 v[56:59], a[96:99], v[0:3], 0\n"
"s_waitcnt vmcnt(20) lgkmcnt(2)\n"
"s_barrier\n"
"v_mfma_f32_16x16x32_bf16 v[56:59], a[100:103], v[16:19], v[56:59]\n"
"v_mfma_f32_16x16x32_bf16 v[56:59], a[104:107], v[32:35], v[56:59]\n"
"s_mov_b32 m0, s36\n"
"ds_read_b128 a[208:211], v217 offset:20480\n"
"v_mfma_f32_16x16x32_bf16 v[56:59], a[108:111], v[48:51], v[56:59]\n"
"buffer_load_dwordx4 v219, s[20:23], s69 offen offset:0 sc1 lds\n"
"v_mfma_f32_16x16x32_bf16 v[56:59], a[112:115], v[64:67], v[56:59]\n"
"v_mfma_f32_16x16x32_bf16 v[56:59], a[116:119], v[80:83], v[56:59]\n"
"ds_read_b128 a[212:215], v217 offset:21504\n"
"v_mfma_f32_16x16x32_bf16 v[56:59], a[120:123], v[96:99], v[56:59]\n"
"v_mfma_f32_16x16x32_bf16 v[56:59], a[124:127], v[112:115], v[56:59]\n"
"v_mfma_f32_16x16x32_bf16 v[60:63], a[96:99], v[4:7], 0\n"
"buffer_load_dwordx4 v219, s[20:23], s69 offen offset:1024 sc1 lds\n"
"ds_read_b128 a[216:219], v217 offset:22528\n"
"v_mfma_f32_16x16x32_bf16 v[60:63], a[100:103], v[20:23], v[60:63]\n"
"v_mfma_f32_16x16x32_bf16 v[60:63], a[104:107], v[36:39], v[60:63]\n"
"v_mfma_f32_16x16x32_bf16 v[60:63], a[108:111], v[52:55], v[60:63]\n"
"v_mfma_f32_16x16x32_bf16 v[60:63], a[112:115], v[68:71], v[60:63]\n"
"v_mfma_f32_16x16x32_bf16 v[60:63], a[116:119], v[84:87], v[60:63]\n"
"buffer_load_dwordx4 v219, s[20:23], s69 offen offset:2048 sc1 lds\n"
"ds_read_b128 a[220:223], v217 offset:23552\n"
"v_mfma_f32_16x16x32_bf16 v[60:63], a[120:123], v[100:103], v[60:63]\n"
"v_cvt_pk_bf16_f32 v144, v40, v41\n"
"v_cvt_pk_bf16_f32 v145, v42, v43\n"
"v_mfma_f32_16x16x32_bf16 v[60:63], a[124:127], v[116:119], v[60:63]\n"
"v_cvt_pk_bf16_f32 v146, v56, v57\n"
"v_cvt_pk_bf16_f32 v147, v58, v59\n"
"v_mfma_f32_16x16x32_bf16 v[72:75], a[128:131], v[0:3], 0\n"
"buffer_store_dwordx4 v[144:147], v225, s[28:31], s53 offen offset:0 sc1\n"
"v_mfma_f32_16x16x32_bf16 v[72:75], a[132:135], v[16:19], v[72:75]\n"
"ds_read_b128 a[224:227], v217 offset:24576\n"
"v_mfma_f32_16x16x32_bf16 v[72:75], a[136:139], v[32:35], v[72:75]\n"
"buffer_load_dwordx4 v219, s[20:23], s69 offen offset:3072 sc1 lds\n"
"s_add_u32 m0, m0, 4096\n"
"v_mfma_f32_16x16x32_bf16 v[72:75], a[140:143], v[48:51], v[72:75]\n"
"v_mfma_f32_16x16x32_bf16 v[72:75], a[144:147], v[64:67], v[72:75]\n"
"v_mfma_f32_16x16x32_bf16 v[72:75], a[148:151], v[80:83], v[72:75]\n"
"ds_read_b128 a[228:231], v217 offset:25600\n"
"v_cvt_pk_bf16_f32 v148, v44, v45\n"
"v_mfma_f32_16x16x32_bf16 v[72:75], a[152:155], v[96:99], v[72:75]\n"
"v_cvt_pk_bf16_f32 v149, v46, v47\n"
"v_cvt_pk_bf16_f32 v150, v60, v61\n"
"v_mfma_f32_16x16x32_bf16 v[72:75], a[156:159], v[112:115], v[72:75]\n"
"v_cvt_pk_bf16_f32 v151, v62, v63\n"
"buffer_store_dwordx4 v[148:151], v249, s[28:31], s53 offen offset:0 sc1\n"
"v_mfma_f32_16x16x32_bf16 v[76:79], a[128:131], v[4:7], 0\n"
"buffer_load_dwordx4 v220, s[20:23], s69 offen offset:0 sc1 lds\n"
"ds_read_b128 a[232:235], v217 offset:26624\n"
"v_mfma_f32_16x16x32_bf16 v[76:79], a[132:135], v[20:23], v[76:79]\n"
"v_mfma_f32_16x16x32_bf16 v[76:79], a[136:139], v[36:39], v[76:79]\n"
"v_mfma_f32_16x16x32_bf16 v[76:79], a[140:143], v[52:55], v[76:79]\n"
"v_mfma_f32_16x16x32_bf16 v[76:79], a[144:147], v[68:71], v[76:79]\n"
"ds_read_b128 a[236:239], v217 offset:27648\n"
"ds_read_b128 a[64:67], v216 offset:0\n"
"v_mfma_f32_16x16x32_bf16 v[76:79], a[148:151], v[84:87], v[76:79]\n"
"v_mfma_f32_16x16x32_bf16 v[76:79], a[152:155], v[100:103], v[76:79]\n"
"buffer_load_dwordx4 v220, s[20:23], s69 offen offset:1024 sc1 lds\n"
"v_mfma_f32_16x16x32_bf16 v[76:79], a[156:159], v[116:119], v[76:79]\n"
"v_mfma_f32_16x16x32_bf16 v[88:91], a[160:163], v[0:3], 0\n"
"v_mfma_f32_16x16x32_bf16 v[88:91], a[164:167], v[16:19], v[88:91]\n"
"ds_read_b128 a[240:243], v217 offset:28672\n"
"v_mfma_f32_16x16x32_bf16 v[88:91], a[168:171], v[32:35], v[88:91]\n"
"v_mfma_f32_16x16x32_bf16 v[88:91], a[172:175], v[48:51], v[88:91]\n"
"v_mfma_f32_16x16x32_bf16 v[88:91], a[176:179], v[64:67], v[88:91]\n"
"buffer_load_dwordx4 v220, s[20:23], s69 offen offset:2048 sc1 lds\n"
"ds_read_b128 a[244:247], v217 offset:29696\n"
"v_mfma_f32_16x16x32_bf16 v[88:91], a[180:183], v[80:83], v[88:91]\n"
"v_mfma_f32_16x16x32_bf16 v[88:91], a[184:187], v[96:99], v[88:91]\n"
"v_mfma_f32_16x16x32_bf16 v[88:91], a[188:191], v[112:115], v[88:91]\n"
"ds_read_b128 a[68:71], v216 offset:1024\n"
"v_mfma_f32_16x16x32_bf16 v[92:95], a[160:163], v[4:7], 0\n"
"ds_read_b128 a[248:251], v217 offset:30720\n"
"ds_read_b128 a[72:75], v216 offset:2048\n"
"v_mfma_f32_16x16x32_bf16 v[92:95], a[164:167], v[20:23], v[92:95]\n"
"v_mfma_f32_16x16x32_bf16 v[92:95], a[168:171], v[36:39], v[92:95]\n"
"buffer_load_dwordx4 v220, s[20:23], s69 offen offset:3072 sc1 lds\n"
"v_mfma_f32_16x16x32_bf16 v[92:95], a[172:175], v[52:55], v[92:95]\n"
"v_mfma_f32_16x16x32_bf16 v[92:95], a[176:179], v[68:71], v[92:95]\n"
"ds_read_b128 a[252:255], v217 offset:31744\n"
"v_mfma_f32_16x16x32_bf16 v[92:95], a[180:183], v[84:87], v[92:95]\n"
"ds_read_b128 a[76:79], v216 offset:3072\n"
"v_cvt_pk_bf16_f32 v152, v72, v73\n"
"v_mfma_f32_16x16x32_bf16 v[92:95], a[184:187], v[100:103], v[92:95]\n"
"ds_read_b128 a[80:83], v216 offset:4096\n"
"v_mfma_f32_16x16x32_bf16 v[92:95], a[188:191], v[116:119], v[92:95]\n"
"ds_read_b128 a[84:87], v216 offset:5120\n"
"v_cvt_pk_bf16_f32 v153, v74, v75\n"
"v_mfma_f32_16x16x32_bf16 v[104:107], a[192:195], v[0:3], 0\n"
"v_cvt_pk_bf16_f32 v154, v88, v89\n"
"v_cvt_pk_bf16_f32 v155, v90, v91\n"
"v_mfma_f32_16x16x32_bf16 v[104:107], a[196:199], v[16:19], v[104:107]\n"
"buffer_store_dwordx4 v[152:155], v225, s[28:31], s54 offen offset:0 sc1\n"
"s_waitcnt lgkmcnt(4)\n"
"v_mfma_f32_16x16x32_bf16 v[104:107], a[200:203], v[32:35], v[104:107]\n"
"s_waitcnt vmcnt(23) lgkmcnt(3)\n"
"s_barrier\n"
"v_mfma_f32_16x16x32_bf16 v[104:107], a[204:207], v[48:51], v[104:107]\n"
"v_mfma_f32_16x16x32_bf16 v[104:107], a[208:211], v[64:67], v[104:107]\n"
"s_mov_b32 m0, s37\n"
"v_mfma_f32_16x16x32_bf16 v[104:107], a[212:215], v[80:83], v[104:107]\n"
"buffer_load_dwordx4 v219, s[20:23], s38 offen offset:0 sc1 lds\n"
"s_add_u32 s37, s68, 32768\n"
"v_mfma_f32_16x16x32_bf16 v[104:107], a[216:219], v[96:99], v[104:107]\n"
"v_cvt_pk_bf16_f32 v156, v76, v77\n"
"v_cvt_pk_bf16_f32 v157, v78, v79\n"
"v_mfma_f32_16x16x32_bf16 v[104:107], a[220:223], v[112:115], v[104:107]\n"
"v_cvt_pk_bf16_f32 v158, v92, v93\n"
"v_cvt_pk_bf16_f32 v159, v94, v95\n"
"v_mfma_f32_16x16x32_bf16 v[108:111], a[192:195], v[4:7], 0\n"
"buffer_store_dwordx4 v[156:159], v249, s[28:31], s54 offen offset:0 sc1\n"
"v_mfma_f32_16x16x32_bf16 v[108:111], a[196:199], v[20:23], v[108:111]\n"
"ds_read_b128 a[88:91], v216 offset:6144\n"
"v_mfma_f32_16x16x32_bf16 v[108:111], a[200:203], v[36:39], v[108:111]\n"
"s_cmp_ge_u32 s37, s39\n"
"s_cselect_b32 s37, s40, s37\n"
"v_mfma_f32_16x16x32_bf16 v[108:111], a[204:207], v[52:55], v[108:111]\n"
"buffer_load_dwordx4 v219, s[20:23], s38 offen offset:1024 sc1 lds\n"
"v_add_u32 v217, s37, v218\n"
"v_mfma_f32_16x16x32_bf16 v[108:111], a[208:211], v[68:71], v[108:111]\n"
"s_mov_b32 s36, s68\n"
"ds_read_b128 a[92:95], v216 offset:7168\n"
"v_mfma_f32_16x16x32_bf16 v[108:111], a[212:215], v[84:87], v[108:111]\n"
"ds_read_b128 a[96:99], v216 offset:8192\n"
"v_mfma_f32_16x16x32_bf16 v[108:111], a[216:219], v[100:103], v[108:111]\n"
"ds_read_b128 a[100:103], v216 offset:9216\n"
"v_mfma_f32_16x16x32_bf16 v[108:111], a[220:223], v[116:119], v[108:111]\n"
"ds_read_b128 a[104:107], v216 offset:10240\n"
"v_mfma_f32_16x16x32_bf16 v[120:123], a[224:227], v[0:3], 0\n"
"ds_read_b128 a[108:111], v216 offset:11264\n"
"v_mfma_f32_16x16x32_bf16 v[120:123], a[228:231], v[16:19], v[120:123]\n"
"buffer_load_dwordx4 v219, s[20:23], s38 offen offset:2048 sc1 lds\n"
"ds_read_b128 a[112:115], v216 offset:12288\n"
"v_mfma_f32_16x16x32_bf16 v[120:123], a[232:235], v[32:35], v[120:123]\n"
"ds_read_b128 a[116:119], v216 offset:13312\n"
"s_add_u32 s52, s52, 65536\n"
"v_mfma_f32_16x16x32_bf16 v[120:123], a[236:239], v[48:51], v[120:123]\n"
"ds_read_b128 a[120:123], v216 offset:14336\n"
"s_add_u32 s53, s53, 65536\n"
"v_mfma_f32_16x16x32_bf16 v[120:123], a[240:243], v[64:67], v[120:123]\n"
"ds_read_b128 a[124:127], v216 offset:15360\n"
"s_add_u32 s54, s54, 65536\n"
"v_mfma_f32_16x16x32_bf16 v[120:123], a[244:247], v[80:83], v[120:123]\n"
"ds_read_b128 a[128:131], v217 offset:0\n"
"v_mfma_f32_16x16x32_bf16 v[120:123], a[248:251], v[96:99], v[120:123]\n"
"ds_read_b128 a[132:135], v217 offset:1024\n"
"v_mfma_f32_16x16x32_bf16 v[120:123], a[252:255], v[112:115], v[120:123]\n"
"buffer_load_dwordx4 v219, s[20:23], s38 offen offset:3072 sc1 lds\n"
"ds_read_b128 a[136:139], v217 offset:2048\n"
"v_mfma_f32_16x16x32_bf16 v[124:127], a[224:227], v[4:7], 0\n"
"ds_read_b128 a[140:143], v217 offset:3072\n"
"v_mfma_f32_16x16x32_bf16 v[124:127], a[228:231], v[20:23], v[124:127]\n"
"ds_read_b128 a[144:147], v217 offset:4096\n"
"v_mfma_f32_16x16x32_bf16 v[124:127], a[232:235], v[36:39], v[124:127]\n"
"ds_read_b128 a[148:151], v217 offset:5120\n"
"v_mfma_f32_16x16x32_bf16 v[124:127], a[236:239], v[52:55], v[124:127]\n"
"ds_read_b128 a[152:155], v217 offset:6144\n"
"v_mfma_f32_16x16x32_bf16 v[124:127], a[240:243], v[68:71], v[124:127]\n"
"v_mfma_f32_16x16x32_bf16 v[124:127], a[244:247], v[84:87], v[124:127]\n"
"v_mfma_f32_16x16x32_bf16 v[124:127], a[248:251], v[100:103], v[124:127]\n"
"v_mfma_f32_16x16x32_bf16 v[124:127], a[252:255], v[116:119], v[124:127]\n"
"s_sub_u32 s41, s41, 1\n"
"s_cbranch_scc0 L_op_loop\n"
"s_waitcnt lgkmcnt(0)\n"
"v_mfma_f32_16x16x32_bf16 v[8:11], a[64:67], v[0:3], 0\n"
"v_mfma_f32_16x16x32_bf16 v[8:11], a[68:71], v[16:19], v[8:11]\n"
"v_mfma_f32_16x16x32_bf16 v[8:11], a[72:75], v[32:35], v[8:11]\n"
"v_add_u32 v224, s36, v218\n"
"v_mfma_f32_16x16x32_bf16 v[8:11], a[76:79], v[48:51], v[8:11]\n"
"s_add_u32 s68, s37, 32768\n"
"v_mfma_f32_16x16x32_bf16 v[8:11], a[80:83], v[64:67], v[8:11]\n"
"s_cmp_ge_u32 s68, s39\n"
"v_mfma_f32_16x16x32_bf16 v[8:11], a[84:87], v[80:83], v[8:11]\n"
"v_mfma_f32_16x16x32_bf16 v[8:11], a[88:91], v[96:99], v[8:11]\n"
"s_cselect_b32 s68, s40, s68\n"
"v_mfma_f32_16x16x32_bf16 v[8:11], a[92:95], v[112:115], v[8:11]\n"
"v_add_u32 v216, s68, v218\n"
"v_mfma_f32_16x16x32_bf16 v[12:15], a[64:67], v[4:7], 0\n"
"s_add_u32 s69, s38, 32768\n"
"v_mfma_f32_16x16x32_bf16 v[12:15], a[68:71], v[20:23], v[12:15]\n"
"v_mfma_f32_16x16x32_bf16 v[12:15], a[72:75], v[36:39], v[12:15]\n"
"ds_read_b128 a[64:67], v224 offset:16384\n"
"v_mfma_f32_16x16x32_bf16 v[12:15], a[76:79], v[52:55], v[12:15]\n"
"v_cvt_pk_bf16_f32 v128, v104, v105\n"
"v_cvt_pk_bf16_f32 v129, v106, v107\n"
"v_mfma_f32_16x16x32_bf16 v[12:15], a[80:83], v[68:71], v[12:15]\n"
"ds_read_b128 a[68:71], v224 offset:17408\n"
"v_cvt_pk_bf16_f32 v130, v120, v121\n"
"v_mfma_f32_16x16x32_bf16 v[12:15], a[84:87], v[84:87], v[12:15]\n"
"v_cvt_pk_bf16_f32 v131, v122, v123\n"
"v_mfma_f32_16x16x32_bf16 v[12:15], a[88:91], v[100:103], v[12:15]\n"
"buffer_store_dwordx4 v[128:131], v225, s[28:31], s55 offen offset:0 sc1\n"
"v_cvt_pk_bf16_f32 v132, v108, v109\n"
"v_mfma_f32_16x16x32_bf16 v[12:15], a[92:95], v[116:119], v[12:15]\n"
"ds_read_b128 a[72:75], v224 offset:18432\n"
"v_mfma_f32_16x16x32_bf16 v[24:27], a[96:99], v[0:3], 0\n"
"ds_read_b128 a[76:79], v224 offset:19456\n"
"v_cvt_pk_bf16_f32 v133, v110, v111\n"
"v_mfma_f32_16x16x32_bf16 v[24:27], a[100:103], v[16:19], v[24:27]\n"
"v_cvt_pk_bf16_f32 v134, v124, v125\n"
"v_cvt_pk_bf16_f32 v135, v126, v127\n"
"v_mfma_f32_16x16x32_bf16 v[24:27], a[104:107], v[32:35], v[24:27]\n"
"ds_read_b128 a[80:83], v224 offset:20480\n"
"buffer_store_dwordx4 v[132:135], v249, s[28:31], s55 offen offset:0 sc1\n"
"v_mfma_f32_16x16x32_bf16 v[24:27], a[108:111], v[48:51], v[24:27]\n"
"s_add_u32 s38, s38, 65536\n"
"v_mfma_f32_16x16x32_bf16 v[24:27], a[112:115], v[64:67], v[24:27]\n"
"v_mfma_f32_16x16x32_bf16 v[24:27], a[116:119], v[80:83], v[24:27]\n"
"ds_read_b128 a[84:87], v224 offset:21504\n"
"v_mfma_f32_16x16x32_bf16 v[24:27], a[120:123], v[96:99], v[24:27]\n"
"v_mfma_f32_16x16x32_bf16 v[24:27], a[124:127], v[112:115], v[24:27]\n"
"ds_read_b128 a[88:91], v224 offset:22528\n"
"v_mfma_f32_16x16x32_bf16 v[28:31], a[96:99], v[4:7], 0\n"
"ds_read_b128 a[92:95], v224 offset:23552\n"
"v_mfma_f32_16x16x32_bf16 v[28:31], a[100:103], v[20:23], v[28:31]\n"
"v_mfma_f32_16x16x32_bf16 v[28:31], a[104:107], v[36:39], v[28:31]\n"
"ds_read_b128 a[96:99], v224 offset:24576\n"
"v_mfma_f32_16x16x32_bf16 v[28:31], a[108:111], v[52:55], v[28:31]\n"
"s_cmp_eq_u32 s38, s49\n"
"v_mfma_f32_16x16x32_bf16 v[28:31], a[112:115], v[68:71], v[28:31]\n"
"v_mfma_f32_16x16x32_bf16 v[28:31], a[116:119], v[84:87], v[28:31]\n"
"ds_read_b128 a[100:103], v224 offset:25600\n"
"s_cselect_b32 s38, 0, s38\n"
"v_mfma_f32_16x16x32_bf16 v[28:31], a[120:123], v[100:103], v[28:31]\n"
"ds_read_b128 a[104:107], v224 offset:26624\n"
"s_cmp_eq_u32 s38, s50\n"
"v_mfma_f32_16x16x32_bf16 v[28:31], a[124:127], v[116:119], v[28:31]\n"
"s_cselect_b32 s38, 2097152, s38\n"
"s_waitcnt lgkmcnt(3)\n"
"v_mfma_f32_16x16x32_bf16 v[40:43], a[64:67], v[0:3], 0\n"
"s_add_u32 s55, s52, 49152\n"
"v_cvt_pk_bf16_f32 v136, v8, v9\n"
"v_mfma_f32_16x16x32_bf16 v[40:43], a[68:71], v[16:19], v[40:43]\n"
"ds_read_b128 a[108:111], v224 offset:27648\n"
"v_cvt_pk_bf16_f32 v137, v10, v11\n"
"v_mfma_f32_16x16x32_bf16 v[40:43], a[72:75], v[32:35], v[40:43]\n"
"v_cvt_pk_bf16_f32 v138, v24, v25\n"
"v_cvt_pk_bf16_f32 v139, v26, v27\n"
"v_mfma_f32_16x16x32_bf16 v[40:43], a[76:79], v[48:51], v[40:43]\n"
"ds_read_b128 a[112:115], v224 offset:28672\n"
"buffer_store_dwordx4 v[136:139], v225, s[28:31], s52 offen offset:0 sc1\n"
"v_mfma_f32_16x16x32_bf16 v[40:43], a[80:83], v[64:67], v[40:43]\n"
"ds_read_b128 a[116:119], v224 offset:29696\n"
"v_mfma_f32_16x16x32_bf16 v[40:43], a[84:87], v[80:83], v[40:43]\n"
"v_cvt_pk_bf16_f32 v140, v12, v13\n"
"v_cvt_pk_bf16_f32 v141, v14, v15\n"
"v_mfma_f32_16x16x32_bf16 v[40:43], a[88:91], v[96:99], v[40:43]\n"
"ds_read_b128 a[120:123], v224 offset:30720\n"
"v_cvt_pk_bf16_f32 v142, v28, v29\n"
"v_mfma_f32_16x16x32_bf16 v[40:43], a[92:95], v[112:115], v[40:43]\n"
"v_cvt_pk_bf16_f32 v143, v30, v31\n"
"buffer_store_dwordx4 v[140:143], v249, s[28:31], s52 offen offset:0 sc1\n"
"v_mfma_f32_16x16x32_bf16 v[44:47], a[64:67], v[4:7], 0\n"
"v_mfma_f32_16x16x32_bf16 v[44:47], a[68:71], v[20:23], v[44:47]\n"
"ds_read_b128 a[124:127], v224 offset:31744\n"
"v_mfma_f32_16x16x32_bf16 v[44:47], a[72:75], v[36:39], v[44:47]\n"
"v_mfma_f32_16x16x32_bf16 v[44:47], a[76:79], v[52:55], v[44:47]\n"
"v_mfma_f32_16x16x32_bf16 v[44:47], a[80:83], v[68:71], v[44:47]\n"
"v_mfma_f32_16x16x32_bf16 v[44:47], a[84:87], v[84:87], v[44:47]\n"
"v_mfma_f32_16x16x32_bf16 v[44:47], a[88:91], v[100:103], v[44:47]\n"
"v_mfma_f32_16x16x32_bf16 v[44:47], a[92:95], v[116:119], v[44:47]\n"
"s_waitcnt lgkmcnt(1)\n"
"v_mfma_f32_16x16x32_bf16 v[56:59], a[96:99], v[0:3], 0\n"
"v_mfma_f32_16x16x32_bf16 v[56:59], a[100:103], v[16:19], v[56:59]\n"
"v_mfma_f32_16x16x32_bf16 v[56:59], a[104:107], v[32:35], v[56:59]\n"
"v_mfma_f32_16x16x32_bf16 v[56:59], a[108:111], v[48:51], v[56:59]\n"
"v_mfma_f32_16x16x32_bf16 v[56:59], a[112:115], v[64:67], v[56:59]\n"
"v_mfma_f32_16x16x32_bf16 v[56:59], a[116:119], v[80:83], v[56:59]\n"
"v_mfma_f32_16x16x32_bf16 v[56:59], a[120:123], v[96:99], v[56:59]\n"
"s_waitcnt lgkmcnt(0)\n"
"v_mfma_f32_16x16x32_bf16 v[56:59], a[124:127], v[112:115], v[56:59]\n"
"v_mfma_f32_16x16x32_bf16 v[60:63], a[96:99], v[4:7], 0\n"
"v_mfma_f32_16x16x32_bf16 v[60:63], a[100:103], v[20:23], v[60:63]\n"
"v_mfma_f32_16x16x32_bf16 v[60:63], a[104:107], v[36:39], v[60:63]\n"
"v_mfma_f32_16x16x32_bf16 v[60:63], a[108:111], v[52:55], v[60:63]\n"
"v_mfma_f32_16x16x32_bf16 v[60:63], a[112:115], v[68:71], v[60:63]\n"
"v_mfma_f32_16x16x32_bf16 v[60:63], a[116:119], v[84:87], v[60:63]\n"
"v_mfma_f32_16x16x32_bf16 v[60:63], a[120:123], v[100:103], v[60:63]\n"
"v_mfma_f32_16x16x32_bf16 v[60:63], a[124:127], v[116:119], v[60:63]\n"
"s_nop 3\n"
"v_cvt_pk_bf16_f32 v144, v40, v41\n"
"v_cvt_pk_bf16_f32 v145, v42, v43\n"
"v_cvt_pk_bf16_f32 v146, v56, v57\n"
"v_cvt_pk_bf16_f32 v147, v58, v59\n"
"buffer_store_dwordx4 v[144:147], v225, s[28:31], s53 offen offset:0 sc1\n"
"v_cvt_pk_bf16_f32 v148, v44, v45\n"
"v_cvt_pk_bf16_f32 v149, v46, v47\n"
"v_cvt_pk_bf16_f32 v150, v60, v61\n"
"v_cvt_pk_bf16_f32 v151, v62, v63\n"
"buffer_store_dwordx4 v[148:151], v249, s[28:31], s53 offen offset:0 sc1\n"
"v_readlane_b32 s44, v255, 27\n"
"v_mov_b32 v246, 0x7ffffff0\n"
"v_writelane_b32 v246, 0, 0\n"
"v_mov_b32 v247, s57\n"
"v_mov_b32 v0, 0\n"
"v_mov_b32 v1, s48\n"
"s_waitcnt vmcnt(0) lgkmcnt(0)\n"
"s_barrier\n"
"buffer_store_dword v247, v246, s[28:31], s44 offen offset:0 sc1\n"
"ds_write_b32 v1, v0 offset:0\n"
"s_waitcnt lgkmcnt(0)\n"
"s_barrier\n"
      : "+{s[20:21]}"(pc), "+{s[24:25]}"(pw), "+{s26}"(wrec), "+{s[28:29]}"(pws), "+{s37}"(bpar), "+{s47}"(svar),
        "+{v232}"(offA[0]), "+{v233}"(offA[1]), "+{v234}"(offA[2]), "+{v235}"(offA[3]),
        "+{v236}"(offA2[0]), "+{v237}"(offA2[1]), "+{v238}"(offA2[2]), "+{v239}"(offA2[3]),
        "+{v240}"(offW[0]), "+{v241}"(offW[1]), "+{v242}"(offW[2]), "+{v243}"(offW[3]),
        "+{v244}"(offX[0]), "+{v245}"(offX[1]), "+{v246}"(offX[2]), "+{v247}"(offX[3]),
        "+{v248}"(vA0), "+{v249}"(vA1), "+{v250}"(vWe), "+{v251}"(vWo), "+{v252}"(vst), "+{v253}"(vstq), "+{v255}"(pv),
        "+{v[160:175]}"(pre0), "+{v[176:183]}"(pre1), "+{v[184:185]}"(pre2), "+{v186}"(vus), "+{v187}"(vupad), "={s57}"(tok)
      : "{s46}"(wid), "{s48}"(ldsb)
      : "v0", "v1", "v2", "v3", "v4", "v5", "v6", "v7", "v8", "v9", "v10", "v11", "v12", "v13", "v14", "v15", "v16", "v17", "v18", "v19", "v20", "v21", "v22", "v23", "v24", "v25", "v26", "v27", "v28", "v29", "v30", "v31", "v32", "v33", "v34", "v35", "v36", "v37", "v38", "v39", "v40", "v41", "v42", "v43", "v44", "v45", "v46", "v47", "v48", "v49", "v50", "v51", "v52", "v53", "v54", "v55", "v56", "v57", "v58", "v59", "v60", "v61", "v62", "v63", "v64", "v65", "v66", "v67", "v68", "v69", "v70", "v71", "v72", "v73", "v74", "v75", "v76", "v77", "v78", "v79", "v80", "v81", "v82", "v83", "v84", "v85", "v86", "v87", "v88", "v89", "v90", "v91", "v92", "v93", "v94", "v95", "v96", "v97", "v98", "v99", "v100", "v101", "v102", "v103", "v104", "v105", "v106", "v107", "v108", "v109", "v110", "v111", "v112", "v113", "v114", "v115", "v116", "v117", "v118", "v119", "v120", "v121", "v122", "v123", "v124", "v125", "v126", "v127", "v128", "v129", "v130", "v131", "v132", "v133", "v134", "v135", "v136", "v137", "v138", "v139", "v140", "v141", "v142", "v143", "v144", "v145", "v146", "v147", "v148", "v149", "v150", "v151", "v152", "v153", "v154", "v155", "v156", "v157", "v158", "v159", "v188", "v189", "v190", "v191", "v192", "v193", "v194", "v195", "v196", "v197", "v198", "v199", "v200", "v201", "v202", "v203", "v204", "v205", "v206", "v207", "v208", "v209", "v210", "v211", "v212", "v213", "v214", "v215", "v216", "v217", "v218", "v219", "v220", "v221", "v222", "v223", "v224", "v225", "v226", "v227", "v228", "v229", "v230", "v231", "v254", "a0", "a1", "a2", "a3", "a4", "a5", "a6", "a7", "a8", "a9", "a10", "a11", "a12", "a13", "a14", "a15", "a16", "a17", "a18", "a19", "a20", "a21", "a22", "a23", "a24", "a25", "a26", "a27", "a28", "a29", "a30", "a31", "a32", "a33", "a34", "a35", "a36", "a37", "a38", "a39", "a40", "a41", "a42", "a43", "a44", "a45", "a46", "a47", "a48", "a49", "a50", "a51", "a52", "a53", "a54", "a55", "a56", "a57", "a58", "a59", "a60", "a61", "a62", "a63", "a64", "a65", "a66", "a67", "a68", "a69", "a70", "a71", "a72", "a73", "a74", "a75", "a76", "a77", "a78", "a79", "a80", "a81", "a82", "a83", "a84", "a85", "a86", "a87", "a88", "a89", "a90", "a91", "a92", "a93", "a94", "a95", "a96", "a97", "a98", "a99", "a100", "a101", "a102", "a103", "a104", "a105", "a106", "a107", "a108", "a109", "a110", "a111", "a112", "a113", "a114", "a115", "a116", "a117", "a118", "a119", "a120", "a121", "a122", "a123", "a124", "a125", "a126", "a127", "a128", "a129", "a130", "a131", "a132", "a133", "a134", "a135", "a136", "a137", "a138", "a139", "a140", "a141", "a142", "a143", "a144", "a145", "a146", "a147", "a148", "a149", "a150", "a151", "a152", "a153", "a154", "a155", "a156", "a157", "a158", "a159", "a160", "a161", "a162", "a163", "a164", "a165", "a166", "a167", "a168", "a169", "a170", "a171", "a172", "a173", "a174", "a175", "a176", "a177", "a178", "a179", "a180", "a181", "a182", "a183", "a184", "a185", "a186", "a187", "a188", "a189", "a190", "a191", "a192", "a193", "a194", "a195", "a196", "a197", "a198", "a199", "a200", "a201", "a202", "a203", "a204", "a205", "a206", "a207", "a208", "a209", "a210", "a211", "a212", "a213", "a214", "a215", "a216", "a217", "a218", "a219", "a220", "a221", "a222", "a223", "a224", "a225", "a226", "a227", "a228", "a229", "a230", "a231", "a232", "a233", "a234", "a235", "a236", "a237", "a238", "a239", "a240", "a241", "a242", "a243", "a244", "a245", "a246", "a247", "a248", "a249", "a250", "a251", "a252", "a253", "a254", "a255", "s22", "s23", "s27", "s30", "s31", "s36", "s38", "s39", "s40", "s41", "s42", "s43", "s44", "s45", "s49", "s50", "s51", "s52", "s53", "s54", "s55", "s56", "s58", "s59", "s60", "s61", "s62", "s63", "s64", "s65", "s66", "s67", "s68", "s69", "s70", "s71", "s72", "s73", "s74", "s75", "s76", "s77", "vcc", "scc", "memory");

  }
  const rsrc_t rout = mk_rsrc(out, 2048 * 896 * 2);
  if (typeB && pi >= 6) {
    b_stage3(head * 2 + (pi - 6), tok, rws, bq, rout, lds, wid, lane_id());

    asm volatile("L_code_end:");
    return;
  }
  {
    const unsigned l2 = lane_id(), wi = typeB ? 512 + (head * 6 + pi) * 4 + wid : (head * 8 + pi) * 4 + wid;
    const unsigned ne = 1, i0 = wi, i1 = 896 + wi;
    const unsigned q0 = i0 / 14, q1 = i1 / 14;
    unsigned vf = (l2 < 32 && (l2 >= 16 || ne)) ? F_C + (((l2 < 16 ? q0 : q1) >> 3) * 16 + (l2 & 15)) * 128 : OOB;
    const unsigned vol = (l2 & 15) * 1792 + (l2 >> 4) * 16;
    asm volatile("s_nop 1\n"
"s_cmp_eq_u32 s39, 0\n"
"s_cselect_b32 s40, 0xffff, 0\n"
"s_cselect_b32 s45, 0x10000, 0\n"
"s_mov_b32 s42, 100000\n"
"s_mov_b32 s46, 0xffff0000\n"
"s_mov_b32 s43, 16384\n"
"s_cmp_eq_u32 s39, 2\n"
"s_cselect_b32 s43, s43, 0x70000000\n"
"v_add_u32 v205, s43, v201\n"
"v_add_u32 v204, 64, v202\n"
"v_mov_b32 v206, s51\n"
"s_mov_b32 s52, s40\n"
"v_mov_b32 v236, 0x7fffff00\n"
"L_rd_poll:\n"
"s_cmp_lg_u32 s50, 0\n"
"s_cbranch_scc1 L_rd_lds\n"
"buffer_load_dword v207, v200, s[28:31], 0 offen offset:0 sc1\n"
"s_waitcnt vmcnt(0)\n"
"v_cmp_eq_u32 vcc, s36, v207\n"
"s_nop 1\n"
"v_cndmask_b32 v200, v200, v236, vcc\n"
"s_or_b32 s52, vcc_lo, s52\n"
"v_mov_b32 v207, s52\n"
"ds_write_b32 v206, v207 offset:0\n"
"s_and_b32 s41, s52, 0xffff\n"
"s_cmp_eq_u32 s41, 0xffff\n"
"s_cselect_b32 s41, s41, 0\n"
"s_and_b32 s44, s52, s46\n"
"s_cmp_eq_u32 s44, s46\n"
"s_cselect_b32 s44, s44, 0\n"
"s_or_b32 s41, s41, s44\n"
"s_branch L_rd_got\n"
"L_rd_lds:\n"
"ds_read_b32 v207, v206 offset:0\n"
"s_waitcnt lgkmcnt(0)\n"
"v_readfirstlane_b32 s41, v207\n"
"L_rd_got:\n"
"s_and_b32 s44, s40, 0xffff\n"
"s_or_b32 s44, s44, s45\n"
"s_andn2_b32 s41, s41, s40\n"
"s_or_b32 s40, s40, s41\n"
"s_bitcmp1_b32 s41, 16\n"
"s_cbranch_scc0 L_rd_s16\n"
"s_add_u32 s47, s38, 0\n"
"buffer_load_dwordx4 v[64:67], v201, s[28:31], s47 offen offset:0 sc1\n"
"L_rd_s16:\n"
"s_bitcmp1_b32 s41, 17\n"
"s_cbranch_scc0 L_rd_s17\n"
"s_add_u32 s47, s38, 0\n"
"buffer_load_dwordx4 v[68:71], v201, s[28:31], s47 offen offset:1024 sc1\n"
"L_rd_s17:\n"
"s_bitcmp1_b32 s41, 18\n"
"s_cbranch_scc0 L_rd_s18\n"
"s_add_u32 s47, s38, 0\n"
"buffer_load_dwordx4 v[72:75], v201, s[28:31], s47 offen offset:2048 sc1\n"
"L_rd_s18:\n"
"s_bitcmp1_b32 s41, 19\n"
"s_cbranch_scc0 L_rd_s19\n"
"s_add_u32 s47, s38, 0\n"
"buffer_load_dwordx4 v[76:79], v201, s[28:31], s47 offen offset:3072 sc1\n"
"L_rd_s19:\n"
"s_bitcmp1_b32 s41, 20\n"
"s_cbranch_scc0 L_rd_s20\n"
"s_add_u32 s47, s38, 4096\n"
"buffer_load_dwordx4 v[80:83], v201, s[28:31], s47 offen offset:0 sc1\n"
"L_rd_s20:\n"
"s_bitcmp1_b32 s41, 21\n"
"s_cbranch_scc0 L_rd_s21\n"
"s_add_u32 s47, s38, 4096\n"
"buffer_load_dwordx4 v[84:87], v201, s[28:31], s47 offen offset:1024 sc1\n"
"L_rd_s21:\n"
"s_bitcmp1_b32 s41, 22\n"
"s_cbranch_scc0 L_rd_s22\n"
"s_add_u32 s47, s38, 4096\n"
"buffer_load_dwordx4 v[88:91], v201, s[28:31], s47 offen offset:2048 sc1\n"
"L_rd_s22:\n"
"s_bitcmp1_b32 s41, 23\n"
"s_cbranch_scc0 L_rd_s23\n"
"s_add_u32 s47, s38, 4096\n"
"buffer_load_dwordx4 v[92:95], v201, s[28:31], s47 offen offset:3072 sc1\n"
"L_rd_s23:\n"
"s_bitcmp1_b32 s41, 24\n"
"s_cbranch_scc0 L_rd_s24\n"
"s_add_u32 s47, s38, 8192\n"
"buffer_load_dwordx4 v[96:99], v201, s[28:31], s47 offen offset:0 sc1\n"
"L_rd_s24:\n"
"s_bitcmp1_b32 s41, 25\n"
"s_cbranch_scc0 L_rd_s25\n"
"s_add_u32 s47, s38, 8192\n"
"buffer_load_dwordx4 v[100:103], v201, s[28:31], s47 offen offset:1024 sc1\n"
"L_rd_s25:\n"
"s_bitcmp1_b32 s41, 26\n"
"s_cbranch_scc0 L_rd_s26\n"
"s_add_u32 s47, s38, 8192\n"
"buffer_load_dwordx4 v[104:107], v201, s[28:31], s47 offen offset:2048 sc1\n"
"L_rd_s26:\n"
"s_bitcmp1_b32 s41, 27\n"
"s_cbranch_scc0 L_rd_s27\n"
"s_add_u32 s47, s38, 8192\n"
"buffer_load_dwordx4 v[108:111], v201, s[28:31], s47 offen offset:3072 sc1\n"
"L_rd_s27:\n"
"s_bitcmp1_b32 s41, 28\n"
"s_cbranch_scc0 L_rd_s28\n"
"s_add_u32 s47, s38, 12288\n"
"buffer_load_dwordx4 v[112:115], v201, s[28:31], s47 offen offset:0 sc1\n"
"L_rd_s28:\n"
"s_bitcmp1_b32 s41, 29\n"
"s_cbranch_scc0 L_rd_s29\n"
"s_add_u32 s47, s38, 12288\n"
"buffer_load_dwordx4 v[116:119], v201, s[28:31], s47 offen offset:1024 sc1\n"
"L_rd_s29:\n"
"s_bitcmp1_b32 s41, 30\n"
"s_cbranch_scc0 L_rd_s30\n"
"s_add_u32 s47, s38, 12288\n"
"buffer_load_dwordx4 v[120:123], v201, s[28:31], s47 offen offset:2048 sc1\n"
"L_rd_s30:\n"
"s_bitcmp1_b32 s41, 31\n"
"s_cbranch_scc0 L_rd_s31\n"
"s_add_u32 s47, s38, 12288\n"
"buffer_load_dwordx4 v[124:127], v201, s[28:31], s47 offen offset:3072 sc1\n"
"L_rd_s31:\n"
"s_bitcmp1_b32 s41, 0\n"
"s_cbranch_scc0 L_rd_s0\n"
"s_add_u32 s47, s37, 0\n"
"buffer_load_dwordx4 v[0:3], v201, s[28:31], s47 offen offset:0 sc1\n"
"buffer_load_dwordx4 v[128:131], v205, s[28:31], s47 offen offset:0 sc1\n"
"L_rd_s0:\n"
"s_bitcmp1_b32 s41, 1\n"
"s_cbranch_scc0 L_rd_s1\n"
"s_add_u32 s47, s37, 0\n"
"buffer_load_dwordx4 v[4:7], v201, s[28:31], s47 offen offset:1024 sc1\n"
"buffer_load_dwordx4 v[132:135], v205, s[28:31], s47 offen offset:1024 sc1\n"
"L_rd_s1:\n"
"s_bitcmp1_b32 s41, 2\n"
"s_cbranch_scc0 L_rd_s2\n"
"s_add_u32 s47, s37, 0\n"
"buffer_load_dwordx4 v[8:11], v201, s[28:31], s47 offen offset:2048 sc1\n"
"buffer_load_dwordx4 v[136:139], v205, s[28:31], s47 offen offset:2048 sc1\n"
"L_rd_s2:\n"
"s_bitcmp1_b32 s41, 3\n"
"s_cbranch_scc0 L_rd_s3\n"
"s_add_u32 s47, s37, 0\n"
"buffer_load_dwordx4 v[12:15], v201, s[28:31], s47 offen offset:3072 sc1\n"
"buffer_load_dwordx4 v[140:143], v205, s[28:31], s47 offen offset:3072 sc1\n"
"L_rd_s3:\n"
"s_bitcmp1_b32 s41, 4\n"
"s_cbranch_scc0 L_rd_s4\n"
"s_add_u32 s47, s37, 4096\n"
"buffer_load_dwordx4 v[16:19], v201, s[28:31], s47 offen offset:0 sc1\n"
"buffer_load_dwordx4 v[144:147], v205, s[28:31], s47 offen offset:0 sc1\n"
"L_rd_s4:\n"
"s_bitcmp1_b32 s41, 5\n"
"s_cbranch_scc0 L_rd_s5\n"
"s_add_u32 s47, s37, 4096\n"
"buffer_load_dwordx4 v[20:23], v201, s[28:31], s47 offen offset:1024 sc1\n"
"buffer_load_dwordx4 v[148:151], v205, s[28:31], s47 offen offset:1024 sc1\n"
"L_rd_s5:\n"
"s_bitcmp1_b32 s41, 6\n"
"s_cbranch_scc0 L_rd_s6\n"
"s_add_u32 s47, s37, 4096\n"
"buffer_load_dwordx4 v[24:27], v201, s[28:31], s47 offen offset:2048 sc1\n"
"buffer_load_dwordx4 v[152:155], v205, s[28:31], s47 offen offset:2048 sc1\n"
"L_rd_s6:\n"
"s_bitcmp1_b32 s41, 7\n"
"s_cbranch_scc0 L_rd_s7\n"
"s_add_u32 s47, s37, 4096\n"
"buffer_load_dwordx4 v[28:31], v201, s[28:31], s47 offen offset:3072 sc1\n"
"buffer_load_dwordx4 v[156:159], v205, s[28:31], s47 offen offset:3072 sc1\n"
"L_rd_s7:\n"
"s_bitcmp1_b32 s41, 8\n"
"s_cbranch_scc0 L_rd_s8\n"
"s_add_u32 s47, s37, 8192\n"
"buffer_load_dwordx4 v[32:35], v201, s[28:31], s47 offen offset:0 sc1\n"
"buffer_load_dwordx4 v[160:163], v205, s[28:31], s47 offen offset:0 sc1\n"
"L_rd_s8:\n"
"s_bitcmp1_b32 s41, 9\n"
"s_cbranch_scc0 L_rd_s9\n"
"s_add_u32 s47, s37, 8192\n"
"buffer_load_dwordx4 v[36:39], v201, s[28:31], s47 offen offset:1024 sc1\n"
"buffer_load_dwordx4 v[164:167], v205, s[28:31], s47 offen offset:1024 sc1\n"
"L_rd_s9:\n"
"s_bitcmp1_b32 s41, 10\n"
"s_cbranch_scc0 L_rd_s10\n"
"s_add_u32 s47, s37, 8192\n"
"buffer_load_dwordx4 v[40:43], v201, s[28:31], s47 offen offset:2048 sc1\n"
"buffer_load_dwordx4 v[168:171], v205, s[28:31], s47 offen offset:2048 sc1\n"
"L_rd_s10:\n"
"s_bitcmp1_b32 s41, 11\n"
"s_cbranch_scc0 L_rd_s11\n"
"s_add_u32 s47, s37, 8192\n"
"buffer_load_dwordx4 v[44:47], v201, s[28:31], s47 offen offset:3072 sc1\n"
"buffer_load_dwordx4 v[172:175], v205, s[28:31], s47 offen offset:3072 sc1\n"
"L_rd_s11:\n"
"s_bitcmp1_b32 s41, 12\n"
"s_cbranch_scc0 L_rd_s12\n"
"s_add_u32 s47, s37, 12288\n"
"buffer_load_dwordx4 v[48:51], v201, s[28:31], s47 offen offset:0 sc1\n"
"buffer_load_dwordx4 v[176:179], v205, s[28:31], s47 offen offset:0 sc1\n"
"L_rd_s12:\n"
"s_bitcmp1_b32 s41, 13\n"
"s_cbranch_scc0 L_rd_s13\n"
"s_add_u32 s47, s37, 12288\n"
"buffer_load_dwordx4 v[52:55], v201, s[28:31], s47 offen offset:1024 sc1\n"
"buffer_load_dwordx4 v[180:183], v205, s[28:31], s47 offen offset:1024 sc1\n"
"L_rd_s13:\n"
"s_bitcmp1_b32 s41, 14\n"
"s_cbranch_scc0 L_rd_s14\n"
"s_add_u32 s47, s37, 12288\n"
"buffer_load_dwordx4 v[56:59], v201, s[28:31], s47 offen offset:2048 sc1\n"
"buffer_load_dwordx4 v[184:187], v205, s[28:31], s47 offen offset:2048 sc1\n"
"L_rd_s14:\n"
"s_bitcmp1_b32 s41, 15\n"
"s_cbranch_scc0 L_rd_s15\n"
"s_add_u32 s47, s37, 12288\n"
"buffer_load_dwordx4 v[60:63], v201, s[28:31], s47 offen offset:3072 sc1\n"
"buffer_load_dwordx4 v[188:191], v205, s[28:31], s47 offen offset:3072 sc1\n"
"L_rd_s15:\n"
"s_cmp_lg_u32 s44, 0xffff\n"
"s_cbranch_scc1 L_rd_n0\n"
"s_waitcnt vmcnt(0)\n"
"L_rd_early:\n"
"v_lshlrev_b32 v208, 16, v0\n"
"v_and_b32 v209, s46, v0\n"
"v_lshlrev_b32 v210, 16, v1\n"
"v_and_b32 v211, s46, v1\n"
"v_lshlrev_b32 v212, 16, v2\n"
"v_and_b32 v213, s46, v2\n"
"v_lshlrev_b32 v214, 16, v3\n"
"v_and_b32 v215, s46, v3\n"
"v_lshlrev_b32 v216, 16, v4\n"
"v_and_b32 v217, s46, v4\n"
"v_lshlrev_b32 v218, 16, v5\n"
"v_and_b32 v219, s46, v5\n"
"v_lshlrev_b32 v220, 16, v6\n"
"v_and_b32 v221, s46, v6\n"
"v_lshlrev_b32 v222, 16, v7\n"
"v_and_b32 v223, s46, v7\n"
"v_pk_add_f32 v[208:209], v[208:209], v[216:217]\n"
"v_pk_add_f32 v[210:211], v[210:211], v[218:219]\n"
"v_pk_add_f32 v[212:213], v[212:213], v[220:221]\n"
"v_pk_add_f32 v[214:215], v[214:215], v[222:223]\n"
"v_lshlrev_b32 v216, 16, v8\n"
"v_and_b32 v217, s46, v8\n"
"v_lshlrev_b32 v218, 16, v9\n"
"v_and_b32 v219, s46, v9\n"
"v_lshlrev_b32 v220, 16, v10\n"
"v_and_b32 v221, s46, v10\n"
"v_lshlrev_b32 v222, 16, v11\n"
"v_and_b32 v223, s46, v11\n"
"v_pk_add_f32 v[208:209], v[208:209], v[216:217]\n"
"v_pk_add_f32 v[210:211], v[210:211], v[218:219]\n"
"v_pk_add_f32 v[212:213], v[212:213], v[220:221]\n"
"v_pk_add_f32 v[214:215], v[214:215], v[222:223]\n"
"v_lshlrev_b32 v216, 16, v12\n"
"v_and_b32 v217, s46, v12\n"
"v_lshlrev_b32 v218, 16, v13\n"
"v_and_b32 v219, s46, v13\n"
"v_lshlrev_b32 v220, 16, v14\n"
"v_and_b32 v221, s46, v14\n"
"v_lshlrev_b32 v222, 16, v15\n"
"v_and_b32 v223, s46, v15\n"
"v_pk_add_f32 v[208:209], v[208:209], v[216:217]\n"
"v_pk_add_f32 v[210:211], v[210:211], v[218:219]\n"
"v_pk_add_f32 v[212:213], v[212:213], v[220:221]\n"
"v_pk_add_f32 v[214:215], v[214:215], v[222:223]\n"
"v_lshlrev_b32 v216, 16, v16\n"
"v_and_b32 v217, s46, v16\n"
"v_lshlrev_b32 v218, 16, v17\n"
"v_and_b32 v219, s46, v17\n"
"v_lshlrev_b32 v220, 16, v18\n"
"v_and_b32 v221, s46, v18\n"
"v_lshlrev_b32 v222, 16, v19\n"
"v_and_b32 v223, s46, v19\n"
"v_pk_add_f32 v[208:209], v[208:209], v[216:217]\n"
"v_pk_add_f32 v[210:211], v[210:211], v[218:219]\n"
"v_pk_add_f32 v[212:213], v[212:213], v[220:221]\n"
"v_pk_add_f32 v[214:215], v[214:215], v[222:223]\n"
"v_lshlrev_b32 v216, 16, v20\n"
"v_and_b32 v217, s46, v20\n"
"v_lshlrev_b32 v218, 16, v21\n"
"v_and_b32 v219, s46, v21\n"
"v_lshlrev_b32 v220, 16, v22\n"
"v_and_b32 v221, s46, v22\n"
"v_lshlrev_b32 v222, 16, v23\n"
"v_and_b32 v223, s46, v23\n"
"v_pk_add_f32 v[208:209], v[208:209], v[216:217]\n"
"v_pk_add_f32 v[210:211], v[210:211], v[218:219]\n"
"v_pk_add_f32 v[212:213], v[212:213], v[220:221]\n"
"v_pk_add_f32 v[214:215], v[214:215], v[222:223]\n"
"v_lshlrev_b32 v216, 16, v24\n"
"v_and_b32 v217, s46, v24\n"
"v_lshlrev_b32 v218, 16, v25\n"
"v_and_b32 v219, s46, v25\n"
"v_lshlrev_b32 v220, 16, v26\n"
"v_and_b32 v221, s46, v26\n"
"v_lshlrev_b32 v222, 16, v27\n"
"v_and_b32 v223, s46, v27\n"
"v_pk_add_f32 v[208:209], v[208:209], v[216:217]\n"
"v_pk_add_f32 v[210:211], v[210:211], v[218:219]\n"
"v_pk_add_f32 v[212:213], v[212:213], v[220:221]\n"
"v_pk_add_f32 v[214:215], v[214:215], v[222:223]\n"
"v_lshlrev_b32 v216, 16, v28\n"
"v_and_b32 v217, s46, v28\n"
"v_lshlrev_b32 v218, 16, v29\n"
"v_and_b32 v219, s46, v29\n"
"v_lshlrev_b32 v220, 16, v30\n"
"v_and_b32 v221, s46, v30\n"
"v_lshlrev_b32 v222, 16, v31\n"
"v_and_b32 v223, s46, v31\n"
"v_pk_add_f32 v[208:209], v[208:209], v[216:217]\n"
"v_pk_add_f32 v[210:211], v[210:211], v[218:219]\n"
"v_pk_add_f32 v[212:213], v[212:213], v[220:221]\n"
"v_pk_add_f32 v[214:215], v[214:215], v[222:223]\n"
"v_lshlrev_b32 v216, 16, v32\n"
"v_and_b32 v217, s46, v32\n"
"v_lshlrev_b32 v218, 16, v33\n"
"v_and_b32 v219, s46, v33\n"
"v_lshlrev_b32 v220, 16, v34\n"
"v_and_b32 v221, s46, v34\n"
"v_lshlrev_b32 v222, 16, v35\n"
"v_and_b32 v223, s46, v35\n"
"v_pk_add_f32 v[208:209], v[208:209], v[216:217]\n"
"v_pk_add_f32 v[210:211], v[210:211], v[218:219]\n"
"v_pk_add_f32 v[212:213], v[212:213], v[220:221]\n"
"v_pk_add_f32 v[214:215], v[214:215], v[222:223]\n"
"v_lshlrev_b32 v216, 16, v36\n"
"v_and_b32 v217, s46, v36\n"
"v_lshlrev_b32 v218, 16, v37\n"
"v_and_b32 v219, s46, v37\n"
"v_lshlrev_b32 v220, 16, v38\n"
"v_and_b32 v221, s46, v38\n"
"v_lshlrev_b32 v222, 16, v39\n"
"v_and_b32 v223, s46, v39\n"
"v_pk_add_f32 v[208:209], v[208:209], v[216:217]\n"
"v_pk_add_f32 v[210:211], v[210:211], v[218:219]\n"
"v_pk_add_f32 v[212:213], v[212:213], v[220:221]\n"
"v_pk_add_f32 v[214:215], v[214:215], v[222:223]\n"
"v_lshlrev_b32 v216, 16, v40\n"
"v_and_b32 v217, s46, v40\n"
"v_lshlrev_b32 v218, 16, v41\n"
"v_and_b32 v219, s46, v41\n"
"v_lshlrev_b32 v220, 16, v42\n"
"v_and_b32 v221, s46, v42\n"
"v_lshlrev_b32 v222, 16, v43\n"
"v_and_b32 v223, s46, v43\n"
"v_pk_add_f32 v[208:209], v[208:209], v[216:217]\n"
"v_pk_add_f32 v[210:211], v[210:211], v[218:219]\n"
"v_pk_add_f32 v[212:213], v[212:213], v[220:221]\n"
"v_pk_add_f32 v[214:215], v[214:215], v[222:223]\n"
"v_lshlrev_b32 v216, 16, v44\n"
"v_and_b32 v217, s46, v44\n"
"v_lshlrev_b32 v218, 16, v45\n"
"v_and_b32 v219, s46, v45\n"
"v_lshlrev_b32 v220, 16, v46\n"
"v_and_b32 v221, s46, v46\n"
"v_lshlrev_b32 v222, 16, v47\n"
"v_and_b32 v223, s46, v47\n"
"v_pk_add_f32 v[208:209], v[208:209], v[216:217]\n"
"v_pk_add_f32 v[210:211], v[210:211], v[218:219]\n"
"v_pk_add_f32 v[212:213], v[212:213], v[220:221]\n"
"v_pk_add_f32 v[214:215], v[214:215], v[222:223]\n"
"v_lshlrev_b32 v216, 16, v48\n"
"v_and_b32 v217, s46, v48\n"
"v_lshlrev_b32 v218, 16, v49\n"
"v_and_b32 v219, s46, v49\n"
"v_lshlrev_b32 v220, 16, v50\n"
"v_and_b32 v221, s46, v50\n"
"v_lshlrev_b32 v222, 16, v51\n"
"v_and_b32 v223, s46, v51\n"
"v_pk_add_f32 v[208:209], v[208:209], v[216:217]\n"
"v_pk_add_f32 v[210:211], v[210:211], v[218:219]\n"
"v_pk_add_f32 v[212:213], v[212:213], v[220:221]\n"
"v_pk_add_f32 v[214:215], v[214:215], v[222:223]\n"
"v_lshlrev_b32 v216, 16, v52\n"
"v_and_b32 v217, s46, v52\n"
"v_lshlrev_b32 v218, 16, v53\n"
"v_and_b32 v219, s46, v53\n"
"v_lshlrev_b32 v220, 16, v54\n"
"v_and_b32 v221, s46, v54\n"
"v_lshlrev_b32 v222, 16, v55\n"
"v_and_b32 v223, s46, v55\n"
"v_pk_add_f32 v[208:209], v[208:209], v[216:217]\n"
"v_pk_add_f32 v[210:211], v[210:211], v[218:219]\n"
"v_pk_add_f32 v[212:213], v[212:213], v[220:221]\n"
"v_pk_add_f32 v[214:215], v[214:215], v[222:223]\n"
"v_lshlrev_b32 v216, 16, v56\n"
"v_and_b32 v217, s46, v56\n"
"v_lshlrev_b32 v218, 16, v57\n"
"v_and_b32 v219, s46, v57\n"
"v_lshlrev_b32 v220, 16, v58\n"
"v_and_b32 v221, s46, v58\n"
"v_lshlrev_b32 v222, 16, v59\n"
"v_and_b32 v223, s46, v59\n"
"v_pk_add_f32 v[208:209], v[208:209], v[216:217]\n"
"v_pk_add_f32 v[210:211], v[210:211], v[218:219]\n"
"v_pk_add_f32 v[212:213], v[212:213], v[220:221]\n"
"v_pk_add_f32 v[214:215], v[214:215], v[222:223]\n"
"v_lshlrev_b32 v216, 16, v60\n"
"v_and_b32 v217, s46, v60\n"
"v_lshlrev_b32 v218, 16, v61\n"
"v_and_b32 v219, s46, v61\n"
"v_lshlrev_b32 v220, 16, v62\n"
"v_and_b32 v221, s46, v62\n"
"v_lshlrev_b32 v222, 16, v63\n"
"v_and_b32 v223, s46, v63\n"
"v_pk_add_f32 v[208:209], v[208:209], v[216:217]\n"
"v_pk_add_f32 v[210:211], v[210:211], v[218:219]\n"
"v_pk_add_f32 v[212:213], v[212:213], v[220:221]\n"
"v_pk_add_f32 v[214:215], v[214:215], v[222:223]\n"
"v_cvt_pk_bf16_f32 v224, v208, v209\n"
"v_cvt_pk_bf16_f32 v225, v210, v211\n"
"v_cvt_pk_bf16_f32 v226, v212, v213\n"
"v_cvt_pk_bf16_f32 v227, v214, v215\n"
"buffer_store_dwordx4 v[224:227], v202, s[20:23], 0 offen offset:0 sc1\n"
"s_cmp_lt_u32 s39, 2\n"
"s_cbranch_scc1 L_rd_ea\n"
"v_lshlrev_b32 v208, 16, v128\n"
"v_and_b32 v209, s46, v128\n"
"v_lshlrev_b32 v210, 16, v129\n"
"v_and_b32 v211, s46, v129\n"
"v_lshlrev_b32 v212, 16, v130\n"
"v_and_b32 v213, s46, v130\n"
"v_lshlrev_b32 v214, 16, v131\n"
"v_and_b32 v215, s46, v131\n"
"v_lshlrev_b32 v216, 16, v132\n"
"v_and_b32 v217, s46, v132\n"
"v_lshlrev_b32 v218, 16, v133\n"
"v_and_b32 v219, s46, v133\n"
"v_lshlrev_b32 v220, 16, v134\n"
"v_and_b32 v221, s46, v134\n"
"v_lshlrev_b32 v222, 16, v135\n"
"v_and_b32 v223, s46, v135\n"
"v_pk_add_f32 v[208:209], v[208:209], v[216:217]\n"
"v_pk_add_f32 v[210:211], v[210:211], v[218:219]\n"
"v_pk_add_f32 v[212:213], v[212:213], v[220:221]\n"
"v_pk_add_f32 v[214:215], v[214:215], v[222:223]\n"
"v_lshlrev_b32 v216, 16, v136\n"
"v_and_b32 v217, s46, v136\n"
"v_lshlrev_b32 v218, 16, v137\n"
"v_and_b32 v219, s46, v137\n"
"v_lshlrev_b32 v220, 16, v138\n"
"v_and_b32 v221, s46, v138\n"
"v_lshlrev_b32 v222, 16, v139\n"
"v_and_b32 v223, s46, v139\n"
"v_pk_add_f32 v[208:209], v[208:209], v[216:217]\n"
"v_pk_add_f32 v[210:211], v[210:211], v[218:219]\n"
"v_pk_add_f32 v[212:213], v[212:213], v[220:221]\n"
"v_pk_add_f32 v[214:215], v[214:215], v[222:223]\n"
"v_lshlrev_b32 v216, 16, v140\n"
"v_and_b32 v217, s46, v140\n"
"v_lshlrev_b32 v218, 16, v141\n"
"v_and_b32 v219, s46, v141\n"
"v_lshlrev_b32 v220, 16, v142\n"
"v_and_b32 v221, s46, v142\n"
"v_lshlrev_b32 v222, 16, v143\n"
"v_and_b32 v223, s46, v143\n"
"v_pk_add_f32 v[208:209], v[208:209], v[216:217]\n"
"v_pk_add_f32 v[210:211], v[210:211], v[218:219]\n"
"v_pk_add_f32 v[212:213], v[212:213], v[220:221]\n"
"v_pk_add_f32 v[214:215], v[214:215], v[222:223]\n"
"v_lshlrev_b32 v216, 16, v144\n"
"v_and_b32 v217, s46, v144\n"
"v_lshlrev_b32 v218, 16, v145\n"
"v_and_b32 v219, s46, v145\n"
"v_lshlrev_b32 v220, 16, v146\n"
"v_and_b32 v221, s46, v146\n"
"v_lshlrev_b32 v222, 16, v147\n"
"v_and_b32 v223, s46, v147\n"
"v_pk_add_f32 v[208:209], v[208:209], v[216:217]\n"
"v_pk_add_f32 v[210:211], v[210:211], v[218:219]\n"
"v_pk_add_f32 v[212:213], v[212:213], v[220:221]\n"
"v_pk_add_f32 v[214:215], v[214:215], v[222:223]\n"
"v_lshlrev_b32 v216, 16, v148\n"
"v_and_b32 v217, s46, v148\n"
"v_lshlrev_b32 v218, 16, v149\n"
"v_and_b32 v219, s46, v149\n"
"v_lshlrev_b32 v220, 16, v150\n"
"v_and_b32 v221, s46, v150\n"
"v_lshlrev_b32 v222, 16, v151\n"
"v_and_b32 v223, s46, v151\n"
"v_pk_add_f32 v[208:209], v[208:209], v[216:217]\n"
"v_pk_add_f32 v[210:211], v[210:211], v[218:219]\n"
"v_pk_add_f32 v[212:213], v[212:213], v[220:221]\n"
"v_pk_add_f32 v[214:215], v[214:215], v[222:223]\n"
"v_lshlrev_b32 v216, 16, v152\n"
"v_and_b32 v217, s46, v152\n"
"v_lshlrev_b32 v218, 16, v153\n"
"v_and_b32 v219, s46, v153\n"
"v_lshlrev_b32 v220, 16, v154\n"
"v_and_b32 v221, s46, v154\n"
"v_lshlrev_b32 v222, 16, v155\n"
"v_and_b32 v223, s46, v155\n"
"v_pk_add_f32 v[208:209], v[208:209], v[216:217]\n"
"v_pk_add_f32 v[210:211], v[210:211], v[218:219]\n"
"v_pk_add_f32 v[212:213], v[212:213], v[220:221]\n"
"v_pk_add_f32 v[214:215], v[214:215], v[222:223]\n"
"v_lshlrev_b32 v216, 16, v156\n"
"v_and_b32 v217, s46, v156\n"
"v_lshlrev_b32 v218, 16, v157\n"
"v_and_b32 v219, s46, v157\n"
"v_lshlrev_b32 v220, 16, v158\n"
"v_and_b32 v221, s46, v158\n"
"v_lshlrev_b32 v222, 16, v159\n"
"v_and_b32 v223, s46, v159\n"
"v_pk_add_f32 v[208:209], v[208:209], v[216:217]\n"
"v_pk_add_f32 v[210:211], v[210:211], v[218:219]\n"
"v_pk_add_f32 v[212:213], v[212:213], v[220:221]\n"
"v_pk_add_f32 v[214:215], v[214:215], v[222:223]\n"
"v_lshlrev_b32 v216, 16, v160\n"
"v_and_b32 v217, s46, v160\n"
"v_lshlrev_b32 v218, 16, v161\n"
"v_and_b32 v219, s46, v161\n"
"v_lshlrev_b32 v220, 16, v162\n"
"v_and_b32 v221, s46, v162\n"
"v_lshlrev_b32 v222, 16, v163\n"
"v_and_b32 v223, s46, v163\n"
"v_pk_add_f32 v[208:209], v[208:209], v[216:217]\n"
"v_pk_add_f32 v[210:211], v[210:211], v[218:219]\n"
"v_pk_add_f32 v[212:213], v[212:213], v[220:221]\n"
"v_pk_add_f32 v[214:215], v[214:215], v[222:223]\n"
"v_lshlrev_b32 v216, 16, v164\n"
"v_and_b32 v217, s46, v164\n"
"v_lshlrev_b32 v218, 16, v165\n"
"v_and_b32 v219, s46, v165\n"
"v_lshlrev_b32 v220, 16, v166\n"
"v_and_b32 v221, s46, v166\n"
"v_lshlrev_b32 v222, 16, v167\n"
"v_and_b32 v223, s46, v167\n"
"v_pk_add_f32 v[208:209], v[208:209], v[216:217]\n"
"v_pk_add_f32 v[210:211], v[210:211], v[218:219]\n"
"v_pk_add_f32 v[212:213], v[212:213], v[220:221]\n"
"v_pk_add_f32 v[214:215], v[214:215], v[222:223]\n"
"v_lshlrev_b32 v216, 16, v168\n"
"v_and_b32 v217, s46, v168\n"
"v_lshlrev_b32 v218, 16, v169\n"
"v_and_b32 v219, s46, v169\n"
"v_lshlrev_b32 v220, 16, v170\n"
"v_and_b32 v221, s46, v170\n"
"v_lshlrev_b32 v222, 16, v171\n"
"v_and_b32 v223, s46, v171\n"
"v_pk_add_f32 v[208:209], v[208:209], v[216:217]\n"
"v_pk_add_f32 v[210:211], v[210:211], v[218:219]\n"
"v_pk_add_f32 v[212:213], v[212:213], v[220:221]\n"
"v_pk_add_f32 v[214:215], v[214:215], v[222:223]\n"
"v_lshlrev_b32 v216, 16, v172\n"
"v_and_b32 v217, s46, v172\n"
"v_lshlrev_b32 v218, 16, v173\n"
"v_and_b32 v219, s46, v173\n"
"v_lshlrev_b32 v220, 16, v174\n"
"v_and_b32 v221, s46, v174\n"
"v_lshlrev_b32 v222, 16, v175\n"
"v_and_b32 v223, s46, v175\n"
"v_pk_add_f32 v[208:209], v[208:209], v[216:217]\n"
"v_pk_add_f32 v[210:211], v[210:211], v[218:219]\n"
"v_pk_add_f32 v[212:213], v[212:213], v[220:221]\n"
"v_pk_add_f32 v[214:215], v[214:215], v[222:223]\n"
"v_lshlrev_b32 v216, 16, v176\n"
"v_and_b32 v217, s46, v176\n"
"v_lshlrev_b32 v218, 16, v177\n"
"v_and_b32 v219, s46, v177\n"
"v_lshlrev_b32 v220, 16, v178\n"
"v_and_b32 v221, s46, v178\n"
"v_lshlrev_b32 v222, 16, v179\n"
"v_and_b32 v223, s46, v179\n"
"v_pk_add_f32 v[208:209], v[208:209], v[216:217]\n"
"v_pk_add_f32 v[210:211], v[210:211], v[218:219]\n"
"v_pk_add_f32 v[212:213], v[212:213], v[220:221]\n"
"v_pk_add_f32 v[214:215], v[214:215], v[222:223]\n"
"v_lshlrev_b32 v216, 16, v180\n"
"v_and_b32 v217, s46, v180\n"
"v_lshlrev_b32 v218, 16, v181\n"
"v_and_b32 v219, s46, v181\n"
"v_lshlrev_b32 v220, 16, v182\n"
"v_and_b32 v221, s46, v182\n"
"v_lshlrev_b32 v222, 16, v183\n"
"v_and_b32 v223, s46, v183\n"
"v_pk_add_f32 v[208:209], v[208:209], v[216:217]\n"
"v_pk_add_f32 v[210:211], v[210:211], v[218:219]\n"
"v_pk_add_f32 v[212:213], v[212:213], v[220:221]\n"
"v_pk_add_f32 v[214:215], v[214:215], v[222:223]\n"
"v_lshlrev_b32 v216, 16, v184\n"
"v_and_b32 v217, s46, v184\n"
"v_lshlrev_b32 v218, 16, v185\n"
"v_and_b32 v219, s46, v185\n"
"v_lshlrev_b32 v220, 16, v186\n"
"v_and_b32 v221, s46, v186\n"
"v_lshlrev_b32 v222, 16, v187\n"
"v_and_b32 v223, s46, v187\n"
"v_pk_add_f32 v[208:209], v[208:209], v[216:217]\n"
"v_pk_add_f32 v[210:211], v[210:211], v[218:219]\n"
"v_pk_add_f32 v[212:213], v[212:213], v[220:221]\n"
"v_pk_add_f32 v[214:215], v[214:215], v[222:223]\n"
"v_lshlrev_b32 v216, 16, v188\n"
"v_and_b32 v217, s46, v188\n"
"v_lshlrev_b32 v218, 16, v189\n"
"v_and_b32 v219, s46, v189\n"
"v_lshlrev_b32 v220, 16, v190\n"
"v_and_b32 v221, s46, v190\n"
"v_lshlrev_b32 v222, 16, v191\n"
"v_and_b32 v223, s46, v191\n"
"v_pk_add_f32 v[208:209], v[208:209], v[216:217]\n"
"v_pk_add_f32 v[210:211], v[210:211], v[218:219]\n"
"v_pk_add_f32 v[212:213], v[212:213], v[220:221]\n"
"v_pk_add_f32 v[214:215], v[214:215], v[222:223]\n"
"v_cvt_pk_bf16_f32 v232, v208, v209\n"
"v_cvt_pk_bf16_f32 v233, v210, v211\n"
"v_cvt_pk_bf16_f32 v234, v212, v213\n"
"v_cvt_pk_bf16_f32 v235, v214, v215\n"
"buffer_store_dwordx4 v[232:235], v204, s[20:23], 0 offen offset:0 sc1\n"
"L_rd_ea:\n"
"s_cmp_eq_u32 s45, 1\n"
"s_cbranch_scc1 L_rd_end\n"
"s_mov_b32 s45, 0x10000\n"
"L_rd_n0:\n"
"s_cmp_eq_u32 s40, -1\n"
"s_cbranch_scc1 L_rd_fin\n"
"s_cmp_lg_u32 s41, 0\n"
"s_cbranch_scc1 L_rd_poll\n"
"s_sleep 3\n"
"s_sub_u32 s42, s42, 1\n"
"s_cbranch_scc0 L_rd_poll\n"
"L_rd_fin:\n"
"s_waitcnt vmcnt(0)\n"
"v_lshlrev_b32 v208, 16, v64\n"
"v_and_b32 v209, s46, v64\n"
"v_lshlrev_b32 v210, 16, v65\n"
"v_and_b32 v211, s46, v65\n"
"v_lshlrev_b32 v212, 16, v66\n"
"v_and_b32 v213, s46, v66\n"
"v_lshlrev_b32 v214, 16, v67\n"
"v_and_b32 v215, s46, v67\n"
"v_lshlrev_b32 v216, 16, v68\n"
"v_and_b32 v217, s46, v68\n"
"v_lshlrev_b32 v218, 16, v69\n"
"v_and_b32 v219, s46, v69\n"
"v_lshlrev_b32 v220, 16, v70\n"
"v_and_b32 v221, s46, v70\n"
"v_lshlrev_b32 v222, 16, v71\n"
"v_and_b32 v223, s46, v71\n"
"v_pk_add_f32 v[208:209], v[208:209], v[216:217]\n"
"v_pk_add_f32 v[210:211], v[210:211], v[218:219]\n"
"v_pk_add_f32 v[212:213], v[212:213], v[220:221]\n"
"v_pk_add_f32 v[214:215], v[214:215], v[222:223]\n"
"v_lshlrev_b32 v216, 16, v72\n"
"v_and_b32 v217, s46, v72\n"
"v_lshlrev_b32 v218, 16, v73\n"
"v_and_b32 v219, s46, v73\n"
"v_lshlrev_b32 v220, 16, v74\n"
"v_and_b32 v221, s46, v74\n"
"v_lshlrev_b32 v222, 16, v75\n"
"v_and_b32 v223, s46, v75\n"
"v_pk_add_f32 v[208:209], v[208:209], v[216:217]\n"
"v_pk_add_f32 v[210:211], v[210:211], v[218:219]\n"
"v_pk_add_f32 v[212:213], v[212:213], v[220:221]\n"
"v_pk_add_f32 v[214:215], v[214:215], v[222:223]\n"
"v_lshlrev_b32 v216, 16, v76\n"
"v_and_b32 v217, s46, v76\n"
"v_lshlrev_b32 v218, 16, v77\n"
"v_and_b32 v219, s46, v77\n"
"v_lshlrev_b32 v220, 16, v78\n"
"v_and_b32 v221, s46, v78\n"
"v_lshlrev_b32 v222, 16, v79\n"
"v_and_b32 v223, s46, v79\n"
"v_pk_add_f32 v[208:209], v[208:209], v[216:217]\n"
"v_pk_add_f32 v[210:211], v[210:211], v[218:219]\n"
"v_pk_add_f32 v[212:213], v[212:213], v[220:221]\n"
"v_pk_add_f32 v[214:215], v[214:215], v[222:223]\n"
"v_lshlrev_b32 v216, 16, v80\n"
"v_and_b32 v217, s46, v80\n"
"v_lshlrev_b32 v218, 16, v81\n"
"v_and_b32 v219, s46, v81\n"
"v_lshlrev_b32 v220, 16, v82\n"
"v_and_b32 v221, s46, v82\n"
"v_lshlrev_b32 v222, 16, v83\n"
"v_and_b32 v223, s46, v83\n"
"v_pk_add_f32 v[208:209], v[208:209], v[216:217]\n"
"v_pk_add_f32 v[210:211], v[210:211], v[218:219]\n"
"v_pk_add_f32 v[212:213], v[212:213], v[220:221]\n"
"v_pk_add_f32 v[214:215], v[214:215], v[222:223]\n"
"v_lshlrev_b32 v216, 16, v84\n"
"v_and_b32 v217, s46, v84\n"
"v_lshlrev_b32 v218, 16, v85\n"
"v_and_b32 v219, s46, v85\n"
"v_lshlrev_b32 v220, 16, v86\n"
"v_and_b32 v221, s46, v86\n"
"v_lshlrev_b32 v222, 16, v87\n"
"v_and_b32 v223, s46, v87\n"
"v_pk_add_f32 v[208:209], v[208:209], v[216:217]\n"
"v_pk_add_f32 v[210:211], v[210:211], v[218:219]\n"
"v_pk_add_f32 v[212:213], v[212:213], v[220:221]\n"
"v_pk_add_f32 v[214:215], v[214:215], v[222:223]\n"
"v_lshlrev_b32 v216, 16, v88\n"
"v_and_b32 v217, s46, v88\n"
"v_lshlrev_b32 v218, 16, v89\n"
"v_and_b32 v219, s46, v89\n"
"v_lshlrev_b32 v220, 16, v90\n"
"v_and_b32 v221, s46, v90\n"
"v_lshlrev_b32 v222, 16, v91\n"
"v_and_b32 v223, s46, v91\n"
"v_pk_add_f32 v[208:209], v[208:209], v[216:217]\n"
"v_pk_add_f32 v[210:211], v[210:211], v[218:219]\n"
"v_pk_add_f32 v[212:213], v[212:213], v[220:221]\n"
"v_pk_add_f32 v[214:215], v[214:215], v[222:223]\n"
"v_lshlrev_b32 v216, 16, v92\n"
"v_and_b32 v217, s46, v92\n"
"v_lshlrev_b32 v218, 16, v93\n"
"v_and_b32 v219, s46, v93\n"
"v_lshlrev_b32 v220, 16, v94\n"
"v_and_b32 v221, s46, v94\n"
"v_lshlrev_b32 v222, 16, v95\n"
"v_and_b32 v223, s46, v95\n"
"v_pk_add_f32 v[208:209], v[208:209], v[216:217]\n"
"v_pk_add_f32 v[210:211], v[210:211], v[218:219]\n"
"v_pk_add_f32 v[212:213], v[212:213], v[220:221]\n"
"v_pk_add_f32 v[214:215], v[214:215], v[222:223]\n"
"v_lshlrev_b32 v216, 16, v96\n"
"v_and_b32 v217, s46, v96\n"
"v_lshlrev_b32 v218, 16, v97\n"
"v_and_b32 v219, s46, v97\n"
"v_lshlrev_b32 v220, 16, v98\n"
"v_and_b32 v221, s46, v98\n"
"v_lshlrev_b32 v222, 16, v99\n"
"v_and_b32 v223, s46, v99\n"
"v_pk_add_f32 v[208:209], v[208:209], v[216:217]\n"
"v_pk_add_f32 v[210:211], v[210:211], v[218:219]\n"
"v_pk_add_f32 v[212:213], v[212:213], v[220:221]\n"
"v_pk_add_f32 v[214:215], v[214:215], v[222:223]\n"
"v_lshlrev_b32 v216, 16, v100\n"
"v_and_b32 v217, s46, v100\n"
"v_lshlrev_b32 v218, 16, v101\n"
"v_and_b32 v219, s46, v101\n"
"v_lshlrev_b32 v220, 16, v102\n"
"v_and_b32 v221, s46, v102\n"
"v_lshlrev_b32 v222, 16, v103\n"
"v_and_b32 v223, s46, v103\n"
"v_pk_add_f32 v[208:209], v[208:209], v[216:217]\n"
"v_pk_add_f32 v[210:211], v[210:211], v[218:219]\n"
"v_pk_add_f32 v[212:213], v[212:213], v[220:221]\n"
"v_pk_add_f32 v[214:215], v[214:215], v[222:223]\n"
"v_lshlrev_b32 v216, 16, v104\n"
"v_and_b32 v217, s46, v104\n"
"v_lshlrev_b32 v218, 16, v105\n"
"v_and_b32 v219, s46, v105\n"
"v_lshlrev_b32 v220, 16, v106\n"
"v_and_b32 v221, s46, v106\n"
"v_lshlrev_b32 v222, 16, v107\n"
"v_and_b32 v223, s46, v107\n"
"v_pk_add_f32 v[208:209], v[208:209], v[216:217]\n"
"v_pk_add_f32 v[210:211], v[210:211], v[218:219]\n"
"v_pk_add_f32 v[212:213], v[212:213], v[220:221]\n"
"v_pk_add_f32 v[214:215], v[214:215], v[222:223]\n"
"v_lshlrev_b32 v216, 16, v108\n"
"v_and_b32 v217, s46, v108\n"
"v_lshlrev_b32 v218, 16, v109\n"
"v_and_b32 v219, s46, v109\n"
"v_lshlrev_b32 v220, 16, v110\n"
"v_and_b32 v221, s46, v110\n"
"v_lshlrev_b32 v222, 16, v111\n"
"v_and_b32 v223, s46, v111\n"
"v_pk_add_f32 v[208:209], v[208:209], v[216:217]\n"
"v_pk_add_f32 v[210:211], v[210:211], v[218:219]\n"
"v_pk_add_f32 v[212:213], v[212:213], v[220:221]\n"
"v_pk_add_f32 v[214:215], v[214:215], v[222:223]\n"
"v_lshlrev_b32 v216, 16, v112\n"
"v_and_b32 v217, s46, v112\n"
"v_lshlrev_b32 v218, 16, v113\n"
"v_and_b32 v219, s46, v113\n"
"v_lshlrev_b32 v220, 16, v114\n"
"v_and_b32 v221, s46, v114\n"
"v_lshlrev_b32 v222, 16, v115\n"
"v_and_b32 v223, s46, v115\n"
"v_pk_add_f32 v[208:209], v[208:209], v[216:217]\n"
"v_pk_add_f32 v[210:211], v[210:211], v[218:219]\n"
"v_pk_add_f32 v[212:213], v[212:213], v[220:221]\n"
"v_pk_add_f32 v[214:215], v[214:215], v[222:223]\n"
"v_lshlrev_b32 v216, 16, v116\n"
"v_and_b32 v217, s46, v116\n"
"v_lshlrev_b32 v218, 16, v117\n"
"v_and_b32 v219, s46, v117\n"
"v_lshlrev_b32 v220, 16, v118\n"
"v_and_b32 v221, s46, v118\n"
"v_lshlrev_b32 v222, 16, v119\n"
"v_and_b32 v223, s46, v119\n"
"v_pk_add_f32 v[208:209], v[208:209], v[216:217]\n"
"v_pk_add_f32 v[210:211], v[210:211], v[218:219]\n"
"v_pk_add_f32 v[212:213], v[212:213], v[220:221]\n"
"v_pk_add_f32 v[214:215], v[214:215], v[222:223]\n"
"v_lshlrev_b32 v216, 16, v120\n"
"v_and_b32 v217, s46, v120\n"
"v_lshlrev_b32 v218, 16, v121\n"
"v_and_b32 v219, s46, v121\n"
"v_lshlrev_b32 v220, 16, v122\n"
"v_and_b32 v221, s46, v122\n"
"v_lshlrev_b32 v222, 16, v123\n"
"v_and_b32 v223, s46, v123\n"
"v_pk_add_f32 v[208:209], v[208:209], v[216:217]\n"
"v_pk_add_f32 v[210:211], v[210:211], v[218:219]\n"
"v_pk_add_f32 v[212:213], v[212:213], v[220:221]\n"
"v_pk_add_f32 v[214:215], v[214:215], v[222:223]\n"
"v_lshlrev_b32 v216, 16, v124\n"
"v_and_b32 v217, s46, v124\n"
"v_lshlrev_b32 v218, 16, v125\n"
"v_and_b32 v219, s46, v125\n"
"v_lshlrev_b32 v220, 16, v126\n"
"v_and_b32 v221, s46, v126\n"
"v_lshlrev_b32 v222, 16, v127\n"
"v_and_b32 v223, s46, v127\n"
"v_pk_add_f32 v[208:209], v[208:209], v[216:217]\n"
"v_pk_add_f32 v[210:211], v[210:211], v[218:219]\n"
"v_pk_add_f32 v[212:213], v[212:213], v[220:221]\n"
"v_pk_add_f32 v[214:215], v[214:215], v[222:223]\n"
"v_cvt_pk_bf16_f32 v228, v208, v209\n"
"v_cvt_pk_bf16_f32 v229, v210, v211\n"
"v_cvt_pk_bf16_f32 v230, v212, v213\n"
"v_cvt_pk_bf16_f32 v231, v214, v215\n"
"buffer_store_dwordx4 v[228:231], v203, s[20:23], 0 offen offset:0 sc1\n"
"s_cmp_lg_u32 s45, 0\n"
"s_cbranch_scc1 L_rd_end\n"
"s_mov_b32 s45, 1\n"
"s_branch L_rd_early\n"
"L_rd_end:\n"
      : "+{v200}"(vf)  : "{s50}"(wid), "{s51}"(ldsb), "{s[28:31]}"(rws), "{s[20:23]}"(rout), "{s36}"(tok), "{s37}"(WS_PO + i0 * 16384), "{s38}"(WS_PO + i1 * 16384), "{s39}"(__builtin_amdgcn_readfirstlane(ne)),
         "{v201}"(l2 * 16), "{v202}"(vol + q0 * 16 * 1792 + (i0 % 14) * 64), "{v203}"(vol + q1 * 16 * 1792 + (i1 % 14) * 64)
      : "v0", "v1", "v2", "v3", "v4", "v5", "v6", "v7", "v8", "v9", "v10", "v11", "v12", "v13", "v14", "v15", "v16", "v17", "v18", "v19", "v20", "v21", "v22", "v23", "v24", "v25", "v26", "v27", "v28", "v29", "v30", "v31", "v32", "v33", "v34", "v35", "v36", "v37", "v38", "v39", "v40", "v41", "v42", "v43", "v44", "v45", "v46", "v47", "v48", "v49", "v50", "v51", "v52", "v53", "v54", "v55", "v56", "v57", "v58", "v59", "v60", "v61", "v62", "v63", "v64", "v65", "v66", "v67", "v68", "v69", "v70", "v71", "v72", "v73", "v74", "v75", "v76", "v77", "v78", "v79", "v80", "v81", "v82", "v83", "v84", "v85", "v86", "v87", "v88", "v89", "v90", "v91", "v92", "v93", "v94", "v95", "v96", "v97", "v98", "v99", "v100", "v101", "v102", "v103", "v104", "v105", "v106", "v107", "v108", "v109", "v110", "v111", "v112", "v113", "v114", "v115", "v116", "v117", "v118", "v119", "v120", "v121", "v122", "v123", "v124", "v125", "v126", "v127", "v128", "v129", "v130", "v131", "v132", "v133", "v134", "v135", "v136", "v137", "v138", "v139", "v140", "v141", "v142", "v143", "v144", "v145", "v146", "v147", "v148", "v149", "v150", "v151", "v152", "v153", "v154", "v155", "v156", "v157", "v158", "v159", "v160", "v161", "v162", "v163", "v164", "v165", "v166", "v167", "v168", "v169", "v170", "v171", "v172", "v173", "v174", "v175", "v176", "v177", "v178", "v179", "v180", "v181", "v182", "v183", "v184", "v185", "v186", "v187", "v188", "v189", "v190", "v191", "v204", "v205", "v206", "v207", "v208", "v209", "v210", "v211", "v212", "v213", "v214", "v215", "v216", "v217", "v218", "v219", "v220", "v221", "v222", "v223", "v224", "v225", "v226", "v227", "v228", "v229", "v230", "v231", "v232", "v233", "v234", "v235", "v236", "s40", "s41", "s42", "s43", "s44", "s45", "s46", "s47", "s49", "s52", "vcc", "scc", "memory");
    if (wi == 895 && lane_id() == 0) __builtin_amdgcn_raw_buffer_store_b32(tok, rws, 0, WS_TOK, 16);

  }
}

extern "C" void kernel_launch(void* const* d_in, const int* in_sizes, int n_in, void* d_out, int out_size, void* d_ws, size_t ws_size, hipStream_t stream) {
  const u16* const* in = (const u16* const*)d_in;
  mega<<<256, 256, 0, stream>>>(in[0], in[1], in[2], in[3], in[4], in[5], in[6], in[7], in[8], in[9], in[10], in[11], (char*)d_ws, (u16*)d_out);
}
```
